```python
import torch

def _softmax(x, axis=-1):
    return torch.softmax(x, dim=axis)
def _gelu(x):
    return torch.nn.functional.gelu(x, approximate="tanh")
def _silu(x):
    return torch.nn.functional.silu(x)
def _sigmoid(x):
    return torch.sigmoid(x)
def _layernorm(x, axis=-1, eps=1e-5):
    m = x.mean(dim=axis, keepdim=True)
    v = x.var(dim=axis, keepdim=True, unbiased=False)
    return (x - m) / torch.sqrt(v + eps)
def _scatter_add(tgt, idx, src, axis=0):
    return tgt.clone().index_add_(axis, idx, src)
def _segment_sum(src, seg, n):
    out = torch.zeros((n,) + src.shape[1:], dtype=src.dtype)
    return out.index_add_(0, seg, src)
def _pad(x, pw):
    flat = tuple(v for pair in reversed(pw) for v in pair)
    return torch.nn.functional.pad(x, flat)

kernel_name = 'concat_heads__mha_block_cached__mha_block_cached__near_ridge'

def setup_inputs():

    _w = (lambda a: (lambda t: t.to(torch.bfloat16) if t.is_floating_point() else t)(torch.as_tensor(a)))
    def _A_setup():
        B, Sq, D = 8, 128, 576
        H, Sk, Dh = 32, 512, 128
        return {
            "x": _w((lambda *s: torch.randn(*s, dtype=torch.bfloat16) * 0.8)(B, Sq, D)),
            "k_cache": _w((lambda *s: torch.randn(*s, dtype=torch.bfloat16) * 0.8)(B, H, Sk, Dh)),
            "v_cache": _w((lambda *s: torch.randn(*s, dtype=torch.bfloat16) * 0.05)(B, H, Sk, Dh)),
            "wq": _w((lambda *s: torch.randn(*s, dtype=torch.bfloat16) * 0.8)(D, H * Dh) * (D ** -0.5)),
            "wo": _w((lambda *s: torch.randn(*s, dtype=torch.bfloat16) * 0.05)(H * Dh, D) * ((H * Dh) ** -0.5)),
        }
    _A_in = _A_setup()

    def _B_setup():
        B, Sq, D = 8, 128, 576
        H, Sk, Dh = 32, 512, 128
        return {
            "x": _w((lambda *s: torch.randn(*s, dtype=torch.bfloat16) * 0.8)(B, Sq, D)),
            "k_cache": _w((lambda *s: torch.randn(*s, dtype=torch.bfloat16) * 0.8)(B, H, Sk, Dh)),
            "v_cache": _w((lambda *s: torch.randn(*s, dtype=torch.bfloat16) * 0.05)(B, H, Sk, Dh)),
            "wq": _w((lambda *s: torch.randn(*s, dtype=torch.bfloat16) * 0.8)(D, H * Dh) * (D ** -0.5)),
            "wo": _w((lambda *s: torch.randn(*s, dtype=torch.bfloat16) * 0.05)(H * Dh, D) * ((H * Dh) ** -0.5)),
        }
    _B_in = _B_setup()

    return {
        "A_x": _A_in["x"],
        "A_k_cache": _A_in["k_cache"],
        "A_v_cache": _A_in["v_cache"],
        "A_wq": _A_in["wq"],
        "A_wo": _A_in["wo"],
        "B_x": _B_in["x"],
        "B_k_cache": _B_in["k_cache"],
        "B_v_cache": _B_in["v_cache"],
        "B_wq": _B_in["wq"],
        "B_wo": _B_in["wo"],
    }

def reference(A_x, A_k_cache, A_v_cache, A_wq, A_wo, B_x, B_k_cache, B_v_cache, B_wq, B_wo):
    def _A_ref(x, k_cache, v_cache, wq, wo):
        B, Sq, D = x.shape
        H, Dh = 32, 128
        q = (lambda x, *d: x.permute(*d))((x @ wq).reshape(B, Sq, H, Dh), 0, 2, 1, 3)
        scores = torch.einsum("bhqd,bhkd->bhqk", q, k_cache) * (Dh ** -0.5)
        p = _softmax(scores, axis=-1)
        o = torch.einsum("bhqk,bhkd->bhqd", p, v_cache)
        return (lambda x, *d: x.permute(*d))(o, 0, 2, 1, 3).reshape(B, Sq, H * Dh) @ wo

    def _B_ref(x, k_cache, v_cache, wq, wo):
        B, Sq, D = x.shape
        H, Dh = 32, 128
        q = (lambda x, *d: x.permute(*d))((x @ wq).reshape(B, Sq, H, Dh), 0, 2, 1, 3)
        scores = torch.einsum("bhqd,bhkd->bhqk", q, k_cache) * (Dh ** -0.5)
        p = _softmax(scores, axis=-1)
        o = torch.einsum("bhqk,bhkd->bhqd", p, v_cache)
        return (lambda x, *d: x.permute(*d))(o, 0, 2, 1, 3).reshape(B, Sq, H * Dh) @ wo

    a = _A_ref(A_x, A_k_cache, A_v_cache, A_wq, A_wo)
    b = _B_ref(B_x, B_k_cache, B_v_cache, B_wq, B_wo)
    return torch.concatenate([a, b], axis=-1)
```

```cpp
#include <hip/hip_runtime.h>
#include <hip/hip_bf16.h>
typedef unsigned short u16;
__device__ __forceinline__ float bf2f(u16 x){ return __uint_as_float(((unsigned)x)<<16); }
__device__ __forceinline__ u16 f2bf(float f){ unsigned u=__float_as_uint(f); u += 0x7fffu + ((u>>16)&1u); return (u16)(u>>16); }
__global__ void gemm_naive(const u16* A,const u16* B,u16* C,int M,int N,int K,int ldc,int coff){
  int n=blockIdx.x*64+threadIdx.x%64; int m=blockIdx.y*4+threadIdx.x/64;
  if(n>=N||m>=M) return;
  float acc=0; for(int k=0;k<K;++k) acc+=bf2f(A[(size_t)m*K+k])*bf2f(B[(size_t)k*N+n]);
  C[(size_t)m*ldc+coff+n]=f2bf(acc);
}
__global__ void attn_naive(const u16* Q,const u16* Kc,const u16* Vc,u16* O){
  int q=blockIdx.x%128, h=(blockIdx.x/128)%32, b=blockIdx.x/(128*32);
  __shared__ float sq[128]; __shared__ float p[512]; __shared__ float red[128];
  int t=threadIdx.x;
  sq[t]=bf2f(Q[((size_t)(b*128+q))*4096+h*128+t]);
  __syncthreads();
  const u16* Kh=Kc+((size_t)(b*32+h))*512*128; const u16* Vh=Vc+((size_t)(b*32+h))*512*128;
  float mx=-1e30f;
  for(int k=t;k<512;k+=128){ float a=0; for(int d=0;d<128;++d) a+=sq[d]*bf2f(Kh[k*128+d]); a*=0.08838834764831845f; p[k]=a; mx=fmaxf(mx,a);}
  red[t]=mx; __syncthreads();
  for(int s=64;s>0;s>>=1){ if(t<s) red[t]=fmaxf(red[t],red[t+s]); __syncthreads(); }
  mx=red[0]; __syncthreads();
  float sum=0; for(int k=t;k<512;k+=128){ float e=__expf(p[k]-mx); p[k]=e; sum+=e; }
  red[t]=sum; __syncthreads();
  for(int s=64;s>0;s>>=1){ if(t<s) red[t]+=red[t+s]; __syncthreads(); }
  sum=red[0];
  float o=0; for(int k=0;k<512;++k) o+=p[k]*bf2f(Vh[k*128+t]);
  O[((size_t)(b*128+q))*4096+h*128+t]=f2bf(o/sum);
}
extern "C" void kernel_launch(void* const* d_in, const int* in_sizes, int n_in,
                              void* d_out, int out_size, void* d_ws, size_t ws_size,
                              hipStream_t stream) {
  u16* Q=(u16*)d_ws; u16* O=Q+(size_t)1024*4096;
  for(int br=0;br<2;++br){
    const u16* x=(const u16*)d_in[br*5+0]; const u16* k=(const u16*)d_in[br*5+1]; const u16* v=(const u16*)d_in[br*5+2];
    const u16* wq=(const u16*)d_in[br*5+3]; const u16* wo=(const u16*)d_in[br*5+4];
    gemm_naive<<<dim3(4096/64,1024/4),256,0,stream>>>(x,wq,Q,1024,4096,576,4096,0);
    attn_naive<<<8*32*128,128,0,stream>>>(Q,k,v,O);
    gemm_naive<<<dim3(576/64,1024/4),256,0,stream>>>(O,wo,(u16*)d_out,1024,576,4096,1152,br*576);
  }
}
```

```cpp
#include <hip/hip_runtime.h>
#include <hip/hip_bf16.h>
#include <stdint.h>
#include <type_traits>

typedef __attribute__((ext_vector_type(8))) __bf16 bf16x8;
typedef __attribute__((ext_vector_type(4))) __bf16 bf16x4;
typedef __attribute__((ext_vector_type(2))) __bf16 bf16x2;
typedef __attribute__((ext_vector_type(2))) float f32x2;
typedef __attribute__((ext_vector_type(4))) float f32x4;
typedef __attribute__((ext_vector_type(16))) float f32x16;
typedef __attribute__((ext_vector_type(4))) unsigned u32x4;
typedef __attribute__((address_space(3))) bf16x4* lds_b4p;
typedef __attribute__((address_space(3))) bf16x8* lds_b8p;
typedef __attribute__((address_space(3))) f32x4* lds_f4p;
typedef __amdgpu_buffer_rsrc_t srd_t;
#define DI __device__ __forceinline__
constexpr unsigned SRD3 = 0x00020000u;
#define KV_NT 1
#define W_NT 1

DI unsigned rfl(unsigned x) { return __builtin_amdgcn_readfirstlane(x); }
template <int NT> DI void dma(unsigned m0v, unsigned voff, srd_t srd, unsigned soff) {
  if constexpr (NT) asm volatile("s_mov_b32 m0, %0\n\ts_nop 0\n\tbuffer_load_dwordx4 %1, %2, %3 offen nt lds" ::"s"(m0v), "v"(voff), "s"(srd), "s"(soff) : "memory");
  else asm volatile("s_mov_b32 m0, %0\n\ts_nop 0\n\tbuffer_load_dwordx4 %1, %2, %3 offen lds" ::"s"(m0v), "v"(voff), "s"(srd), "s"(soff) : "memory");
}
template <int N> DI void waitvm() { asm volatile("s_waitcnt vmcnt(%0)" ::"i"(N) : "memory"); }
DI void bar() { asm volatile("s_waitcnt lgkmcnt(0)\n\ts_barrier" ::: "memory"); }
DI bf16x8 trA(unsigned a0, unsigned a1) {
  bf16x4 lo = __builtin_amdgcn_ds_read_tr16_b64_v4bf16((lds_b4p)a0);
  bf16x4 hi = __builtin_amdgcn_ds_read_tr16_b64_v4bf16((lds_b4p)a1);
  return __builtin_shufflevector(lo, hi, 0, 1, 2, 3, 4, 5, 6, 7);
}
DI bf16x8 ldsA(unsigned a) { return *(lds_b8p)a; }
DI unsigned cvtpk(float lo, float hi) { f32x2 v = {lo, hi}; bf16x2 b = __builtin_convertvector(v, bf16x2); return __builtin_bit_cast(unsigned, b); }
DI bf16x8 pack8(const f32x16& x, int s, float c) {
  u32x4 w = {cvtpk(x[8 * s] * c, x[8 * s + 1] * c), cvtpk(x[8 * s + 2] * c, x[8 * s + 3] * c), cvtpk(x[8 * s + 4] * c, x[8 * s + 5] * c), cvtpk(x[8 * s + 6] * c, x[8 * s + 7] * c)};
  return __builtin_bit_cast(bf16x8, w);
}
DI bf16x8 pack8n(const f32x16& x, int s) {
  u32x4 w = {cvtpk(x[8 * s], x[8 * s + 1]), cvtpk(x[8 * s + 2], x[8 * s + 3]), cvtpk(x[8 * s + 4], x[8 * s + 5]), cvtpk(x[8 * s + 6], x[8 * s + 7])};
  return __builtin_bit_cast(bf16x8, w);
}
#define MFMA32(a, b, c) __builtin_amdgcn_mfma_f32_32x32x16_bf16((a), (b), (c), 0, 0, 0)
DI f32x16 mfma_c(bf16x8 a, bf16x8 b, const f32x16& c) {
  f32x16 d; asm volatile("v_mfma_f32_32x32x16_bf16 %0, %1, %2, %3" : "=&v"(d) : "v"(a), "v"(b), "v"(c)); return d;
}
DI float swapmax(float x) { auto r = __builtin_amdgcn_permlane32_swap(__float_as_uint(x), __float_as_uint(x), false, false); return fmaxf(__uint_as_float(r[0]), __uint_as_float(r[1])); }
DI float swapsum(float x) { auto r = __builtin_amdgcn_permlane32_swap(__float_as_uint(x), __float_as_uint(x), false, false); return __uint_as_float(r[0]) + __uint_as_float(r[1]); }

constexpr float QSCALE = 0.12751743082f;
constexpr float THR = 8.0f;
constexpr unsigned XS_OFF = 65536, WS_OFF = 98304;
DI constexpr unsigned slotaddr(int u) { return (u % 5 == 0) ? 0u : (u % 5 == 1) ? 32768u : (u % 5 == 2) ? 131072u : (u % 5 == 3) ? 65536u : 98304u; }

struct K1Args { const char* x[2]; const char* k[2]; const char* v[2]; const char* wq[2]; char* ows; };

DI float max3f(float a, float b, float c) { float r; asm("v_max3_f32 %0, %1, %2, %3" : "=v"(r) : "v"(a), "v"(b), "v"(c)); return r; }
#define SB() do { asm volatile("" ::: "memory"); __builtin_amdgcn_sched_barrier(0); } while (0)
#define STAMP(i)
#define STAMPR(i)
#define STAMP_DECL
#define STAMP_STORE()
DI unsigned slot_of(unsigned i) { return ((0x32410u >> (4u * i)) & 7u) << 15; }

struct Frag4 { bf16x8 f[4]; };

template <int NT> DI void vld(u32x4& d, unsigned voff, srd_t srd, unsigned soff) {
  if constexpr (NT) asm volatile("buffer_load_dwordx4 %0, %1, %2, %3 offen nt" : "=v"(d) : "v"(voff), "s"(srd), "s"(soff) : "memory");
  else asm volatile("buffer_load_dwordx4 %0, %1, %2, %3 offen" : "=v"(d) : "v"(voff), "s"(srd), "s"(soff) : "memory");
}
typedef __attribute__((address_space(3))) u32x4* lds_u4p;
template <int... Is> struct iseq_t {};
template <int N, int... Is> struct mkseq : mkseq<N - 1, N - 1, Is...> {};
template <int... Is> struct mkseq<0, Is...> { typedef iseq_t<Is...> type; };
template <class F, int... Is> DI void static_for_impl(F&& f, iseq_t<Is...>) { (f(std::integral_constant<int, Is>{}), ...); }
template <int N, class F> DI void static_for(F&& f) { static_for_impl(f, typename mkseq<N>::type{}); }

__global__ __launch_bounds__(512, 2) void attn_k1(const char* xA, const char* kA, const char* vA, const char* wqA,
                                                  const char* xB, const char* kB, const char* vB, const char* wqB, char* ows) {
  extern __shared__ __attribute__((aligned(16))) char lds[];
  const unsigned tid = threadIdx.x, lane = tid & 63u, wid = rfl(tid >> 6);
  const unsigned ldsb = (unsigned)(size_t)lds;
  const unsigned blk = blockIdx.x, xcd = blk & 7u, idx = blk >> 3;
  const unsigned br = xcd >> 2, hp = (xcd & 3u) * 4u + (idx & 3u), b = idx >> 2, h0 = hp * 2u;
  const unsigned ha = wid >> 2, qt = wid & 3u;
  const char* xp = br ? xB : xA; const char* kp = br ? kB : kA; const char* vp = br ? vB : vA; const char* wp = br ? wqB : wqA;
  const srd_t sX = __builtin_amdgcn_make_buffer_rsrc((void*)(xp + (size_t)b * 147456u), (short)0, 147456, SRD3);
  const srd_t sW = __builtin_amdgcn_make_buffer_rsrc((void*)(wp + (size_t)h0 * 256u), (short)0, 576 * 8192 - h0 * 256u, SRD3);
  const srd_t sK = __builtin_amdgcn_make_buffer_rsrc((void*)(kp + (size_t)(b * 32u + h0) * 131072u), (short)0, 262144, SRD3);
  const srd_t sV = __builtin_amdgcn_make_buffer_rsrc((void*)(vp + (size_t)(b * 32u + h0) * 131072u), (short)0, 262144, SRD3);
  const unsigned vx0 = (lane >> 3) * 1152u + 16u * ((lane & 7u) ^ (lane >> 4));
  const unsigned vx1 = (8u + (lane >> 3)) * 1152u + 16u * ((lane & 7u) ^ (4u + (lane >> 4)));
  const unsigned vw = ((lane >> 2) & 7u) * 8192u + (lane >> 5) * 64u + (lane & 3u) * 16u;
  const unsigned vk0 = (lane >> 4) * 256u + 16u * ((lane & 15u) ^ (lane >> 4));
  const unsigned vk1 = vk0 ^ 64u, vk2 = vk0 ^ 128u, vk3 = vk0 ^ 192u;
  const unsigned vv = ((lane >> 2) & 7u) * 256u + (lane >> 5) * 64u + (lane & 3u) * 16u;
  const unsigned kvw = ha * 131072u + qt * 4096u;
  const unsigned kvd = ldsb + ha * 16384u + qt * 4096u;
  auto issue_x = [&](unsigned s) {
    const unsigned dx = ldsb + XS_OFF + (s & 1u) * 16384u + wid * 2048u, sx = wid * 16u * 1152u + s * 128u;
    dma<W_NT>(dx, vx0, sX, sx);
    dma<W_NT>(dx + 1024u, vx1, sX, sx);
  };
  auto issue_w = [&](unsigned hs) {
    const unsigned dw = ldsb + WS_OFF + (hs & 3u) * 16384u + ha * 8192u + qt * 2048u, sw = (hs * 32u + qt * 8u) * 8192u + ha * 256u;
    dma<W_NT>(dw, vw, sW, sw);
    dma<W_NT>(dw + 1024u, vw, sW, sw + 128u);
  };
  auto k_piece = [&](unsigned j, unsigned slot, int i) {
    dma<KV_NT>(kvd + slot + i * 1024u, i == 0 ? vk0 : i == 1 ? vk1 : i == 2 ? vk2 : vk3, sK, kvw + j * 16384u + i * 1024u);
  };
  auto v_piece = [&](unsigned j, unsigned slot, int i) {
    dma<KV_NT>(kvd + slot + i * 1024u, vv, sV, kvw + j * 16384u + (i >> 1) * 2048u + (i & 1) * 128u);
  };
  auto issue_k = [&](unsigned j, unsigned slot) { k_piece(j, slot, 0); k_piece(j, slot, 1); k_piece(j, slot, 2); k_piece(j, slot, 3); };
  auto issue_v = [&](unsigned j, unsigned slot) { v_piece(j, slot, 0); v_piece(j, slot, 1); v_piece(j, slot, 2); v_piece(j, slot, 3); };
  STAMP_DECL
  STAMPR(60); STAMP(0);
  issue_w(0); issue_x(0); issue_w(1);
  const unsigned l31 = lane & 31u, hh = lane >> 5, qq = (lane & 15u) >> 2, pp = lane & 3u, blkk = (lane >> 4) & 1u;
  const unsigned swp = ((pp & 1u) << 1) | (pp >> 1);
  const unsigned aX = ldsb + XS_OFF + (qt * 32u + l31) * 128u + 16u * (hh ^ ((l31 >> 1) & 7u));
  const unsigned aW = ldsb + WS_OFF + ha * 8192u + 2048u * hh + 64u * qq + 32u * blkk + 8u * swp;
  const unsigned aK = ldsb + ha * 16384u + 256u * l31 + 16u * (hh ^ (l31 & 15u));
  const unsigned aV = ldsb + ha * 16384u + 256u * hh + 64u * qq + 32u * blkk + 8u * swp;

  f32x16 acc[4] = {};
  Frag4 fa, fb; bf16x8 xf[4];
  u32x4 r2[4], r3[4];
  auto ldW1 = [&](int hs, int s2, int dt) -> bf16x8 { const unsigned a = aW + (hs & 3) * 16384u + 4096u * s2 + 512u * dt; return trA(a, a + 256u); };
  auto ldX1 = [&](int s, int s4) -> bf16x8 { return ldsA((aX ^ (32u * s4)) + (s & 1) * 16384u); };
  auto w_piece = [&](unsigned hs, int i) {
    dma<W_NT>(ldsb + WS_OFF + (hs & 3u) * 16384u + ha * 8192u + qt * 2048u + i * 1024u, vw, sW, (hs * 32u + qt * 8u) * 8192u + ha * 256u + i * 128u);
  };
  auto x_piece = [&](unsigned s, int i) {
    dma<W_NT>(ldsb + XS_OFF + (s & 1u) * 16384u + wid * 2048u + i * 1024u, i ? vx1 : vx0, sX, wid * 16u * 1152u + s * 128u);
  };
  waitvm<2>();
  bar();
  STAMP(1);
  w_piece(2, 0); w_piece(2, 1); x_piece(1, 0); x_piece(1, 1);
#pragma unroll
  for (int i = 0; i < 4; ++i) { xf[i] = ldX1(0, i); fa.f[i] = ldW1(0, 0, i); }
  w_piece(3, 0); w_piece(3, 1); k_piece(0, 0u, 0); k_piece(0, 0u, 1);
  SB();
#pragma unroll
  for (int i = 0; i < 4; ++i) { fb.f[i] = ldW1(0, 1, i); SB(); acc[i] = MFMA32(fa.f[i], xf[0], acc[i]); SB(); }
  auto grp_op = [&](auto HS, auto N) {
    constexpr int hs = decltype(HS)::value, n = decltype(N)::value;
    if constexpr (n < 2) { if constexpr (hs <= 13) w_piece(hs + 4, n); else if constexpr (hs == 17) v_piece(1, 98304u, n); }
    else if constexpr (hs == 17) v_piece(1, 98304u, n);
    else if constexpr ((hs & 1) == 0) { if constexpr (hs <= 12) x_piece(hs / 2 + 2, n - 2); }
    else {
      constexpr int i = n - 2;
      if constexpr (hs == 1) k_piece(0, 0u, 2 + i);
      if constexpr (hs == 3) k_piece(1, 32768u, i);
      if constexpr (hs == 5) k_piece(1, 32768u, 2 + i);
      if constexpr (hs == 7) vld<KV_NT>(r2[i], vv, sV, kvw + i * 128u);
      if constexpr (hs == 9) vld<KV_NT>(r2[2 + i], vv, sV, kvw + 2048u + i * 128u);
      if constexpr (hs == 11) vld<KV_NT>(r3[i], i ? vk1 : vk0, sK, kvw + 32768u + i * 1024u);
      if constexpr (hs == 13) vld<KV_NT>(r3[2 + i], i ? vk3 : vk2, sK, kvw + 32768u + (2 + i) * 1024u);
    }
  };
  static_for<18>([&](auto HS) {
    constexpr int hs = decltype(HS)::value;
    if constexpr (hs == 0) waitvm<8>();
    else if constexpr (hs == 1) waitvm<8>();
    else if constexpr (hs <= 13) waitvm<(hs & 1) ? 8 : 10>();
    else if constexpr (hs == 14) waitvm<10>();
    else if constexpr (hs == 15) waitvm<4>();
    else if constexpr (hs == 16) waitvm<0>();
    bar();
    STAMP(2 + hs);
    const bf16x8 x1 = xf[2 * (hs & 1) + 1];
    bf16x8 xn[4];
    Frag4 fn;
    static_for<4>([&](auto I) {
      constexpr int i = decltype(I)::value;
      acc[i] = MFMA32(fb.f[i], x1, acc[i]); SB();
      if constexpr (hs < 17) { fa.f[i] = ldW1(hs + 1, 0, i); if constexpr (hs & 1) xn[i] = ldX1((hs + 1) / 2, i); }
      else fa.f[i] = ldsA(aK ^ (32u * i));
      if constexpr (hs == 15) { asm volatile("" : "+v"(r2[i])); *(lds_u4p)(kvd + 131072u + i * 1024u + lane * 16u) = r2[i]; }
      if constexpr (hs == 16) { asm volatile("" : "+v"(r3[i])); *(lds_u4p)(kvd + 65536u + i * 1024u + lane * 16u) = r3[i]; }
      if constexpr (hs == 17) grp_op(HS, I);
      SB();
    });
    if constexpr (hs & 1) { if constexpr (hs < 17) { xf[0] = xn[0]; xf[1] = xn[1]; xf[2] = xn[2]; xf[3] = xn[3]; } }
    if constexpr (hs < 17) {
      const bf16x8 x0 = xf[2 * ((hs + 1) & 1)];
      static_for<4>([&](auto I) {
        constexpr int i = decltype(I)::value;
        acc[i] = MFMA32(fa.f[i], x0, acc[i]); SB();
        fn.f[i] = ldW1(hs + 1, 1, i);
        grp_op(HS, I);
        SB();
      });
      fb = fn;
    }
  });
  bf16x8 qf[8];
#pragma unroll
  for (int dt = 0; dt < 4; ++dt) { qf[2 * dt] = pack8(acc[dt], 0, QSCALE); qf[2 * dt + 1] = pack8(acc[dt], 1, QSCALE); }

  f32x16 o[4] = {};
  f32x16 negm = {};
  f32x16 sA, sB, sC;
  bf16x8 p0, p1, p2, p3;
  bf16x8 f0 = fa.f[0], f1 = fa.f[1], f2 = fa.f[2], f3 = fa.f[3];
  f32x4 lacc = {};
  float m = 0.f;
  bf16x8 sel;
  {
    const unsigned one2 = (((lane & 3u) == 0u) && (((lane >> 2) & 1u) == ((lane >> 4) & 1u))) ? 0x3F803F80u : 0u;
    u32x4 w = {one2, one2, one2, one2}; sel = __builtin_bit_cast(bf16x8, w);
  }
  auto kfrag = [&](unsigned base, int i) -> bf16x8 { return ldsA((base ^ (32u * (i & 7))) + 8192u * (i >> 3)); };
  auto vfrag = [&](unsigned base, int i) -> bf16x8 { const unsigned a = base + 4096u * (i >> 2) + 512u * (i & 3); return trA(a, a + 2048u); };
  auto FR = [&](auto I) -> bf16x8& { constexpr int k = decltype(I)::value & 3; if constexpr (k == 0) return f0; else if constexpr (k == 1) return f1; else if constexpr (k == 2) return f2; else return f3; };
  auto ex = [&](float x) -> float { float e = __builtin_amdgcn_exp2f(x); asm volatile("" : "+v"(e)); return e; };
  auto pk = [&](float a, float b2) -> unsigned { unsigned r = cvtpk(a, b2); asm volatile("" : "+v"(r)); return r; };
  auto lsum = [&](const bf16x8& p) { lacc = __builtin_amdgcn_mfma_f32_16x16x32_bf16(sel, p, lacc, 0, 0, 0); };
  {
    const unsigned kb = aK;
    static_for<16>([&](auto I) {
      constexpr int i = decltype(I)::value;
      if constexpr (i == 0) sA = mfma_c(FR(I), qf[0], negm);
      else if constexpr (i < 8) sA = MFMA32(FR(I), qf[i & 7], sA);
      else if constexpr (i == 8) sB = mfma_c(FR(I), qf[0], negm);
      else sB = MFMA32(FR(I), qf[i & 7], sB);
      SB();
      if constexpr (i + 4 < 16) FR(I) = kfrag(kb, i + 4);
      if constexpr (i == 12) { waitvm<12>(); bar(); STAMP(20); }
      if constexpr (i == 13) { f0 = kfrag(aK + 32768u, 0); f1 = kfrag(aK + 32768u, 1); }
      if constexpr (i == 14) f2 = kfrag(aK + 32768u, 2);
      if constexpr (i == 15) f3 = kfrag(aK + 32768u, 3);
      SB();
    });
    k_piece(3, 0u, 0); k_piece(3, 0u, 1); k_piece(3, 0u, 2); k_piece(3, 0u, 3);
    asm volatile("s_nop 7\n\ts_nop 3" : "+v"(sA), "+v"(sB));
    float a = max3f(sA[0], sB[0], sA[1]), b2 = max3f(sB[1], sA[2], sB[2]);
#pragma unroll
    for (int r = 3; r < 15; r += 2) { a = max3f(a, sA[r], sB[r]); b2 = max3f(b2, sA[r + 1], sB[r + 1]); }
    a = max3f(a, sA[15], sB[15]);
    const float tm = swapmax(fmaxf(a, b2));
    m = tm;
#pragma unroll
    for (int r = 0; r < 16; ++r) { sA[r] -= tm; sB[r] -= tm; negm[r] = -tm; }
    u32x4 w0, w1;
#pragma unroll
    for (int i = 0; i < 4; ++i) { w0[i] = pk(ex(sA[2 * i]), ex(sA[2 * i + 1])); w1[i] = pk(ex(sA[8 + 2 * i]), ex(sA[9 + 2 * i])); }
    p0 = __builtin_bit_cast(bf16x8, w0); p1 = __builtin_bit_cast(bf16x8, w1);
    SB();
  }
  unsigned iK = 1;
  auto step = [&](unsigned j, f32x16& sOld, f32x16& sNew, auto FIRST) {
    constexpr bool first = decltype(FIRST)::value;
    const unsigned iV = iK == 4u ? 0u : iK + 1u, iK2 = iV == 4u ? 0u : iV + 1u;
    const unsigned iVp = iK == 0u ? 4u : iK - 1u;
    const unsigned slK = slot_of(iK), slV = slot_of(iV), slK2 = slot_of(iK2), slVp = slot_of(iVp);
    const unsigned kb = aK + slK, vb = aV + slV, kb2 = aK + slK2;
    const unsigned srcK = j <= 5 ? kvw + (j + 2u) * 16384u : 0x40000000u;
    const unsigned srcV = j <= 6 ? kvw + (j + 1u) * 16384u : 0x40000000u;
    float mxa, mxb;
    {
      u32x4 w2, w3; float e0;
      static_for<16>([&](auto I) {
        constexpr int i = decltype(I)::value;
        if constexpr (i == 0) sA = mfma_c(FR(I), qf[0], negm);
        else if constexpr (i < 8) sA = MFMA32(FR(I), qf[i & 7], sA);
        else if constexpr (i == 8) sNew = mfma_c(FR(I), qf[0], negm);
        else sNew = MFMA32(FR(I), qf[i & 7], sNew);
        SB();
        if constexpr (i + 4 < 16) FR(I) = kfrag(kb, i + 4);
        if constexpr ((i & 1) == 0) e0 = ex(sOld[i]);
        else { const unsigned r = pk(e0, ex(sOld[i])); if constexpr (i < 8) w2[i >> 1] = r; else w3[(i - 8) >> 1] = r; }
        if constexpr (!first && (i == 0 || i == 3 || i == 6 || i == 9)) {
          constexpr int q = i / 3;
          dma<KV_NT>(kvd + slVp + q * 1024u, q == 0 ? vk0 : q == 1 ? vk1 : q == 2 ? vk2 : vk3, sK, srcK + q * 1024u);
        }
        if constexpr (i == 10) { mxa = max3f(sA[0], sA[1], sA[2]); mxb = max3f(sA[3], sA[4], sA[5]); }
        if constexpr (i == 11) { mxa = max3f(mxa, sA[6], sA[7]); mxb = max3f(mxb, sA[8], sA[9]); }
        if constexpr (i == 13) { mxa = max3f(mxa, sA[10], sA[11]); mxb = max3f(mxb, sA[12], sA[13]); }
        if constexpr (i == 14) { mxa = max3f(mxa, sA[14], sA[15]); asm volatile("" : "+v"(mxa), "+v"(mxb)); }
        if constexpr (i == 12) { waitvm<12>(); bar(); STAMP(19 + 2 * j); }
        if constexpr (i == 13) { f0 = vfrag(vb, 0); f1 = vfrag(vb, 1); }
        if constexpr (i == 14) f2 = vfrag(vb, 2);
        if constexpr (i == 15) f3 = vfrag(vb, 3);
        SB();
      });
      p2 = __builtin_bit_cast(bf16x8, w2); p3 = __builtin_bit_cast(bf16x8, w3);
    }
    {
      float al = 1.f; bool pend = false;
      u32x4 w0, w1; float e0;
      static_for<16>([&](auto I) {
        constexpr int i = decltype(I)::value;
        const bf16x8& pg = (i >> 2) == 0 ? p0 : (i >> 2) == 1 ? p1 : (i >> 2) == 2 ? p2 : p3;
        o[i & 3] = MFMA32(FR(I), pg, o[i & 3]);
        SB();
        if constexpr (i + 4 < 16) FR(I) = vfrag(vb, i + 4);
        if constexpr ((i & 3) == 1) lsum(pg);
        if constexpr (i == 0 || i == 5 || i == 8 || i == 11) {
          constexpr int q = i == 0 ? 0 : i == 5 ? 1 : i == 8 ? 2 : 3;
          dma<KV_NT>(kvd + slK + q * 1024u, vv, sV, srcV + (q >> 1) * 2048u + (q & 1) * 128u);
        }
        if constexpr (i == 2) {
          asm volatile("s_nop 1" : "+v"(sNew));
          mxa = max3f(mxa, sNew[0], sNew[1]); mxb = max3f(mxb, sNew[2], sNew[3]); mxa = max3f(mxa, sNew[4], sNew[5]); mxb = max3f(mxb, sNew[6], sNew[7]);
        }
        if constexpr (i == 3) {
          mxa = max3f(mxa, sNew[8], sNew[9]); mxb = max3f(mxb, sNew[10], sNew[11]); mxa = max3f(mxa, sNew[12], sNew[13]); mxb = max3f(mxb, sNew[14], sNew[15]);
          const float tm = swapmax(max3f(mxa, mxb, mxb));
          if (__any(tm > THR)) {
            const float dl = fmaxf(tm, 0.f);
            al = __builtin_amdgcn_exp2f(-dl); pend = true;
            m += dl;
#pragma unroll
            for (int r = 0; r < 16; ++r) { sA[r] -= dl; sNew[r] -= dl; negm[r] = -m; }
          }
        }
        if constexpr (i >= 4) {
          static_for<16>([&](auto E) {
            constexpr int e = decltype(E)::value;
            if constexpr (4 + (e * 12) / 16 == i) {
              if constexpr ((e & 1) == 0) e0 = ex(sA[e]);
              else { const unsigned r = pk(e0, ex(sA[e])); if constexpr (e < 8) w0[e >> 1] = r; else w1[(e - 8) >> 1] = r; }
            }
          });
        }
        if constexpr (i == 12) { waitvm<12>(); bar(); STAMP(20 + 2 * j); }
        if constexpr (i == 13) { f0 = kfrag(kb2, 0); f1 = kfrag(kb2, 1); }
        if constexpr (i == 14) f2 = kfrag(kb2, 2);
        if constexpr (i == 15) f3 = kfrag(kb2, 3);
        SB();
      });
      p0 = __builtin_bit_cast(bf16x8, w0); p1 = __builtin_bit_cast(bf16x8, w1);
      if (pend) {
        asm volatile("s_nop 7\n\ts_nop 3" : "+v"(lacc));
        lacc[0] *= al;
#pragma unroll
        for (int dt = 0; dt < 4; ++dt)
#pragma unroll
          for (int r = 0; r < 16; ++r) o[dt][r] *= al;
      }
      SB();
    }
    iK = iK2;
  };
  step(1u, sB, sC, std::true_type{});
#pragma unroll 1
  for (unsigned j = 2; j < 8; j += 2) {
    step(j, sC, sB, std::false_type{});
    step(j + 1u, sB, sC, std::false_type{});
  }
  {
    u32x4 w2, w3;
#pragma unroll
    for (int i = 0; i < 4; ++i) { w2[i] = pk(ex(sC[2 * i]), ex(sC[2 * i + 1])); w3[i] = pk(ex(sC[8 + 2 * i]), ex(sC[9 + 2 * i])); }
    p2 = __builtin_bit_cast(bf16x8, w2); p3 = __builtin_bit_cast(bf16x8, w3);
    waitvm<8>(); bar(); STAMP(35);
    const unsigned vb = aV + 32768u;
    f0 = vfrag(vb, 0); f1 = vfrag(vb, 1); f2 = vfrag(vb, 2); f3 = vfrag(vb, 3);
    static_for<16>([&](auto I) {
      constexpr int i = decltype(I)::value;
      const bf16x8& pg = (i >> 2) == 0 ? p0 : (i >> 2) == 1 ? p1 : (i >> 2) == 2 ? p2 : p3;
      o[i & 3] = MFMA32(FR(I), pg, o[i & 3]);
      SB();
      if constexpr (i + 4 < 16) FR(I) = vfrag(vb, i + 4);
      if constexpr ((i & 3) == 1) lsum(pg);
      SB();
    });
  }
  const float inv = 1.0f / lacc[0];
  char* op = ows + ((size_t)(((br * 8u + b) * 32u + h0 + ha) * 4u + qt) * 8u) * 1024u + lane * 16u;
#pragma unroll
  for (int dt = 0; dt < 4; ++dt) {
    *(bf16x8*)(op + (2 * dt) * 1024) = pack8(o[dt], 0, inv);
    *(bf16x8*)(op + (2 * dt + 1) * 1024) = pack8(o[dt], 1, inv);
  }
  waitvm<0>();
  STAMP(36); STAMPR(61); STAMP_STORE();
}

constexpr unsigned P2_STAGE = 9216u;
constexpr int P2_NB = 3;
constexpr unsigned P2_LDS = 4u * P2_NB * P2_STAGE;
__global__ __launch_bounds__(256, 1) void outproj_k2(const char* ows, const char* woA, const char* woB, char* out) {
  extern __shared__ __attribute__((aligned(16))) char lds[];
  const unsigned tid = threadIdx.x, lane = tid & 63u, wid = rfl(tid >> 6);
  const unsigned ldsb = (unsigned)(size_t)lds;
  const unsigned blk = blockIdx.x, xcd = blk & 7u, idx = blk >> 3;
  const unsigned rt = xcd * 4u + (idx & 3u), ct = idx >> 2;
  const unsigned br = rt >> 4, b = (rt >> 1) & 7u, qh = rt & 1u;
  const char* wp = br ? woB : woA;
  const srd_t sW = __builtin_amdgcn_make_buffer_rsrc((void*)(wp + (size_t)ct * 144u), (short)0, 4096 * 1152 - ct * 144u, SRD3);
  const char* obase = ows + (size_t)((br * 8u + b) * 32u) * 32768u + qh * 16384u;
  const srd_t sO = __builtin_amdgcn_make_buffer_rsrc((void*)obase, (short)0, 32 * 32768 - qh * 16384u, SRD3);
  unsigned vw[9];
#pragma unroll
  for (int j = 0; j < 9; ++j) { const unsigned i = 64u * j + lane; vw[j] = (i / 9u) * 1152u + (i % 9u) * 16u; }
  const unsigned wb = ldsb + wid * (P2_NB * P2_STAGE);
  u32x4 of[P2_NB][8];
  auto issue = [&](int st, u32x4* ofr) {
    const unsigned krow = wid * 1024u + st * 64u;
#pragma unroll
    for (int j = 0; j < 9; ++j) dma<0>(wb + (st % P2_NB) * P2_STAGE + j * 1024u, vw[j], sW, krow * 1152u);
    const unsigned h = wid * 8u + (st >> 1);
#pragma unroll
    for (int i = 0; i < 8; ++i)
      asm volatile("buffer_load_dwordx4 %0, %1, %2, %3 offen" : "=v"(ofr[i]) : "v"(lane * 16u), "s"(sO), "s"(h * 32768u + (i >> 2) * 8192u + ((st & 1) * 4u + (i & 3)) * 1024u) : "memory");
  };
  const unsigned l31 = lane & 31u, hh = lane >> 5, qq = (lane & 15u) >> 2, pp = lane & 3u, blkk = (lane >> 4) & 1u;
  const unsigned aW01 = wb + (8u * hh + qq) * 144u + 32u * blkk + 8u * pp;
  const unsigned aW2 = wb + (8u * hh + qq) * 144u + 128u + 8u * (pp & 1u);
  f32x16 acc[3][2] = {};
  issue(0, of[0]); issue(1, of[1]);
#pragma unroll
  for (int st = 0; st < 16; ++st) {
    if (st + 2 < 16) { issue(st + 2, of[(st + 2) % P2_NB]); waitvm<34>(); } else if (st + 1 < 16) waitvm<17>(); else waitvm<0>();
    u32x4* oc = of[st % P2_NB];
#pragma unroll
    for (int i = 0; i < 8; ++i) asm volatile("" : "+v"(oc[i]));
    const unsigned bo = (st % P2_NB) * P2_STAGE;
#pragma unroll
    for (int s4 = 0; s4 < 4; ++s4) {
      const unsigned ro = bo + s4 * 16u * 144u;
      const bf16x8 a0 = trA(aW01 + ro, aW01 + ro + 4u * 144u);
      const bf16x8 a1 = trA(aW01 + ro + 64u, aW01 + ro + 64u + 4u * 144u);
      const bf16x8 a2 = trA(aW2 + ro, aW2 + ro + 4u * 144u);
      const bf16x8 b0 = __builtin_bit_cast(bf16x8, oc[s4]), b1 = __builtin_bit_cast(bf16x8, oc[4 + s4]);
      acc[0][0] = MFMA32(a0, b0, acc[0][0]); acc[0][1] = MFMA32(a0, b1, acc[0][1]);
      acc[1][0] = MFMA32(a1, b0, acc[1][0]); acc[1][1] = MFMA32(a1, b1, acc[1][1]);
      acc[2][0] = MFMA32(a2, b0, acc[2][0]); acc[2][1] = MFMA32(a2, b1, acc[2][1]);
    }
    asm volatile("s_waitcnt lgkmcnt(0)" ::: "memory");
  }
  bar();
  {
    const unsigned rb = ldsb + wid * (64u * 288u);
#pragma unroll
    for (int q2 = 0; q2 < 2; ++q2) {
      const unsigned row = rb + (q2 * 32u + l31) * 288u + hh * 16u;
#pragma unroll
      for (int nt = 0; nt < 2; ++nt)
#pragma unroll
        for (int g = 0; g < 4; ++g) {
          f32x4 v = {acc[nt][q2][4 * g], acc[nt][q2][4 * g + 1], acc[nt][q2][4 * g + 2], acc[nt][q2][4 * g + 3]};
          *(lds_f4p)(row + nt * 128u + g * 32u) = v;
        }
      f32x4 v = {acc[2][q2][0], acc[2][q2][1], acc[2][q2][2], acc[2][q2][3]};
      *(lds_f4p)(row + 256u) = v;
    }
  }
  bar();
  for (unsigned c = tid; c < 576u; c += 256u) {
    const unsigned q = c / 9u, ch = c % 9u;
    const unsigned a = ldsb + q * 288u + ch * 32u;
    f32x4 lo = *(lds_f4p)a, hi = *(lds_f4p)(a + 16u);
#pragma unroll
    for (int w = 1; w < 4; ++w) { lo += *(lds_f4p)(a + w * 64u * 288u); hi += *(lds_f4p)(a + w * 64u * 288u + 16u); }
    u32x4 r = {cvtpk(lo[0], lo[1]), cvtpk(lo[2], lo[3]), cvtpk(hi[0], hi[1]), cvtpk(hi[2], hi[3])};
    *(u32x4*)(out + (size_t)(b * 128u + qh * 64u + q) * 2304u + br * 1152u + ct * 144u + ch * 16u) = r;
  }
}

extern "C" void kernel_launch(void* const* d_in, const int* in_sizes, int n_in, void* d_out, int out_size, void* d_ws, size_t ws_size, hipStream_t stream) {
  (void)in_sizes; (void)n_in; (void)out_size; (void)ws_size;
  hipFuncSetAttribute((const void*)attn_k1, hipFuncAttributeMaxDynamicSharedMemorySize, 163840);
  hipFuncSetAttribute((const void*)outproj_k2, hipFuncAttributeMaxDynamicSharedMemorySize, P2_LDS);
  char* ows = (char*)d_ws;
  hipLaunchKernelGGL(attn_k1, dim3(256), dim3(512), 163840, stream, (const char*)d_in[0], (const char*)d_in[1], (const char*)d_in[2], (const char*)d_in[3],
                     (const char*)d_in[5], (const char*)d_in[6], (const char*)d_in[7], (const char*)d_in[8], ows);
  hipLaunchKernelGGL(outproj_k2, dim3(256), dim3(256), P2_LDS, stream, (const char*)ows, (const char*)d_in[4], (const char*)d_in[9], (char*)d_out);
}
```

```cpp
#include <hip/hip_runtime.h>
#include <hip/hip_bf16.h>
#include <stdint.h>

__global__ __launch_bounds__(512, 2) void fused_mha_k(const char* xA, const char* kA, const char* vA, const char* wqA,
                                                      const char* xB, const char* kB, const char* vB, const char* wqB,
                                                      char* ows, const char* woA, const char* woB, char* out) {
  unsigned t = threadIdx.x;
  asm volatile(
"s_load_dwordx16 s[4:19], s[0:1], 0x0\n"
"s_load_dwordx8 s[92:99], s[0:1], 0x40\n"
"v_lshrrev_b32 v231, 6, v0\n"
"v_and_b32 v230, 63, v0\n"
"v_readfirstlane_b32 s44, v231\n"
"s_and_b32 s54, s2, 7\n"
"s_lshr_b32 s55, s2, 3\n"
"s_lshr_b32 s56, s54, 2\n"
"s_and_b32 s57, s54, 3\n"
"s_lshl_b32 s57, s57, 2\n"
"s_and_b32 s58, s55, 3\n"
"s_add_u32 s57, s57, s58\n"
"s_lshl_b32 s57, s57, 1\n"
"s_lshr_b32 s58, s55, 2\n"
"v_bfe_u32 v231, v230, 2, 2\n"
"v_lshrrev_b32 v232, 4, v230\n"
"v_and_b32 v233, 3, v230\n"
"v_lshlrev_b32 v223, 13, v231\n"
"v_lshl_add_u32 v223, v232, 6, v223\n"
"v_lshl_add_u32 v223, v233, 4, v223\n"
"v_add_u32 v224, 0x7c00, v223\n"
"v_lshlrev_b32 v213, 8, v231\n"
"v_lshl_add_u32 v213, v232, 6, v213\n"
"v_lshl_add_u32 v213, v233, 4, v213\n"
"v_lshrrev_b32 v231, 3, v230\n"
"v_mul_u32_u24 v231, 0x480, v231\n"
"v_and_b32 v233, 7, v230\n"
"v_xor_b32 v233, v233, v232\n"
"v_lshl_add_u32 v221, v233, 4, v231\n"
"v_xor_b32 v233, 4, v233\n"
"v_lshl_add_u32 v222, v233, 4, v231\n"
"v_add_u32 v222, 0x2000, v222\n"
"s_lshr_b32 s45, s44, 2\n"
"s_and_b32 s46, s44, 3\n"
"s_mul_i32 s47, s45, 81920\n"
"s_lshl_b32 s48, s45, 17\n"
"s_lshl_b32 s59, s46, 12\n"
"s_add_u32 s48, s48, s59\n"
"s_add_u32 s49, s47, s59\n"
"s_lshl_b32 s50, s44, 11\n"
"s_mul_i32 s51, s44, 18432\n"
"s_lshl_b32 s52, s45, 13\n"
"s_lshl_b32 s59, s46, 11\n"
"s_add_u32 s52, s52, s59\n"
"s_lshl_b32 s53, s46, 16\n"
"s_lshl_b32 s59, s45, 8\n"
"s_add_u32 s53, s53, s59\n"
"s_waitcnt lgkmcnt(0)\n"
"s_mov_b64 s[20:21], s[92:93]\n"
"s_cmp_lg_u32 s56, 0\n"
"s_cselect_b64 s[4:5], s[12:13], s[4:5]\n"
"s_cselect_b64 s[6:7], s[14:15], s[6:7]\n"
"s_cselect_b64 s[8:9], s[16:17], s[8:9]\n"
"s_cselect_b64 s[10:11], s[18:19], s[10:11]\n"
"s_mul_i32 s12, s58, 147456\n"
"s_add_u32 s24, s4, s12\n"
"s_addc_u32 s25, s5, 0\n"
"s_and_b32 s25, s25, 0xffff\n"
"s_mov_b32 s26, 147456\n"
"s_mov_b32 s27, 0x20000\n"
"s_lshl_b32 s12, s57, 8\n"
"s_add_u32 s28, s10, s12\n"
"s_addc_u32 s29, s11, 0\n"
"s_and_b32 s29, s29, 0xffff\n"
"s_sub_u32 s30, 4718592, s12\n"
"s_mov_b32 s31, 0x20000\n"
"s_mov_b32 s70, s56\n"
"s_mov_b32 s71, s57\n"
"s_mov_b32 s72, s58\n"
"s_cmp_lg_u32 s44, 0\n"
"s_cbranch_scc1 KF_E0\n"
"s_lshl_b32 s12, s2, 7\n"
"s_add_u32 s12, s12, 62914560\n"
"v_mov_b32 v4, s12\n"
"s_mov_b64 exec, 1\n"
"global_load_dwordx2 v[2:3], v4, s[20:21] sc1\n"
"s_mov_b64 exec, -1\n"
"KF_E0:\n"
"s_add_u32 m0, s52, 32768\n"
"s_add_u32 s54, s53, 0\n"
"buffer_load_dwordx4 v223, s[28:31], s54 offen nt lds\n"
"buffer_load_dwordx4 v224, s[28:31], s54 offen offset:1024 nt lds\n"
"s_add_u32 m0, s50, 65536\n"
"s_add_u32 s55, s51, 0\n"
"buffer_load_dwordx4 v221, s[24:27], s55 offen nt lds\n"
"buffer_load_dwordx4 v222, s[24:27], s55 offen offset:1024 nt lds\n"
"s_add_u32 m0, s52, 49152\n"
"s_add_u32 s54, s53, 262144\n"
"buffer_load_dwordx4 v223, s[28:31], s54 offen nt lds\n"
"buffer_load_dwordx4 v224, s[28:31], s54 offen offset:1024 nt lds\n"
"s_lshl_b32 s12, s72, 5\n"
"s_add_u32 s12, s12, s71\n"
"s_lshl_b32 s13, s12, 17\n"
"s_add_u32 s32, s6, s13\n"
"s_addc_u32 s33, s7, 0\n"
"s_and_b32 s33, s33, 0xffff\n"
"s_mov_b32 s34, 262144\n"
"s_mov_b32 s35, 0x20000\n"
"s_add_u32 s36, s8, s13\n"
"s_addc_u32 s37, s9, 0\n"
"s_and_b32 s37, s37, 0xffff\n"
"s_mov_b32 s38, 262144\n"
"s_mov_b32 s39, 0x20000\n"
"s_lshl_b32 s14, s70, 8\n"
"s_add_u32 s14, s14, s12\n"
"s_add_u32 s14, s14, s45\n"
"s_lshl_b32 s14, s14, 2\n"
"s_add_u32 s14, s14, s46\n"
"s_lshl_b32 s14, s14, 13\n"
"s_add_u32 s40, s20, s14\n"
"s_addc_u32 s41, s21, 0\n"
"s_and_b32 s41, s41, 0xffff\n"
"s_mov_b32 s42, 8192\n"
"s_mov_b32 s43, 0x20000\n"
"s_mov_b32 s62, 0x3e0293ee\n"
"s_mov_b32 s63, 0x3e0293ee\n"
"s_mov_b32 s64, 0x41000000\n"
"v_and_b32 v234, 31, v230\n"
"v_lshrrev_b32 v235, 5, v230\n"
"v_and_b32 v237, 15, v230\n"
"v_xor_b32 v237, v237, v232\n"
"v_lshlrev_b32 v237, 4, v237\n"
"v_lshl_add_u32 v209, v232, 8, v237\n"
"v_xor_b32 v210, 64, v209\n"
"v_xor_b32 v211, 128, v209\n"
"v_xor_b32 v212, 192, v209\n"
"v_bfe_u32 v236, v230, 2, 2\n"
"v_lshlrev_b32 v237, 6, v236\n"
"v_bfe_u32 v238, v230, 4, 1\n"
"v_lshl_add_u32 v237, v238, 5, v237\n"
"v_and_b32 v238, 1, v230\n"
"v_lshl_add_u32 v237, v238, 4, v237\n"
"v_bfe_u32 v238, v230, 1, 1\n"
"v_lshl_add_u32 v237, v238, 3, v237\n"
"v_lshl_add_u32 v219, v235, 11, v237\n"
"s_lshl_b32 s12, s45, 13\n"
"s_add_u32 s12, s12, 32768\n"
"v_add_u32 v219, s12, v219\n"
"v_add_u32 v220, 0x18000, v219\n"
"v_lshl_add_u32 v208, v235, 10, v237\n"
"s_add_u32 s13, s47, 32768\n"
"v_add_u32 v208, s13, v208\n"
"v_bfe_u32 v238, v234, 1, 3\n"
"v_xor_b32 v238, v238, v235\n"
"v_lshlrev_b32 v238, 4, v238\n"
"v_lshl_add_u32 v238, v234, 7, v238\n"
"s_lshl_b32 s12, s46, 12\n"
"s_add_u32 s12, s12, 65536\n"
"v_add_u32 v215, s12, v238\n"
"v_xor_b32 v216, 32, v215\n"
"v_xor_b32 v217, 64, v215\n"
"v_xor_b32 v218, 96, v215\n"
"v_and_b32 v238, 15, v234\n"
"v_xor_b32 v238, v238, v235\n"
"v_lshlrev_b32 v238, 4, v238\n"
"v_lshl_add_u32 v238, v234, 8, v238\n"
"v_add_u32 v200, s47, v238\n"
"v_xor_b32 v201, 32, v200\n"
"v_xor_b32 v202, 64, v200\n"
"v_xor_b32 v203, 96, v200\n"
"v_xor_b32 v204, 128, v200\n"
"v_xor_b32 v205, 160, v200\n"
"v_xor_b32 v206, 192, v200\n"
"v_xor_b32 v207, 224, v200\n"
"v_lshlrev_b32 v240, 4, v230\n"
"s_add_u32 s12, s49, 32768\n"
"v_add_u32 v214, s12, v240\n"
"v_and_b32 v237, 3, v230\n"
"v_bfe_u32 v238, v230, 2, 1\n"
"v_bfe_u32 v231, v230, 4, 1\n"
"v_xor_b32 v238, v238, v231\n"
"v_or_b32 v237, v237, v238\n"
"v_cmp_eq_u32 vcc, 0, v237\n"
"v_mov_b32 v238, 0x3f803f80\n"
"s_nop 0\n"
"v_cndmask_b32 v196, 0, v238, vcc\n"
"v_mov_b32 v197, v196\n"
"v_mov_b32 v198, v196\n"
"v_mov_b32 v199, v196\n"
"v_sub_u32 v237, 1, v235\n"
"v_cvt_f32_u32 v229, v237\n"
"s_and_b32 s73, s2, 3\n"
"s_lshr_b32 s12, s2, 3\n"
"s_and_b32 s74, s12, 3\n"
"s_lshl_b32 s75, s70, 3\n"
"s_add_u32 s75, s75, s72\n"
"s_cmp_lg_u32 s70, 0\n"
"s_cselect_b64 s[94:95], s[96:97], s[94:95]\n"
"s_lshl_b32 s12, s74, 8\n"
"s_sub_u32 s12, s12, 2048\n"
"s_ashr_i32 s13, s12, 31\n"
"s_add_u32 s76, s94, s12\n"
"s_addc_u32 s77, s95, s13\n"
"s_and_b32 s77, s77, 0xffff\n"
"s_sub_u32 s78, 4718592, s12\n"
"s_mov_b32 s79, 0x20000\n"
"s_lshl_b32 s12, s75, 20\n"
"s_lshl_b32 s13, s73, 18\n"
"s_add_u32 s12, s12, s13\n"
"s_add_u32 s80, s20, s12\n"
"s_addc_u32 s81, s21, 0\n"
"s_and_b32 s81, s81, 0xffff\n"
"s_mov_b32 s82, 262144\n"
"s_mov_b32 s83, 0x20000\n"
"s_lshl_b32 s12, s75, 2\n"
"s_add_u32 s12, s12, s74\n"
"s_mul_i32 s12, s12, 147456\n"
"s_add_u32 s12, s12, 33554432\n"
"s_add_u32 s84, s20, s12\n"
"s_addc_u32 s85, s21, 0\n"
"s_and_b32 s85, s85, 0xffff\n"
"s_mov_b32 s86, 147456\n"
"s_mov_b32 s87, 0x20000\n"
"s_mul_i32 s12, s72, 294912\n"
"s_mul_i32 s13, s70, 1152\n"
"s_add_u32 s12, s12, s13\n"
"s_mul_i32 s13, s74, 288\n"
"s_add_u32 s12, s12, s13\n"
"s_add_u32 s88, s98, s12\n"
"s_addc_u32 s89, s99, 0\n"
"s_and_b32 s89, s89, 0xffff\n"
"s_mov_b32 s90, 292896\n"
"s_mov_b32 s91, 0x20000\n"
"s_lshl_b32 s12, s73, 10\n"
"s_lshl_b32 s13, s44, 3\n"
"s_add_u32 s12, s12, s13\n"
"s_mul_i32 s92, s12, 1152\n"
"s_lshl_b32 s93, s46, 13\n"
"s_lshl_b32 s12, s45, 11\n"
"s_add_u32 s93, s93, s12\n"
"s_mul_i32 s94, s44, 3072\n"
"s_lshl_b32 s95, s46, 12\n"
"s_lshl_b32 s12, s45, 11\n"
"s_add_u32 s95, s95, s12\n"
"s_add_u32 s95, s95, 24576\n"
"v_lshrrev_b32 v234, 5, v230\n"
"v_bfe_u32 v235, v230, 2, 2\n"
"v_lshl_add_u32 v236, v234, 2, v235\n"
"v_mul_u32_u24 v242, 0x480, v236\n"
"v_bfe_u32 v236, v230, 4, 1\n"
"v_lshl_add_u32 v242, v236, 6, v242\n"
"v_and_b32 v237, 3, v230\n"
"v_lshl_add_u32 v242, v237, 4, v242\n"
"v_add_u32 v242, 0x800, v242\n"
"v_add_u32 v243, 0xfffffc80, v242\n"
"v_add_u32 v244, 0xfffff900, v242\n"
"v_mul_u32_u24 v238, 0xc00, v234\n"
"v_lshl_add_u32 v238, v235, 6, v238\n"
"v_lshl_add_u32 v238, v237, 3, v238\n"
"s_mul_i32 s12, s45, 6\n"
"s_add_u32 s12, s12, s74\n"
"s_xor_b32 s13, s45, 1\n"
"v_add_u32 v239, s12, v236\n"
"v_lshrrev_b32 v231, 2, v239\n"
"v_lshl_add_u32 v245, v231, 10, v238\n"
"v_bfe_u32 v231, v239, 1, 1\n"
"v_lshl_add_u32 v245, v231, 8, v245\n"
"v_and_b32 v231, 1, v239\n"
"v_lshl_add_u32 v245, v231, 5, v245\n"
"v_add_u32 v248, 0x14000, v245\n"
"v_and_b32 v239, s13, v236\n"
"v_add_u32 v239, s12, v239\n"
"v_add_u32 v239, 2, v239\n"
"v_lshrrev_b32 v231, 2, v239\n"
"v_lshl_add_u32 v246, v231, 10, v238\n"
"v_bfe_u32 v231, v239, 1, 1\n"
"v_lshl_add_u32 v246, v231, 8, v246\n"
"v_and_b32 v231, 1, v239\n"
"v_lshl_add_u32 v246, v231, 5, v246\n"
"v_add_u32 v249, 0x14000, v246\n"
"v_and_b32 v239, s13, v236\n"
"v_add_u32 v239, s12, v239\n"
"v_add_u32 v239, 4, v239\n"
"v_lshrrev_b32 v231, 2, v239\n"
"v_lshl_add_u32 v247, v231, 10, v238\n"
"v_bfe_u32 v231, v239, 1, 1\n"
"v_lshl_add_u32 v247, v231, 8, v247\n"
"v_and_b32 v231, 1, v239\n"
"v_lshl_add_u32 v247, v231, 5, v247\n"
"v_add_u32 v250, 0x14000, v247\n"
"s_lshl_b32 s12, s46, 12\n"
"s_add_u32 s12, s12, 24576\n"
"v_add_u32 v251, s12, v240\n"
"v_add_u32 v252, 0x14000, v251\n"
"s_lshl_b32 s13, s45, 11\n"
"v_add_u32 v253, s13, v251\n"
"v_add_u32 v254, 0x14000, v253\n"
"s_mul_i32 s13, s45, 81920\n"
"v_add_u32 v255, s13, v251\n"
"s_waitcnt vmcnt(2)\n"
"s_waitcnt lgkmcnt(0)\n"
"s_barrier\n"
"s_cmp_lg_u32 s44, 0\n"
"s_cbranch_scc1 KF_E1\n"
"v_add_co_u32 v2, vcc, 1, v2\n"
"s_nop 1\n"
"v_addc_co_u32 v3, vcc, 0, v3, vcc\n"
"s_nop 1\n"
"v_readfirstlane_b32 s96, v2\n"
"v_readfirstlane_b32 s97, v3\n"
"s_mov_b64 exec, 1\n"
"global_store_dwordx2 v4, v[2:3], s[20:21] sc1\n"
"s_mov_b64 exec, -1\n"
"KF_E1:\n"
"s_add_u32 m0, s52, 131072\n"
"s_add_u32 s54, s53, 524288\n"
"buffer_load_dwordx4 v223, s[28:31], s54 offen nt lds\n"
"buffer_load_dwordx4 v224, s[28:31], s54 offen offset:1024 nt lds\n"
"s_add_u32 m0, s50, 114688\n"
"s_add_u32 s55, s51, 128\n"
"buffer_load_dwordx4 v221, s[24:27], s55 offen nt lds\n"
"buffer_load_dwordx4 v222, s[24:27], s55 offen offset:1024 nt lds\n"
"ds_read_b128 v[32:35], v215 offset:0\n"
"ds_read_b128 v[36:39], v216 offset:0\n"
"ds_read_b128 v[40:43], v217 offset:0\n"
"ds_read_b128 v[44:47], v218 offset:0\n"
"ds_read_b64_tr_b16 v[160:161], v219 offset:0\n"
"ds_read_b64_tr_b16 v[162:163], v219 offset:1024\n"
"ds_read_b64_tr_b16 v[164:165], v219 offset:256\n"
"ds_read_b64_tr_b16 v[166:167], v219 offset:1280\n"
"ds_read_b64_tr_b16 v[168:169], v219 offset:512\n"
"ds_read_b64_tr_b16 v[170:171], v219 offset:1536\n"
"ds_read_b64_tr_b16 v[172:173], v219 offset:768\n"
"ds_read_b64_tr_b16 v[174:175], v219 offset:1792\n"
"s_add_u32 m0, s52, 147456\n"
"s_add_u32 s54, s53, 786432\n"
"buffer_load_dwordx4 v223, s[28:31], s54 offen nt lds\n"
"buffer_load_dwordx4 v224, s[28:31], s54 offen offset:1024 nt lds\n"
"s_add_u32 m0, s49, 0\n"
"s_add_u32 s56, s48, 0\n"
"buffer_load_dwordx4 v209, s[32:35], s56 offen nt lds\n"
"buffer_load_dwordx4 v210, s[32:35], s56 offen offset:1024 nt lds\n"
"s_waitcnt lgkmcnt(6)\n"
"v_mfma_f32_32x32x16_bf16 v[96:111], v[160:163], v[32:35], 0\n"
"ds_read_b64_tr_b16 v[176:177], v219 offset:4096\n"
"ds_read_b64_tr_b16 v[178:179], v219 offset:5120\n"
"s_waitcnt lgkmcnt(6)\n"
"v_mfma_f32_32x32x16_bf16 v[112:127], v[164:167], v[32:35], 0\n"
"ds_read_b64_tr_b16 v[180:181], v219 offset:4352\n"
"ds_read_b64_tr_b16 v[182:183], v219 offset:5376\n"
"s_waitcnt lgkmcnt(6)\n"
"v_mfma_f32_32x32x16_bf16 v[128:143], v[168:171], v[32:35], 0\n"
"ds_read_b64_tr_b16 v[184:185], v219 offset:4608\n"
"ds_read_b64_tr_b16 v[186:187], v219 offset:5632\n"
"s_waitcnt lgkmcnt(6)\n"
"v_mfma_f32_32x32x16_bf16 v[144:159], v[172:175], v[32:35], 0\n"
"ds_read_b64_tr_b16 v[188:189], v219 offset:4864\n"
"ds_read_b64_tr_b16 v[190:191], v219 offset:5888\n"
"s_waitcnt vmcnt(8)\n"
"s_waitcnt lgkmcnt(0)\n"
"s_barrier\n"
"v_mfma_f32_32x32x16_bf16 v[96:111], v[176:179], v[36:39], v[96:111]\n"
"ds_read_b64_tr_b16 v[160:161], v219 offset:16384\n"
"ds_read_b64_tr_b16 v[162:163], v219 offset:17408\n"
"v_mfma_f32_32x32x16_bf16 v[112:127], v[180:183], v[36:39], v[112:127]\n"
"ds_read_b64_tr_b16 v[164:165], v219 offset:16640\n"
"ds_read_b64_tr_b16 v[166:167], v219 offset:17664\n"
"v_mfma_f32_32x32x16_bf16 v[128:143], v[184:187], v[36:39], v[128:143]\n"
"ds_read_b64_tr_b16 v[168:169], v219 offset:16896\n"
"ds_read_b64_tr_b16 v[170:171], v219 offset:17920\n"
"v_mfma_f32_32x32x16_bf16 v[144:159], v[188:191], v[36:39], v[144:159]\n"
"ds_read_b64_tr_b16 v[172:173], v219 offset:17152\n"
"ds_read_b64_tr_b16 v[174:175], v219 offset:18176\n"
"s_waitcnt lgkmcnt(6)\n"
"v_mfma_f32_32x32x16_bf16 v[96:111], v[160:163], v[40:43], v[96:111]\n"
"ds_read_b64_tr_b16 v[176:177], v219 offset:20480\n"
"ds_read_b64_tr_b16 v[178:179], v219 offset:21504\n"
"s_add_u32 m0, s52, 32768\n"
"s_add_u32 s54, s53, 1048576\n"
"buffer_load_dwordx4 v223, s[28:31], s54 offen nt lds\n"
"s_waitcnt lgkmcnt(6)\n"
"v_mfma_f32_32x32x16_bf16 v[112:127], v[164:167], v[40:43], v[112:127]\n"
"ds_read_b64_tr_b16 v[180:181], v219 offset:20736\n"
"ds_read_b64_tr_b16 v[182:183], v219 offset:21760\n"
"buffer_load_dwordx4 v224, s[28:31], s54 offen offset:1024 nt lds\n"
"s_waitcnt lgkmcnt(6)\n"
"v_mfma_f32_32x32x16_bf16 v[128:143], v[168:171], v[40:43], v[128:143]\n"
"ds_read_b64_tr_b16 v[184:185], v219 offset:20992\n"
"ds_read_b64_tr_b16 v[186:187], v219 offset:22016\n"
"s_add_u32 m0, s50, 65536\n"
"s_add_u32 s55, s51, 256\n"
"buffer_load_dwordx4 v221, s[24:27], s55 offen nt lds\n"
"s_waitcnt lgkmcnt(6)\n"
"v_mfma_f32_32x32x16_bf16 v[144:159], v[172:175], v[40:43], v[144:159]\n"
"ds_read_b64_tr_b16 v[188:189], v219 offset:21248\n"
"ds_read_b64_tr_b16 v[190:191], v219 offset:22272\n"
"buffer_load_dwordx4 v222, s[24:27], s55 offen offset:1024 nt lds\n"
"s_waitcnt vmcnt(8)\n"
"s_waitcnt lgkmcnt(0)\n"
"s_barrier\n"
"v_mfma_f32_32x32x16_bf16 v[96:111], v[176:179], v[44:47], v[96:111]\n"
"ds_read_b64_tr_b16 v[160:161], v220 offset:0\n"
"ds_read_b64_tr_b16 v[162:163], v220 offset:1024\n"
"ds_read_b128 v[32:35], v215 offset:49152\n"
"v_mfma_f32_32x32x16_bf16 v[112:127], v[180:183], v[44:47], v[112:127]\n"
"ds_read_b64_tr_b16 v[164:165], v220 offset:256\n"
"ds_read_b64_tr_b16 v[166:167], v220 offset:1280\n"
"ds_read_b128 v[36:39], v216 offset:49152\n"
"v_mfma_f32_32x32x16_bf16 v[128:143], v[184:187], v[44:47], v[128:143]\n"
"ds_read_b64_tr_b16 v[168:169], v220 offset:512\n"
"ds_read_b64_tr_b16 v[170:171], v220 offset:1536\n"
"ds_read_b128 v[40:43], v217 offset:49152\n"
"v_mfma_f32_32x32x16_bf16 v[144:159], v[188:191], v[44:47], v[144:159]\n"
"ds_read_b64_tr_b16 v[172:173], v220 offset:768\n"
"ds_read_b64_tr_b16 v[174:175], v220 offset:1792\n"
"s_waitcnt lgkmcnt(8)\n"
"v_mfma_f32_32x32x16_bf16 v[96:111], v[160:163], v[32:35], v[96:111]\n"
"ds_read_b128 v[44:47], v218 offset:49152\n"
"ds_read_b64_tr_b16 v[176:177], v220 offset:4096\n"
"ds_read_b64_tr_b16 v[178:179], v220 offset:5120\n"
"s_add_u32 m0, s52, 49152\n"
"s_add_u32 s54, s53, 1310720\n"
"buffer_load_dwordx4 v223, s[28:31], s54 offen nt lds\n"
"s_waitcnt lgkmcnt(9)\n"
"v_mfma_f32_32x32x16_bf16 v[112:127], v[164:167], v[32:35], v[112:127]\n"
"ds_read_b64_tr_b16 v[180:181], v220 offset:4352\n"
"ds_read_b64_tr_b16 v[182:183], v220 offset:5376\n"
"buffer_load_dwordx4 v224, s[28:31], s54 offen offset:1024 nt lds\n"
"s_waitcnt lgkmcnt(8)\n"
"v_mfma_f32_32x32x16_bf16 v[128:143], v[168:171], v[32:35], v[128:143]\n"
"ds_read_b64_tr_b16 v[184:185], v220 offset:4608\n"
"ds_read_b64_tr_b16 v[186:187], v220 offset:5632\n"
"s_add_u32 m0, s49, 0\n"
"s_add_u32 s56, s48, 0\n"
"buffer_load_dwordx4 v211, s[32:35], s56 offen offset:2048 nt lds\n"
"s_waitcnt lgkmcnt(7)\n"
"v_mfma_f32_32x32x16_bf16 v[144:159], v[172:175], v[32:35], v[144:159]\n"
"ds_read_b64_tr_b16 v[188:189], v220 offset:4864\n"
"ds_read_b64_tr_b16 v[190:191], v220 offset:5888\n"
"buffer_load_dwordx4 v212, s[32:35], s56 offen offset:3072 nt lds\n"
"s_waitcnt vmcnt(10)\n"
"s_waitcnt lgkmcnt(0)\n"
"s_barrier\n"
"v_mfma_f32_32x32x16_bf16 v[96:111], v[176:179], v[36:39], v[96:111]\n"
"ds_read_b64_tr_b16 v[160:161], v220 offset:16384\n"
"ds_read_b64_tr_b16 v[162:163], v220 offset:17408\n"
"v_mfma_f32_32x32x16_bf16 v[112:127], v[180:183], v[36:39], v[112:127]\n"
"ds_read_b64_tr_b16 v[164:165], v220 offset:16640\n"
"ds_read_b64_tr_b16 v[166:167], v220 offset:17664\n"
"v_mfma_f32_32x32x16_bf16 v[128:143], v[184:187], v[36:39], v[128:143]\n"
"ds_read_b64_tr_b16 v[168:169], v220 offset:16896\n"
"ds_read_b64_tr_b16 v[170:171], v220 offset:17920\n"
"v_mfma_f32_32x32x16_bf16 v[144:159], v[188:191], v[36:39], v[144:159]\n"
"ds_read_b64_tr_b16 v[172:173], v220 offset:17152\n"
"ds_read_b64_tr_b16 v[174:175], v220 offset:18176\n"
"s_waitcnt lgkmcnt(6)\n"
"v_mfma_f32_32x32x16_bf16 v[96:111], v[160:163], v[40:43], v[96:111]\n"
"ds_read_b64_tr_b16 v[176:177], v220 offset:20480\n"
"ds_read_b64_tr_b16 v[178:179], v220 offset:21504\n"
"s_add_u32 m0, s52, 131072\n"
"s_add_u32 s54, s53, 1572864\n"
"buffer_load_dwordx4 v223, s[28:31], s54 offen nt lds\n"
"s_waitcnt lgkmcnt(6)\n"
"v_mfma_f32_32x32x16_bf16 v[112:127], v[164:167], v[40:43], v[112:127]\n"
"ds_read_b64_tr_b16 v[180:181], v220 offset:20736\n"
"ds_read_b64_tr_b16 v[182:183], v220 offset:21760\n"
"buffer_load_dwordx4 v224, s[28:31], s54 offen offset:1024 nt lds\n"
"s_waitcnt lgkmcnt(6)\n"
"v_mfma_f32_32x32x16_bf16 v[128:143], v[168:171], v[40:43], v[128:143]\n"
"ds_read_b64_tr_b16 v[184:185], v220 offset:20992\n"
"ds_read_b64_tr_b16 v[186:187], v220 offset:22016\n"
"s_add_u32 m0, s50, 114688\n"
"s_add_u32 s55, s51, 384\n"
"buffer_load_dwordx4 v221, s[24:27], s55 offen nt lds\n"
"s_waitcnt lgkmcnt(6)\n"
"v_mfma_f32_32x32x16_bf16 v[144:159], v[172:175], v[40:43], v[144:159]\n"
"ds_read_b64_tr_b16 v[188:189], v220 offset:21248\n"
"ds_read_b64_tr_b16 v[190:191], v220 offset:22272\n"
"buffer_load_dwordx4 v222, s[24:27], s55 offen offset:1024 nt lds\n"
"s_waitcnt vmcnt(8)\n"
"s_waitcnt lgkmcnt(0)\n"
"s_barrier\n"
"v_mfma_f32_32x32x16_bf16 v[96:111], v[176:179], v[44:47], v[96:111]\n"
"ds_read_b64_tr_b16 v[160:161], v219 offset:0\n"
"ds_read_b64_tr_b16 v[162:163], v219 offset:1024\n"
"ds_read_b128 v[32:35], v215 offset:0\n"
"v_mfma_f32_32x32x16_bf16 v[112:127], v[180:183], v[44:47], v[112:127]\n"
"ds_read_b64_tr_b16 v[164:165], v219 offset:256\n"
"ds_read_b64_tr_b16 v[166:167], v219 offset:1280\n"
"ds_read_b128 v[36:39], v216 offset:0\n"
"v_mfma_f32_32x32x16_bf16 v[128:143], v[184:187], v[44:47], v[128:143]\n"
"ds_read_b64_tr_b16 v[168:169], v219 offset:512\n"
"ds_read_b64_tr_b16 v[170:171], v219 offset:1536\n"
"ds_read_b128 v[40:43], v217 offset:0\n"
"v_mfma_f32_32x32x16_bf16 v[144:159], v[188:191], v[44:47], v[144:159]\n"
"ds_read_b64_tr_b16 v[172:173], v219 offset:768\n"
"ds_read_b64_tr_b16 v[174:175], v219 offset:1792\n"
"s_waitcnt lgkmcnt(8)\n"
"v_mfma_f32_32x32x16_bf16 v[96:111], v[160:163], v[32:35], v[96:111]\n"
"ds_read_b128 v[44:47], v218 offset:0\n"
"ds_read_b64_tr_b16 v[176:177], v219 offset:4096\n"
"ds_read_b64_tr_b16 v[178:179], v219 offset:5120\n"
"s_add_u32 m0, s52, 147456\n"
"s_add_u32 s54, s53, 1835008\n"
"buffer_load_dwordx4 v223, s[28:31], s54 offen nt lds\n"
"s_waitcnt lgkmcnt(9)\n"
"v_mfma_f32_32x32x16_bf16 v[112:127], v[164:167], v[32:35], v[112:127]\n"
"ds_read_b64_tr_b16 v[180:181], v219 offset:4352\n"
"ds_read_b64_tr_b16 v[182:183], v219 offset:5376\n"
"buffer_load_dwordx4 v224, s[28:31], s54 offen offset:1024 nt lds\n"
"s_waitcnt lgkmcnt(8)\n"
"v_mfma_f32_32x32x16_bf16 v[128:143], v[168:171], v[32:35], v[128:143]\n"
"ds_read_b64_tr_b16 v[184:185], v219 offset:4608\n"
"ds_read_b64_tr_b16 v[186:187], v219 offset:5632\n"
"s_add_u32 m0, s49, 16384\n"
"s_add_u32 s56, s48, 16384\n"
"buffer_load_dwordx4 v209, s[32:35], s56 offen nt lds\n"
"s_waitcnt lgkmcnt(7)\n"
"v_mfma_f32_32x32x16_bf16 v[144:159], v[172:175], v[32:35], v[144:159]\n"
"ds_read_b64_tr_b16 v[188:189], v219 offset:4864\n"
"ds_read_b64_tr_b16 v[190:191], v219 offset:5888\n"
"buffer_load_dwordx4 v210, s[32:35], s56 offen offset:1024 nt lds\n"
"s_waitcnt vmcnt(10)\n"
"s_waitcnt lgkmcnt(0)\n"
"s_barrier\n"
"v_mfma_f32_32x32x16_bf16 v[96:111], v[176:179], v[36:39], v[96:111]\n"
"ds_read_b64_tr_b16 v[160:161], v219 offset:16384\n"
"ds_read_b64_tr_b16 v[162:163], v219 offset:17408\n"
"v_mfma_f32_32x32x16_bf16 v[112:127], v[180:183], v[36:39], v[112:127]\n"
"ds_read_b64_tr_b16 v[164:165], v219 offset:16640\n"
"ds_read_b64_tr_b16 v[166:167], v219 offset:17664\n"
"v_mfma_f32_32x32x16_bf16 v[128:143], v[184:187], v[36:39], v[128:143]\n"
"ds_read_b64_tr_b16 v[168:169], v219 offset:16896\n"
"ds_read_b64_tr_b16 v[170:171], v219 offset:17920\n"
"v_mfma_f32_32x32x16_bf16 v[144:159], v[188:191], v[36:39], v[144:159]\n"
"ds_read_b64_tr_b16 v[172:173], v219 offset:17152\n"
"ds_read_b64_tr_b16 v[174:175], v219 offset:18176\n"
"s_waitcnt lgkmcnt(6)\n"
"v_mfma_f32_32x32x16_bf16 v[96:111], v[160:163], v[40:43], v[96:111]\n"
"ds_read_b64_tr_b16 v[176:177], v219 offset:20480\n"
"ds_read_b64_tr_b16 v[178:179], v219 offset:21504\n"
"s_add_u32 m0, s52, 32768\n"
"s_add_u32 s54, s53, 2097152\n"
"buffer_load_dwordx4 v223, s[28:31], s54 offen nt lds\n"
"s_waitcnt lgkmcnt(6)\n"
"v_mfma_f32_32x32x16_bf16 v[112:127], v[164:167], v[40:43], v[112:127]\n"
"ds_read_b64_tr_b16 v[180:181], v219 offset:20736\n"
"ds_read_b64_tr_b16 v[182:183], v219 offset:21760\n"
"buffer_load_dwordx4 v224, s[28:31], s54 offen offset:1024 nt lds\n"
"s_waitcnt lgkmcnt(6)\n"
"v_mfma_f32_32x32x16_bf16 v[128:143], v[168:171], v[40:43], v[128:143]\n"
"ds_read_b64_tr_b16 v[184:185], v219 offset:20992\n"
"ds_read_b64_tr_b16 v[186:187], v219 offset:22016\n"
"s_add_u32 m0, s50, 65536\n"
"s_add_u32 s55, s51, 512\n"
"buffer_load_dwordx4 v221, s[24:27], s55 offen nt lds\n"
"s_waitcnt lgkmcnt(6)\n"
"v_mfma_f32_32x32x16_bf16 v[144:159], v[172:175], v[40:43], v[144:159]\n"
"ds_read_b64_tr_b16 v[188:189], v219 offset:21248\n"
"ds_read_b64_tr_b16 v[190:191], v219 offset:22272\n"
"buffer_load_dwordx4 v222, s[24:27], s55 offen offset:1024 nt lds\n"
"s_waitcnt vmcnt(8)\n"
"s_waitcnt lgkmcnt(0)\n"
"s_barrier\n"
"v_mfma_f32_32x32x16_bf16 v[96:111], v[176:179], v[44:47], v[96:111]\n"
"ds_read_b64_tr_b16 v[160:161], v220 offset:0\n"
"ds_read_b64_tr_b16 v[162:163], v220 offset:1024\n"
"ds_read_b128 v[32:35], v215 offset:49152\n"
"v_mfma_f32_32x32x16_bf16 v[112:127], v[180:183], v[44:47], v[112:127]\n"
"ds_read_b64_tr_b16 v[164:165], v220 offset:256\n"
"ds_read_b64_tr_b16 v[166:167], v220 offset:1280\n"
"ds_read_b128 v[36:39], v216 offset:49152\n"
"v_mfma_f32_32x32x16_bf16 v[128:143], v[184:187], v[44:47], v[128:143]\n"
"ds_read_b64_tr_b16 v[168:169], v220 offset:512\n"
"ds_read_b64_tr_b16 v[170:171], v220 offset:1536\n"
"ds_read_b128 v[40:43], v217 offset:49152\n"
"v_mfma_f32_32x32x16_bf16 v[144:159], v[188:191], v[44:47], v[144:159]\n"
"ds_read_b64_tr_b16 v[172:173], v220 offset:768\n"
"ds_read_b64_tr_b16 v[174:175], v220 offset:1792\n"
"s_waitcnt lgkmcnt(8)\n"
"v_mfma_f32_32x32x16_bf16 v[96:111], v[160:163], v[32:35], v[96:111]\n"
"ds_read_b128 v[44:47], v218 offset:49152\n"
"ds_read_b64_tr_b16 v[176:177], v220 offset:4096\n"
"ds_read_b64_tr_b16 v[178:179], v220 offset:5120\n"
"s_add_u32 m0, s52, 49152\n"
"s_add_u32 s54, s53, 2359296\n"
"buffer_load_dwordx4 v223, s[28:31], s54 offen nt lds\n"
"s_waitcnt lgkmcnt(9)\n"
"v_mfma_f32_32x32x16_bf16 v[112:127], v[164:167], v[32:35], v[112:127]\n"
"ds_read_b64_tr_b16 v[180:181], v220 offset:4352\n"
"ds_read_b64_tr_b16 v[182:183], v220 offset:5376\n"
"buffer_load_dwordx4 v224, s[28:31], s54 offen offset:1024 nt lds\n"
"s_waitcnt lgkmcnt(8)\n"
"v_mfma_f32_32x32x16_bf16 v[128:143], v[168:171], v[32:35], v[128:143]\n"
"ds_read_b64_tr_b16 v[184:185], v220 offset:4608\n"
"ds_read_b64_tr_b16 v[186:187], v220 offset:5632\n"
"s_add_u32 m0, s49, 16384\n"
"s_add_u32 s56, s48, 16384\n"
"buffer_load_dwordx4 v211, s[32:35], s56 offen offset:2048 nt lds\n"
"s_waitcnt lgkmcnt(7)\n"
"v_mfma_f32_32x32x16_bf16 v[144:159], v[172:175], v[32:35], v[144:159]\n"
"ds_read_b64_tr_b16 v[188:189], v220 offset:4864\n"
"ds_read_b64_tr_b16 v[190:191], v220 offset:5888\n"
"buffer_load_dwordx4 v212, s[32:35], s56 offen offset:3072 nt lds\n"
"s_waitcnt vmcnt(10)\n"
"s_waitcnt lgkmcnt(0)\n"
"s_barrier\n"
"v_mfma_f32_32x32x16_bf16 v[96:111], v[176:179], v[36:39], v[96:111]\n"
"ds_read_b64_tr_b16 v[160:161], v220 offset:16384\n"
"ds_read_b64_tr_b16 v[162:163], v220 offset:17408\n"
"v_mfma_f32_32x32x16_bf16 v[112:127], v[180:183], v[36:39], v[112:127]\n"
"ds_read_b64_tr_b16 v[164:165], v220 offset:16640\n"
"ds_read_b64_tr_b16 v[166:167], v220 offset:17664\n"
"v_mfma_f32_32x32x16_bf16 v[128:143], v[184:187], v[36:39], v[128:143]\n"
"ds_read_b64_tr_b16 v[168:169], v220 offset:16896\n"
"ds_read_b64_tr_b16 v[170:171], v220 offset:17920\n"
"v_mfma_f32_32x32x16_bf16 v[144:159], v[188:191], v[36:39], v[144:159]\n"
"ds_read_b64_tr_b16 v[172:173], v220 offset:17152\n"
"ds_read_b64_tr_b16 v[174:175], v220 offset:18176\n"
"s_waitcnt lgkmcnt(6)\n"
"v_mfma_f32_32x32x16_bf16 v[96:111], v[160:163], v[40:43], v[96:111]\n"
"ds_read_b64_tr_b16 v[176:177], v220 offset:20480\n"
"ds_read_b64_tr_b16 v[178:179], v220 offset:21504\n"
"s_add_u32 m0, s52, 131072\n"
"s_add_u32 s54, s53, 2621440\n"
"buffer_load_dwordx4 v223, s[28:31], s54 offen nt lds\n"
"s_waitcnt lgkmcnt(6)\n"
"v_mfma_f32_32x32x16_bf16 v[112:127], v[164:167], v[40:43], v[112:127]\n"
"ds_read_b64_tr_b16 v[180:181], v220 offset:20736\n"
"ds_read_b64_tr_b16 v[182:183], v220 offset:21760\n"
"buffer_load_dwordx4 v224, s[28:31], s54 offen offset:1024 nt lds\n"
"s_waitcnt lgkmcnt(6)\n"
"v_mfma_f32_32x32x16_bf16 v[128:143], v[168:171], v[40:43], v[128:143]\n"
"ds_read_b64_tr_b16 v[184:185], v220 offset:20992\n"
"ds_read_b64_tr_b16 v[186:187], v220 offset:22016\n"
"s_add_u32 m0, s50, 114688\n"
"s_add_u32 s55, s51, 640\n"
"buffer_load_dwordx4 v221, s[24:27], s55 offen nt lds\n"
"s_waitcnt lgkmcnt(6)\n"
"v_mfma_f32_32x32x16_bf16 v[144:159], v[172:175], v[40:43], v[144:159]\n"
"ds_read_b64_tr_b16 v[188:189], v220 offset:21248\n"
"ds_read_b64_tr_b16 v[190:191], v220 offset:22272\n"
"buffer_load_dwordx4 v222, s[24:27], s55 offen offset:1024 nt lds\n"
"s_waitcnt vmcnt(8)\n"
"s_waitcnt lgkmcnt(0)\n"
"s_barrier\n"
"v_mfma_f32_32x32x16_bf16 v[96:111], v[176:179], v[44:47], v[96:111]\n"
"ds_read_b64_tr_b16 v[160:161], v219 offset:0\n"
"ds_read_b64_tr_b16 v[162:163], v219 offset:1024\n"
"ds_read_b128 v[32:35], v215 offset:0\n"
"v_mfma_f32_32x32x16_bf16 v[112:127], v[180:183], v[44:47], v[112:127]\n"
"ds_read_b64_tr_b16 v[164:165], v219 offset:256\n"
"ds_read_b64_tr_b16 v[166:167], v219 offset:1280\n"
"ds_read_b128 v[36:39], v216 offset:0\n"
"v_mfma_f32_32x32x16_bf16 v[128:143], v[184:187], v[44:47], v[128:143]\n"
"ds_read_b64_tr_b16 v[168:169], v219 offset:512\n"
"ds_read_b64_tr_b16 v[170:171], v219 offset:1536\n"
"ds_read_b128 v[40:43], v217 offset:0\n"
"v_mfma_f32_32x32x16_bf16 v[144:159], v[188:191], v[44:47], v[144:159]\n"
"ds_read_b64_tr_b16 v[172:173], v219 offset:768\n"
"ds_read_b64_tr_b16 v[174:175], v219 offset:1792\n"
"s_waitcnt lgkmcnt(8)\n"
"v_mfma_f32_32x32x16_bf16 v[96:111], v[160:163], v[32:35], v[96:111]\n"
"ds_read_b128 v[44:47], v218 offset:0\n"
"ds_read_b64_tr_b16 v[176:177], v219 offset:4096\n"
"ds_read_b64_tr_b16 v[178:179], v219 offset:5120\n"
"s_add_u32 m0, s52, 147456\n"
"s_add_u32 s54, s53, 2883584\n"
"buffer_load_dwordx4 v223, s[28:31], s54 offen nt lds\n"
"s_waitcnt lgkmcnt(9)\n"
"v_mfma_f32_32x32x16_bf16 v[112:127], v[164:167], v[32:35], v[112:127]\n"
"ds_read_b64_tr_b16 v[180:181], v219 offset:4352\n"
"ds_read_b64_tr_b16 v[182:183], v219 offset:5376\n"
"buffer_load_dwordx4 v224, s[28:31], s54 offen offset:1024 nt lds\n"
"s_waitcnt lgkmcnt(8)\n"
"v_mfma_f32_32x32x16_bf16 v[128:143], v[168:171], v[32:35], v[128:143]\n"
"ds_read_b64_tr_b16 v[184:185], v219 offset:4608\n"
"ds_read_b64_tr_b16 v[186:187], v219 offset:5632\n"
"s_add_u32 s57, s48, 0\n"
"buffer_load_dwordx4 v[0:3], v213, s[36:39], s57 offen nt\n"
"s_waitcnt lgkmcnt(7)\n"
"v_mfma_f32_32x32x16_bf16 v[144:159], v[172:175], v[32:35], v[144:159]\n"
"ds_read_b64_tr_b16 v[188:189], v219 offset:4864\n"
"ds_read_b64_tr_b16 v[190:191], v219 offset:5888\n"
"buffer_load_dwordx4 v[4:7], v213, s[36:39], s57 offen offset:1024 nt\n"
"s_waitcnt vmcnt(10)\n"
"s_waitcnt lgkmcnt(0)\n"
"s_barrier\n"
"v_mfma_f32_32x32x16_bf16 v[96:111], v[176:179], v[36:39], v[96:111]\n"
"ds_read_b64_tr_b16 v[160:161], v219 offset:16384\n"
"ds_read_b64_tr_b16 v[162:163], v219 offset:17408\n"
"v_mfma_f32_32x32x16_bf16 v[112:127], v[180:183], v[36:39], v[112:127]\n"
"ds_read_b64_tr_b16 v[164:165], v219 offset:16640\n"
"ds_read_b64_tr_b16 v[166:167], v219 offset:17664\n"
"v_mfma_f32_32x32x16_bf16 v[128:143], v[184:187], v[36:39], v[128:143]\n"
"ds_read_b64_tr_b16 v[168:169], v219 offset:16896\n"
"ds_read_b64_tr_b16 v[170:171], v219 offset:17920\n"
"v_mfma_f32_32x32x16_bf16 v[144:159], v[188:191], v[36:39], v[144:159]\n"
"ds_read_b64_tr_b16 v[172:173], v219 offset:17152\n"
"ds_read_b64_tr_b16 v[174:175], v219 offset:18176\n"
"s_waitcnt lgkmcnt(6)\n"
"v_mfma_f32_32x32x16_bf16 v[96:111], v[160:163], v[40:43], v[96:111]\n"
"ds_read_b64_tr_b16 v[176:177], v219 offset:20480\n"
"ds_read_b64_tr_b16 v[178:179], v219 offset:21504\n"
"s_add_u32 m0, s52, 32768\n"
"s_add_u32 s54, s53, 3145728\n"
"buffer_load_dwordx4 v223, s[28:31], s54 offen nt lds\n"
"s_waitcnt lgkmcnt(6)\n"
"v_mfma_f32_32x32x16_bf16 v[112:127], v[164:167], v[40:43], v[112:127]\n"
"ds_read_b64_tr_b16 v[180:181], v219 offset:20736\n"
"ds_read_b64_tr_b16 v[182:183], v219 offset:21760\n"
"buffer_load_dwordx4 v224, s[28:31], s54 offen offset:1024 nt lds\n"
"s_waitcnt lgkmcnt(6)\n"
"v_mfma_f32_32x32x16_bf16 v[128:143], v[168:171], v[40:43], v[128:143]\n"
"ds_read_b64_tr_b16 v[184:185], v219 offset:20992\n"
"ds_read_b64_tr_b16 v[186:187], v219 offset:22016\n"
"s_add_u32 m0, s50, 65536\n"
"s_add_u32 s55, s51, 768\n"
"buffer_load_dwordx4 v221, s[24:27], s55 offen nt lds\n"
"s_waitcnt lgkmcnt(6)\n"
"v_mfma_f32_32x32x16_bf16 v[144:159], v[172:175], v[40:43], v[144:159]\n"
"ds_read_b64_tr_b16 v[188:189], v219 offset:21248\n"
"ds_read_b64_tr_b16 v[190:191], v219 offset:22272\n"
"buffer_load_dwordx4 v222, s[24:27], s55 offen offset:1024 nt lds\n"
"s_waitcnt vmcnt(8)\n"
"s_waitcnt lgkmcnt(0)\n"
"s_barrier\n"
"v_mfma_f32_32x32x16_bf16 v[96:111], v[176:179], v[44:47], v[96:111]\n"
"ds_read_b64_tr_b16 v[160:161], v220 offset:0\n"
"ds_read_b64_tr_b16 v[162:163], v220 offset:1024\n"
"ds_read_b128 v[32:35], v215 offset:49152\n"
"v_mfma_f32_32x32x16_bf16 v[112:127], v[180:183], v[44:47], v[112:127]\n"
"ds_read_b64_tr_b16 v[164:165], v220 offset:256\n"
"ds_read_b64_tr_b16 v[166:167], v220 offset:1280\n"
"ds_read_b128 v[36:39], v216 offset:49152\n"
"v_mfma_f32_32x32x16_bf16 v[128:143], v[184:187], v[44:47], v[128:143]\n"
"ds_read_b64_tr_b16 v[168:169], v220 offset:512\n"
"ds_read_b64_tr_b16 v[170:171], v220 offset:1536\n"
"ds_read_b128 v[40:43], v217 offset:49152\n"
"v_mfma_f32_32x32x16_bf16 v[144:159], v[188:191], v[44:47], v[144:159]\n"
"ds_read_b64_tr_b16 v[172:173], v220 offset:768\n"
"ds_read_b64_tr_b16 v[174:175], v220 offset:1792\n"
"s_waitcnt lgkmcnt(8)\n"
"v_mfma_f32_32x32x16_bf16 v[96:111], v[160:163], v[32:35], v[96:111]\n"
"ds_read_b128 v[44:47], v218 offset:49152\n"
"ds_read_b64_tr_b16 v[176:177], v220 offset:4096\n"
"ds_read_b64_tr_b16 v[178:179], v220 offset:5120\n"
"s_add_u32 m0, s52, 49152\n"
"s_add_u32 s54, s53, 3407872\n"
"buffer_load_dwordx4 v223, s[28:31], s54 offen nt lds\n"
"s_waitcnt lgkmcnt(9)\n"
"v_mfma_f32_32x32x16_bf16 v[112:127], v[164:167], v[32:35], v[112:127]\n"
"ds_read_b64_tr_b16 v[180:181], v220 offset:4352\n"
"ds_read_b64_tr_b16 v[182:183], v220 offset:5376\n"
"buffer_load_dwordx4 v224, s[28:31], s54 offen offset:1024 nt lds\n"
"s_waitcnt lgkmcnt(8)\n"
"v_mfma_f32_32x32x16_bf16 v[128:143], v[168:171], v[32:35], v[128:143]\n"
"ds_read_b64_tr_b16 v[184:185], v220 offset:4608\n"
"ds_read_b64_tr_b16 v[186:187], v220 offset:5632\n"
"s_add_u32 s57, s48, 0\n"
"buffer_load_dwordx4 v[8:11], v213, s[36:39], s57 offen offset:2048 nt\n"
"s_waitcnt lgkmcnt(7)\n"
"v_mfma_f32_32x32x16_bf16 v[144:159], v[172:175], v[32:35], v[144:159]\n"
"ds_read_b64_tr_b16 v[188:189], v220 offset:4864\n"
"ds_read_b64_tr_b16 v[190:191], v220 offset:5888\n"
"buffer_load_dwordx4 v[12:15], v213, s[36:39], s57 offen offset:3072 nt\n"
"s_waitcnt vmcnt(10)\n"
"s_waitcnt lgkmcnt(0)\n"
"s_barrier\n"
"v_mfma_f32_32x32x16_bf16 v[96:111], v[176:179], v[36:39], v[96:111]\n"
"ds_read_b64_tr_b16 v[160:161], v220 offset:16384\n"
"ds_read_b64_tr_b16 v[162:163], v220 offset:17408\n"
"v_mfma_f32_32x32x16_bf16 v[112:127], v[180:183], v[36:39], v[112:127]\n"
"ds_read_b64_tr_b16 v[164:165], v220 offset:16640\n"
"ds_read_b64_tr_b16 v[166:167], v220 offset:17664\n"
"v_mfma_f32_32x32x16_bf16 v[128:143], v[184:187], v[36:39], v[128:143]\n"
"ds_read_b64_tr_b16 v[168:169], v220 offset:16896\n"
"ds_read_b64_tr_b16 v[170:171], v220 offset:17920\n"
"v_mfma_f32_32x32x16_bf16 v[144:159], v[188:191], v[36:39], v[144:159]\n"
"ds_read_b64_tr_b16 v[172:173], v220 offset:17152\n"
"ds_read_b64_tr_b16 v[174:175], v220 offset:18176\n"
"s_waitcnt lgkmcnt(6)\n"
"v_mfma_f32_32x32x16_bf16 v[96:111], v[160:163], v[40:43], v[96:111]\n"
"ds_read_b64_tr_b16 v[176:177], v220 offset:20480\n"
"ds_read_b64_tr_b16 v[178:179], v220 offset:21504\n"
"s_add_u32 m0, s52, 131072\n"
"s_add_u32 s54, s53, 3670016\n"
"buffer_load_dwordx4 v223, s[28:31], s54 offen nt lds\n"
"s_waitcnt lgkmcnt(6)\n"
"v_mfma_f32_32x32x16_bf16 v[112:127], v[164:167], v[40:43], v[112:127]\n"
"ds_read_b64_tr_b16 v[180:181], v220 offset:20736\n"
"ds_read_b64_tr_b16 v[182:183], v220 offset:21760\n"
"buffer_load_dwordx4 v224, s[28:31], s54 offen offset:1024 nt lds\n"
"s_waitcnt lgkmcnt(6)\n"
"v_mfma_f32_32x32x16_bf16 v[128:143], v[168:171], v[40:43], v[128:143]\n"
"ds_read_b64_tr_b16 v[184:185], v220 offset:20992\n"
"ds_read_b64_tr_b16 v[186:187], v220 offset:22016\n"
"s_add_u32 m0, s50, 114688\n"
"s_add_u32 s55, s51, 896\n"
"buffer_load_dwordx4 v221, s[24:27], s55 offen nt lds\n"
"s_waitcnt lgkmcnt(6)\n"
"v_mfma_f32_32x32x16_bf16 v[144:159], v[172:175], v[40:43], v[144:159]\n"
"ds_read_b64_tr_b16 v[188:189], v220 offset:21248\n"
"ds_read_b64_tr_b16 v[190:191], v220 offset:22272\n"
"buffer_load_dwordx4 v222, s[24:27], s55 offen offset:1024 nt lds\n"
"s_waitcnt vmcnt(8)\n"
"s_waitcnt lgkmcnt(0)\n"
"s_barrier\n"
"v_mfma_f32_32x32x16_bf16 v[96:111], v[176:179], v[44:47], v[96:111]\n"
"ds_read_b64_tr_b16 v[160:161], v219 offset:0\n"
"ds_read_b64_tr_b16 v[162:163], v219 offset:1024\n"
"ds_read_b128 v[32:35], v215 offset:0\n"
"v_mfma_f32_32x32x16_bf16 v[112:127], v[180:183], v[44:47], v[112:127]\n"
"ds_read_b64_tr_b16 v[164:165], v219 offset:256\n"
"ds_read_b64_tr_b16 v[166:167], v219 offset:1280\n"
"ds_read_b128 v[36:39], v216 offset:0\n"
"v_mfma_f32_32x32x16_bf16 v[128:143], v[184:187], v[44:47], v[128:143]\n"
"ds_read_b64_tr_b16 v[168:169], v219 offset:512\n"
"ds_read_b64_tr_b16 v[170:171], v219 offset:1536\n"
"ds_read_b128 v[40:43], v217 offset:0\n"
"v_mfma_f32_32x32x16_bf16 v[144:159], v[188:191], v[44:47], v[144:159]\n"
"ds_read_b64_tr_b16 v[172:173], v219 offset:768\n"
"ds_read_b64_tr_b16 v[174:175], v219 offset:1792\n"
"s_waitcnt lgkmcnt(8)\n"
"v_mfma_f32_32x32x16_bf16 v[96:111], v[160:163], v[32:35], v[96:111]\n"
"ds_read_b128 v[44:47], v218 offset:0\n"
"ds_read_b64_tr_b16 v[176:177], v219 offset:4096\n"
"ds_read_b64_tr_b16 v[178:179], v219 offset:5120\n"
"s_add_u32 m0, s52, 147456\n"
"s_add_u32 s54, s53, 3932160\n"
"buffer_load_dwordx4 v223, s[28:31], s54 offen nt lds\n"
"s_waitcnt lgkmcnt(9)\n"
"v_mfma_f32_32x32x16_bf16 v[112:127], v[164:167], v[32:35], v[112:127]\n"
"ds_read_b64_tr_b16 v[180:181], v219 offset:4352\n"
"ds_read_b64_tr_b16 v[182:183], v219 offset:5376\n"
"buffer_load_dwordx4 v224, s[28:31], s54 offen offset:1024 nt lds\n"
"s_waitcnt lgkmcnt(8)\n"
"v_mfma_f32_32x32x16_bf16 v[128:143], v[168:171], v[32:35], v[128:143]\n"
"ds_read_b64_tr_b16 v[184:185], v219 offset:4608\n"
"ds_read_b64_tr_b16 v[186:187], v219 offset:5632\n"
"s_add_u32 s57, s48, 16384\n"
"buffer_load_dwordx4 v[16:19], v213, s[36:39], s57 offen nt\n"
"s_waitcnt lgkmcnt(7)\n"
"v_mfma_f32_32x32x16_bf16 v[144:159], v[172:175], v[32:35], v[144:159]\n"
"ds_read_b64_tr_b16 v[188:189], v219 offset:4864\n"
"ds_read_b64_tr_b16 v[190:191], v219 offset:5888\n"
"buffer_load_dwordx4 v[20:23], v213, s[36:39], s57 offen offset:1024 nt\n"
"s_waitcnt vmcnt(10)\n"
"s_waitcnt lgkmcnt(0)\n"
"s_barrier\n"
"v_mfma_f32_32x32x16_bf16 v[96:111], v[176:179], v[36:39], v[96:111]\n"
"ds_read_b64_tr_b16 v[160:161], v219 offset:16384\n"
"ds_read_b64_tr_b16 v[162:163], v219 offset:17408\n"
"v_mfma_f32_32x32x16_bf16 v[112:127], v[180:183], v[36:39], v[112:127]\n"
"ds_read_b64_tr_b16 v[164:165], v219 offset:16640\n"
"ds_read_b64_tr_b16 v[166:167], v219 offset:17664\n"
"v_mfma_f32_32x32x16_bf16 v[128:143], v[184:187], v[36:39], v[128:143]\n"
"ds_read_b64_tr_b16 v[168:169], v219 offset:16896\n"
"ds_read_b64_tr_b16 v[170:171], v219 offset:17920\n"
"v_mfma_f32_32x32x16_bf16 v[144:159], v[188:191], v[36:39], v[144:159]\n"
"ds_read_b64_tr_b16 v[172:173], v219 offset:17152\n"
"ds_read_b64_tr_b16 v[174:175], v219 offset:18176\n"
"s_waitcnt lgkmcnt(6)\n"
"v_mfma_f32_32x32x16_bf16 v[96:111], v[160:163], v[40:43], v[96:111]\n"
"ds_read_b64_tr_b16 v[176:177], v219 offset:20480\n"
"ds_read_b64_tr_b16 v[178:179], v219 offset:21504\n"
"s_add_u32 m0, s52, 32768\n"
"s_add_u32 s54, s53, 4194304\n"
"buffer_load_dwordx4 v223, s[28:31], s54 offen nt lds\n"
"s_waitcnt lgkmcnt(6)\n"
"v_mfma_f32_32x32x16_bf16 v[112:127], v[164:167], v[40:43], v[112:127]\n"
"ds_read_b64_tr_b16 v[180:181], v219 offset:20736\n"
"ds_read_b64_tr_b16 v[182:183], v219 offset:21760\n"
"buffer_load_dwordx4 v224, s[28:31], s54 offen offset:1024 nt lds\n"
"s_waitcnt lgkmcnt(6)\n"
"v_mfma_f32_32x32x16_bf16 v[128:143], v[168:171], v[40:43], v[128:143]\n"
"ds_read_b64_tr_b16 v[184:185], v219 offset:20992\n"
"ds_read_b64_tr_b16 v[186:187], v219 offset:22016\n"
"s_add_u32 m0, s50, 65536\n"
"s_add_u32 s55, s51, 1024\n"
"buffer_load_dwordx4 v221, s[24:27], s55 offen nt lds\n"
"s_waitcnt lgkmcnt(6)\n"
"v_mfma_f32_32x32x16_bf16 v[144:159], v[172:175], v[40:43], v[144:159]\n"
"ds_read_b64_tr_b16 v[188:189], v219 offset:21248\n"
"ds_read_b64_tr_b16 v[190:191], v219 offset:22272\n"
"buffer_load_dwordx4 v222, s[24:27], s55 offen offset:1024 nt lds\n"
"s_waitcnt vmcnt(8)\n"
"s_waitcnt lgkmcnt(0)\n"
"s_barrier\n"
"v_mfma_f32_32x32x16_bf16 v[96:111], v[176:179], v[44:47], v[96:111]\n"
"ds_read_b64_tr_b16 v[160:161], v220 offset:0\n"
"ds_read_b64_tr_b16 v[162:163], v220 offset:1024\n"
"ds_read_b128 v[32:35], v215 offset:49152\n"
"v_mfma_f32_32x32x16_bf16 v[112:127], v[180:183], v[44:47], v[112:127]\n"
"ds_read_b64_tr_b16 v[164:165], v220 offset:256\n"
"ds_read_b64_tr_b16 v[166:167], v220 offset:1280\n"
"ds_read_b128 v[36:39], v216 offset:49152\n"
"v_mfma_f32_32x32x16_bf16 v[128:143], v[184:187], v[44:47], v[128:143]\n"
"ds_read_b64_tr_b16 v[168:169], v220 offset:512\n"
"ds_read_b64_tr_b16 v[170:171], v220 offset:1536\n"
"ds_read_b128 v[40:43], v217 offset:49152\n"
"v_mfma_f32_32x32x16_bf16 v[144:159], v[188:191], v[44:47], v[144:159]\n"
"ds_read_b64_tr_b16 v[172:173], v220 offset:768\n"
"ds_read_b64_tr_b16 v[174:175], v220 offset:1792\n"
"s_waitcnt lgkmcnt(8)\n"
"v_mfma_f32_32x32x16_bf16 v[96:111], v[160:163], v[32:35], v[96:111]\n"
"ds_read_b128 v[44:47], v218 offset:49152\n"
"ds_read_b64_tr_b16 v[176:177], v220 offset:4096\n"
"ds_read_b64_tr_b16 v[178:179], v220 offset:5120\n"
"s_add_u32 m0, s52, 49152\n"
"s_add_u32 s54, s53, 4456448\n"
"buffer_load_dwordx4 v223, s[28:31], s54 offen nt lds\n"
"s_waitcnt lgkmcnt(9)\n"
"v_mfma_f32_32x32x16_bf16 v[112:127], v[164:167], v[32:35], v[112:127]\n"
"ds_read_b64_tr_b16 v[180:181], v220 offset:4352\n"
"ds_read_b64_tr_b16 v[182:183], v220 offset:5376\n"
"buffer_load_dwordx4 v224, s[28:31], s54 offen offset:1024 nt lds\n"
"s_waitcnt lgkmcnt(8)\n"
"v_mfma_f32_32x32x16_bf16 v[128:143], v[168:171], v[32:35], v[128:143]\n"
"ds_read_b64_tr_b16 v[184:185], v220 offset:4608\n"
"ds_read_b64_tr_b16 v[186:187], v220 offset:5632\n"
"s_add_u32 s57, s48, 16384\n"
"buffer_load_dwordx4 v[24:27], v213, s[36:39], s57 offen offset:2048 nt\n"
"s_waitcnt lgkmcnt(7)\n"
"v_mfma_f32_32x32x16_bf16 v[144:159], v[172:175], v[32:35], v[144:159]\n"
"ds_read_b64_tr_b16 v[188:189], v220 offset:4864\n"
"ds_read_b64_tr_b16 v[190:191], v220 offset:5888\n"
"buffer_load_dwordx4 v[28:31], v213, s[36:39], s57 offen offset:3072 nt\n"
"s_waitcnt vmcnt(10)\n"
"s_waitcnt lgkmcnt(0)\n"
"s_barrier\n"
"v_mfma_f32_32x32x16_bf16 v[96:111], v[176:179], v[36:39], v[96:111]\n"
"ds_read_b64_tr_b16 v[160:161], v220 offset:16384\n"
"ds_read_b64_tr_b16 v[162:163], v220 offset:17408\n"
"v_mfma_f32_32x32x16_bf16 v[112:127], v[180:183], v[36:39], v[112:127]\n"
"ds_read_b64_tr_b16 v[164:165], v220 offset:16640\n"
"ds_read_b64_tr_b16 v[166:167], v220 offset:17664\n"
"v_mfma_f32_32x32x16_bf16 v[128:143], v[184:187], v[36:39], v[128:143]\n"
"ds_read_b64_tr_b16 v[168:169], v220 offset:16896\n"
"ds_read_b64_tr_b16 v[170:171], v220 offset:17920\n"
"v_mfma_f32_32x32x16_bf16 v[144:159], v[188:191], v[36:39], v[144:159]\n"
"ds_read_b64_tr_b16 v[172:173], v220 offset:17152\n"
"ds_read_b64_tr_b16 v[174:175], v220 offset:18176\n"
"s_waitcnt lgkmcnt(6)\n"
"v_mfma_f32_32x32x16_bf16 v[96:111], v[160:163], v[40:43], v[96:111]\n"
"ds_read_b64_tr_b16 v[176:177], v220 offset:20480\n"
"ds_read_b64_tr_b16 v[178:179], v220 offset:21504\n"
"s_waitcnt lgkmcnt(6)\n"
"v_mfma_f32_32x32x16_bf16 v[112:127], v[164:167], v[40:43], v[112:127]\n"
"ds_read_b64_tr_b16 v[180:181], v220 offset:20736\n"
"ds_read_b64_tr_b16 v[182:183], v220 offset:21760\n"
"s_waitcnt lgkmcnt(6)\n"
"v_mfma_f32_32x32x16_bf16 v[128:143], v[168:171], v[40:43], v[128:143]\n"
"ds_read_b64_tr_b16 v[184:185], v220 offset:20992\n"
"ds_read_b64_tr_b16 v[186:187], v220 offset:22016\n"
"s_waitcnt lgkmcnt(6)\n"
"v_mfma_f32_32x32x16_bf16 v[144:159], v[172:175], v[40:43], v[144:159]\n"
"ds_read_b64_tr_b16 v[188:189], v220 offset:21248\n"
"ds_read_b64_tr_b16 v[190:191], v220 offset:22272\n"
"s_waitcnt vmcnt(4)\n"
"s_waitcnt lgkmcnt(0)\n"
"s_barrier\n"
"v_mfma_f32_32x32x16_bf16 v[96:111], v[176:179], v[44:47], v[96:111]\n"
"ds_read_b64_tr_b16 v[160:161], v219 offset:0\n"
"ds_read_b64_tr_b16 v[162:163], v219 offset:1024\n"
"ds_read_b128 v[32:35], v215 offset:0\n"
"v_mfma_f32_32x32x16_bf16 v[112:127], v[180:183], v[44:47], v[112:127]\n"
"ds_read_b64_tr_b16 v[164:165], v219 offset:256\n"
"ds_read_b64_tr_b16 v[166:167], v219 offset:1280\n"
"ds_read_b128 v[36:39], v216 offset:0\n"
"v_mfma_f32_32x32x16_bf16 v[128:143], v[184:187], v[44:47], v[128:143]\n"
"ds_read_b64_tr_b16 v[168:169], v219 offset:512\n"
"ds_read_b64_tr_b16 v[170:171], v219 offset:1536\n"
"ds_read_b128 v[40:43], v217 offset:0\n"
"v_mfma_f32_32x32x16_bf16 v[144:159], v[188:191], v[44:47], v[144:159]\n"
"ds_read_b64_tr_b16 v[172:173], v219 offset:768\n"
"ds_read_b64_tr_b16 v[174:175], v219 offset:1792\n"
"s_waitcnt lgkmcnt(8)\n"
"v_mfma_f32_32x32x16_bf16 v[96:111], v[160:163], v[32:35], v[96:111]\n"
"ds_read_b128 v[44:47], v218 offset:0\n"
"ds_read_b64_tr_b16 v[176:177], v219 offset:4096\n"
"ds_read_b64_tr_b16 v[178:179], v219 offset:5120\n"
"s_waitcnt lgkmcnt(9)\n"
"v_mfma_f32_32x32x16_bf16 v[112:127], v[164:167], v[32:35], v[112:127]\n"
"ds_read_b64_tr_b16 v[180:181], v219 offset:4352\n"
"ds_read_b64_tr_b16 v[182:183], v219 offset:5376\n"
"s_waitcnt lgkmcnt(8)\n"
"v_mfma_f32_32x32x16_bf16 v[128:143], v[168:171], v[32:35], v[128:143]\n"
"ds_read_b64_tr_b16 v[184:185], v219 offset:4608\n"
"ds_read_b64_tr_b16 v[186:187], v219 offset:5632\n"
"s_waitcnt lgkmcnt(7)\n"
"v_mfma_f32_32x32x16_bf16 v[144:159], v[172:175], v[32:35], v[144:159]\n"
"ds_read_b64_tr_b16 v[188:189], v219 offset:4864\n"
"ds_read_b64_tr_b16 v[190:191], v219 offset:5888\n"
"s_waitcnt vmcnt(2)\n"
"s_waitcnt lgkmcnt(0)\n"
"s_barrier\n"
"v_mfma_f32_32x32x16_bf16 v[96:111], v[176:179], v[36:39], v[96:111]\n"
"ds_read_b64_tr_b16 v[160:161], v219 offset:16384\n"
"ds_read_b64_tr_b16 v[162:163], v219 offset:17408\n"
"ds_write_b128 v214, v[0:3] offset:0\n"
"s_add_u32 m0, s49, 65536\n"
"s_add_u32 s56, s48, 32768\n"
"buffer_load_dwordx4 v213, s[36:39], s56 offen nt lds\n"
"v_mfma_f32_32x32x16_bf16 v[112:127], v[180:183], v[36:39], v[112:127]\n"
"ds_read_b64_tr_b16 v[164:165], v219 offset:16640\n"
"ds_read_b64_tr_b16 v[166:167], v219 offset:17664\n"
"ds_write_b128 v214, v[4:7] offset:1024\n"
"buffer_load_dwordx4 v213, s[36:39], s56 offen offset:1024 nt lds\n"
"v_mfma_f32_32x32x16_bf16 v[128:143], v[184:187], v[36:39], v[128:143]\n"
"ds_read_b64_tr_b16 v[168:169], v219 offset:16896\n"
"ds_read_b64_tr_b16 v[170:171], v219 offset:17920\n"
"ds_write_b128 v214, v[8:11] offset:2048\n"
"buffer_load_dwordx4 v213, s[36:39], s56 offen offset:2048 nt lds\n"
"v_mfma_f32_32x32x16_bf16 v[144:159], v[188:191], v[36:39], v[144:159]\n"
"ds_read_b64_tr_b16 v[172:173], v219 offset:17152\n"
"ds_read_b64_tr_b16 v[174:175], v219 offset:18176\n"
"ds_write_b128 v214, v[12:15] offset:3072\n"
"buffer_load_dwordx4 v213, s[36:39], s56 offen offset:3072 nt lds\n"
"s_waitcnt lgkmcnt(10)\n"
"v_mfma_f32_32x32x16_bf16 v[96:111], v[160:163], v[40:43], v[96:111]\n"
"ds_read_b64_tr_b16 v[176:177], v219 offset:20480\n"
"ds_read_b64_tr_b16 v[178:179], v219 offset:21504\n"
"s_waitcnt lgkmcnt(9)\n"
"v_mfma_f32_32x32x16_bf16 v[112:127], v[164:167], v[40:43], v[112:127]\n"
"ds_read_b64_tr_b16 v[180:181], v219 offset:20736\n"
"ds_read_b64_tr_b16 v[182:183], v219 offset:21760\n"
"s_waitcnt lgkmcnt(8)\n"
"v_mfma_f32_32x32x16_bf16 v[128:143], v[168:171], v[40:43], v[128:143]\n"
"ds_read_b64_tr_b16 v[184:185], v219 offset:20992\n"
"ds_read_b64_tr_b16 v[186:187], v219 offset:22016\n"
"s_waitcnt lgkmcnt(7)\n"
"v_mfma_f32_32x32x16_bf16 v[144:159], v[172:175], v[40:43], v[144:159]\n"
"ds_read_b64_tr_b16 v[188:189], v219 offset:21248\n"
"ds_read_b64_tr_b16 v[190:191], v219 offset:22272\n"
"s_waitcnt vmcnt(4)\n"
"s_waitcnt lgkmcnt(0)\n"
"s_barrier\n"
"v_mfma_f32_32x32x16_bf16 v[96:111], v[176:179], v[44:47], v[96:111]\n"
"ds_read_b128 v[160:163], v200 offset:0\n"
"ds_write_b128 v214, v[16:19] offset:16384\n"
"v_mfma_f32_32x32x16_bf16 v[112:127], v[180:183], v[44:47], v[112:127]\n"
"ds_read_b128 v[164:167], v201 offset:0\n"
"ds_write_b128 v214, v[20:23] offset:17408\n"
"v_mfma_f32_32x32x16_bf16 v[128:143], v[184:187], v[44:47], v[128:143]\n"
"ds_read_b128 v[168:171], v202 offset:0\n"
"ds_write_b128 v214, v[24:27] offset:18432\n"
"v_mfma_f32_32x32x16_bf16 v[144:159], v[188:191], v[44:47], v[144:159]\n"
"ds_read_b128 v[172:175], v203 offset:0\n"
"ds_write_b128 v214, v[28:31] offset:19456\n"
"s_nop 0\n"
"v_pk_mul_f32 v[96:97], v[96:97], s[62:63]\n"
"v_pk_mul_f32 v[98:99], v[98:99], s[62:63]\n"
"v_pk_mul_f32 v[100:101], v[100:101], s[62:63]\n"
"v_pk_mul_f32 v[102:103], v[102:103], s[62:63]\n"
"v_pk_mul_f32 v[104:105], v[104:105], s[62:63]\n"
"v_pk_mul_f32 v[106:107], v[106:107], s[62:63]\n"
"v_pk_mul_f32 v[108:109], v[108:109], s[62:63]\n"
"v_pk_mul_f32 v[110:111], v[110:111], s[62:63]\n"
"v_cvt_pk_bf16_f32 v64, v96, v97\n"
"v_cvt_pk_bf16_f32 v65, v98, v99\n"
"v_cvt_pk_bf16_f32 v66, v100, v101\n"
"v_cvt_pk_bf16_f32 v67, v102, v103\n"
"v_cvt_pk_bf16_f32 v68, v104, v105\n"
"v_cvt_pk_bf16_f32 v69, v106, v107\n"
"v_cvt_pk_bf16_f32 v70, v108, v109\n"
"v_cvt_pk_bf16_f32 v71, v110, v111\n"
"v_pk_mul_f32 v[112:113], v[112:113], s[62:63]\n"
"v_pk_mul_f32 v[114:115], v[114:115], s[62:63]\n"
"v_pk_mul_f32 v[116:117], v[116:117], s[62:63]\n"
"v_pk_mul_f32 v[118:119], v[118:119], s[62:63]\n"
"v_pk_mul_f32 v[120:121], v[120:121], s[62:63]\n"
"v_pk_mul_f32 v[122:123], v[122:123], s[62:63]\n"
"v_pk_mul_f32 v[124:125], v[124:125], s[62:63]\n"
"v_pk_mul_f32 v[126:127], v[126:127], s[62:63]\n"
"v_cvt_pk_bf16_f32 v72, v112, v113\n"
"v_cvt_pk_bf16_f32 v73, v114, v115\n"
"v_cvt_pk_bf16_f32 v74, v116, v117\n"
"v_cvt_pk_bf16_f32 v75, v118, v119\n"
"v_cvt_pk_bf16_f32 v76, v120, v121\n"
"v_cvt_pk_bf16_f32 v77, v122, v123\n"
"v_cvt_pk_bf16_f32 v78, v124, v125\n"
"v_cvt_pk_bf16_f32 v79, v126, v127\n"
"v_pk_mul_f32 v[128:129], v[128:129], s[62:63]\n"
"v_pk_mul_f32 v[130:131], v[130:131], s[62:63]\n"
"v_pk_mul_f32 v[132:133], v[132:133], s[62:63]\n"
"v_pk_mul_f32 v[134:135], v[134:135], s[62:63]\n"
"v_pk_mul_f32 v[136:137], v[136:137], s[62:63]\n"
"v_pk_mul_f32 v[138:139], v[138:139], s[62:63]\n"
"v_pk_mul_f32 v[140:141], v[140:141], s[62:63]\n"
"v_pk_mul_f32 v[142:143], v[142:143], s[62:63]\n"
"v_cvt_pk_bf16_f32 v80, v128, v129\n"
"v_cvt_pk_bf16_f32 v81, v130, v131\n"
"v_cvt_pk_bf16_f32 v82, v132, v133\n"
"v_cvt_pk_bf16_f32 v83, v134, v135\n"
"v_cvt_pk_bf16_f32 v84, v136, v137\n"
"v_cvt_pk_bf16_f32 v85, v138, v139\n"
"v_cvt_pk_bf16_f32 v86, v140, v141\n"
"v_cvt_pk_bf16_f32 v87, v142, v143\n"
"v_pk_mul_f32 v[144:145], v[144:145], s[62:63]\n"
"v_pk_mul_f32 v[146:147], v[146:147], s[62:63]\n"
"v_pk_mul_f32 v[148:149], v[148:149], s[62:63]\n"
"v_pk_mul_f32 v[150:151], v[150:151], s[62:63]\n"
"v_pk_mul_f32 v[152:153], v[152:153], s[62:63]\n"
"v_pk_mul_f32 v[154:155], v[154:155], s[62:63]\n"
"v_pk_mul_f32 v[156:157], v[156:157], s[62:63]\n"
"v_pk_mul_f32 v[158:159], v[158:159], s[62:63]\n"
"v_cvt_pk_bf16_f32 v88, v144, v145\n"
"v_cvt_pk_bf16_f32 v89, v146, v147\n"
"v_cvt_pk_bf16_f32 v90, v148, v149\n"
"v_cvt_pk_bf16_f32 v91, v150, v151\n"
"v_cvt_pk_bf16_f32 v92, v152, v153\n"
"v_cvt_pk_bf16_f32 v93, v154, v155\n"
"v_cvt_pk_bf16_f32 v94, v156, v157\n"
"v_cvt_pk_bf16_f32 v95, v158, v159\n"
"s_waitcnt lgkmcnt(7)\n"
"v_mfma_f32_32x32x16_bf16 v[96:111], v[160:163], v[64:67], 0\n"
"ds_read_b128 v[160:163], v204 offset:0\n"
"s_waitcnt lgkmcnt(6)\n"
"v_mfma_f32_32x32x16_bf16 v[96:111], v[164:167], v[68:71], v[96:111]\n"
"ds_read_b128 v[164:167], v205 offset:0\n"
"s_waitcnt lgkmcnt(5)\n"
"v_mfma_f32_32x32x16_bf16 v[96:111], v[168:171], v[72:75], v[96:111]\n"
"ds_read_b128 v[168:171], v206 offset:0\n"
"s_waitcnt lgkmcnt(4)\n"
"v_mfma_f32_32x32x16_bf16 v[96:111], v[172:175], v[76:79], v[96:111]\n"
"ds_read_b128 v[172:175], v207 offset:0\n"
"s_waitcnt lgkmcnt(3)\n"
"v_mfma_f32_32x32x16_bf16 v[96:111], v[160:163], v[80:83], v[96:111]\n"
"ds_read_b128 v[160:163], v200 offset:8192\n"
"s_waitcnt lgkmcnt(3)\n"
"v_mfma_f32_32x32x16_bf16 v[96:111], v[164:167], v[84:87], v[96:111]\n"
"ds_read_b128 v[164:167], v201 offset:8192\n"
"s_waitcnt lgkmcnt(3)\n"
"v_mfma_f32_32x32x16_bf16 v[96:111], v[168:171], v[88:91], v[96:111]\n"
"ds_read_b128 v[168:171], v202 offset:8192\n"
"s_waitcnt lgkmcnt(3)\n"
"v_mfma_f32_32x32x16_bf16 v[96:111], v[172:175], v[92:95], v[96:111]\n"
"ds_read_b128 v[172:175], v203 offset:8192\n"
"s_waitcnt lgkmcnt(3)\n"
"v_mfma_f32_32x32x16_bf16 v[112:127], v[160:163], v[64:67], 0\n"
"ds_read_b128 v[160:163], v204 offset:8192\n"
"s_waitcnt lgkmcnt(3)\n"
"v_mfma_f32_32x32x16_bf16 v[112:127], v[164:167], v[68:71], v[112:127]\n"
"ds_read_b128 v[164:167], v205 offset:8192\n"
"s_waitcnt lgkmcnt(3)\n"
"v_mfma_f32_32x32x16_bf16 v[112:127], v[168:171], v[72:75], v[112:127]\n"
"ds_read_b128 v[168:171], v206 offset:8192\n"
"s_waitcnt lgkmcnt(3)\n"
"v_mfma_f32_32x32x16_bf16 v[112:127], v[172:175], v[76:79], v[112:127]\n"
"ds_read_b128 v[172:175], v207 offset:8192\n"
"s_waitcnt lgkmcnt(3)\n"
"v_mfma_f32_32x32x16_bf16 v[112:127], v[160:163], v[80:83], v[112:127]\n"
"s_waitcnt lgkmcnt(0)\n"
"s_barrier\n"
"v_mfma_f32_32x32x16_bf16 v[112:127], v[164:167], v[84:87], v[112:127]\n"
"ds_read_b128 v[160:163], v200 offset:16384\n"
"ds_read_b128 v[164:167], v201 offset:16384\n"
"v_mfma_f32_32x32x16_bf16 v[112:127], v[168:171], v[88:91], v[112:127]\n"
"ds_read_b128 v[168:171], v202 offset:16384\n"
"v_mfma_f32_32x32x16_bf16 v[112:127], v[172:175], v[92:95], v[112:127]\n"
"ds_read_b128 v[172:175], v203 offset:16384\n"
"s_add_u32 m0, s49, 0\n"
"s_add_u32 s56, s48, 32768\n"
"buffer_load_dwordx4 v209, s[32:35], s56 offen nt lds\n"
"buffer_load_dwordx4 v210, s[32:35], s56 offen offset:1024 nt lds\n"
"buffer_load_dwordx4 v211, s[32:35], s56 offen offset:2048 nt lds\n"
"buffer_load_dwordx4 v212, s[32:35], s56 offen offset:3072 nt lds\n"
"v_max3_f32 v226, v96, v97, v98\n"
"v_max3_f32 v227, v99, v100, v101\n"
"v_max3_f32 v226, v226, v102, v103\n"
"v_max3_f32 v227, v227, v104, v105\n"
"v_max3_f32 v226, v226, v106, v107\n"
"v_max3_f32 v227, v227, v108, v109\n"
"v_max3_f32 v226, v226, v110, v111\n"
"v_max3_f32 v226, v226, v112, v113\n"
"v_max3_f32 v227, v227, v114, v115\n"
"v_max3_f32 v226, v226, v116, v117\n"
"v_max3_f32 v227, v227, v118, v119\n"
"v_max3_f32 v226, v226, v120, v121\n"
"v_max3_f32 v227, v227, v122, v123\n"
"v_max3_f32 v226, v226, v124, v125\n"
"v_max3_f32 v227, v227, v126, v127\n"
"v_max3_f32 v226, v226, v227, v227\n"
"v_mov_b32 v227, v226\n"
"s_nop 1\n"
"v_permlane32_swap_b32 v226, v227\n"
"v_max_f32 v226, v226, v227\n"
"v_mov_b32 v225, v226\n"
"v_xor_b32 v230, 0x80000000, v226\n"
"v_sub_f32 v96, v96, v226\n"
"v_sub_f32 v112, v112, v226\n"
"v_mov_b32 v176, v230\n"
"v_sub_f32 v97, v97, v226\n"
"v_sub_f32 v113, v113, v226\n"
"v_mov_b32 v177, v230\n"
"v_sub_f32 v98, v98, v226\n"
"v_sub_f32 v114, v114, v226\n"
"v_mov_b32 v178, v230\n"
"v_sub_f32 v99, v99, v226\n"
"v_sub_f32 v115, v115, v226\n"
"v_mov_b32 v179, v230\n"
"v_sub_f32 v100, v100, v226\n"
"v_sub_f32 v116, v116, v226\n"
"v_mov_b32 v180, v230\n"
"v_sub_f32 v101, v101, v226\n"
"v_sub_f32 v117, v117, v226\n"
"v_mov_b32 v181, v230\n"
"v_sub_f32 v102, v102, v226\n"
"v_sub_f32 v118, v118, v226\n"
"v_mov_b32 v182, v230\n"
"v_sub_f32 v103, v103, v226\n"
"v_sub_f32 v119, v119, v226\n"
"v_mov_b32 v183, v230\n"
"v_sub_f32 v104, v104, v226\n"
"v_sub_f32 v120, v120, v226\n"
"v_mov_b32 v184, v230\n"
"v_sub_f32 v105, v105, v226\n"
"v_sub_f32 v121, v121, v226\n"
"v_mov_b32 v185, v230\n"
"v_sub_f32 v106, v106, v226\n"
"v_sub_f32 v122, v122, v226\n"
"v_mov_b32 v186, v230\n"
"v_sub_f32 v107, v107, v226\n"
"v_sub_f32 v123, v123, v226\n"
"v_mov_b32 v187, v230\n"
"v_sub_f32 v108, v108, v226\n"
"v_sub_f32 v124, v124, v226\n"
"v_mov_b32 v188, v230\n"
"v_sub_f32 v109, v109, v226\n"
"v_sub_f32 v125, v125, v226\n"
"v_mov_b32 v189, v230\n"
"v_sub_f32 v110, v110, v226\n"
"v_sub_f32 v126, v126, v226\n"
"v_mov_b32 v190, v230\n"
"v_sub_f32 v111, v111, v226\n"
"v_sub_f32 v127, v127, v226\n"
"v_mov_b32 v191, v230\n"
"v_exp_f32 v96, v96\n"
"v_exp_f32 v97, v97\n"
"v_exp_f32 v98, v98\n"
"v_exp_f32 v99, v99\n"
"v_exp_f32 v100, v100\n"
"v_exp_f32 v101, v101\n"
"v_exp_f32 v102, v102\n"
"v_exp_f32 v103, v103\n"
"v_exp_f32 v104, v104\n"
"v_exp_f32 v105, v105\n"
"v_exp_f32 v106, v106\n"
"v_exp_f32 v107, v107\n"
"v_exp_f32 v108, v108\n"
"v_exp_f32 v109, v109\n"
"v_exp_f32 v110, v110\n"
"v_exp_f32 v111, v111\n"
"v_cvt_pk_bf16_f32 v144, v96, v97\n"
"v_cvt_pk_bf16_f32 v145, v98, v99\n"
"v_cvt_pk_bf16_f32 v146, v100, v101\n"
"v_cvt_pk_bf16_f32 v147, v102, v103\n"
"v_cvt_pk_bf16_f32 v148, v104, v105\n"
"v_cvt_pk_bf16_f32 v149, v106, v107\n"
"v_cvt_pk_bf16_f32 v150, v108, v109\n"
"v_cvt_pk_bf16_f32 v151, v110, v111\n"
"s_waitcnt lgkmcnt(3)\n"
"v_mfma_f32_32x32x16_bf16 v[96:111], v[160:163], v[64:67], v[176:191]\n"
"ds_read_b128 v[160:163], v204 offset:16384\n"
"v_exp_f32 v112, v112\n"
"s_waitcnt lgkmcnt(3)\n"
"v_mfma_f32_32x32x16_bf16 v[96:111], v[164:167], v[68:71], v[96:111]\n"
"ds_read_b128 v[164:167], v205 offset:16384\n"
"v_exp_f32 v113, v113\n"
"s_waitcnt lgkmcnt(3)\n"
"v_mfma_f32_32x32x16_bf16 v[96:111], v[168:171], v[72:75], v[96:111]\n"
"ds_read_b128 v[168:171], v206 offset:16384\n"
"v_exp_f32 v114, v114\n"
"v_cvt_pk_bf16_f32 v152, v112, v113\n"
"s_waitcnt lgkmcnt(3)\n"
"v_mfma_f32_32x32x16_bf16 v[96:111], v[172:175], v[76:79], v[96:111]\n"
"ds_read_b128 v[172:175], v207 offset:16384\n"
"v_exp_f32 v115, v115\n"
"s_waitcnt lgkmcnt(3)\n"
"v_mfma_f32_32x32x16_bf16 v[96:111], v[160:163], v[80:83], v[96:111]\n"
"ds_read_b128 v[160:163], v200 offset:24576\n"
"v_exp_f32 v116, v116\n"
"v_cvt_pk_bf16_f32 v153, v114, v115\n"
"s_waitcnt lgkmcnt(3)\n"
"v_mfma_f32_32x32x16_bf16 v[96:111], v[164:167], v[84:87], v[96:111]\n"
"ds_read_b128 v[164:167], v201 offset:24576\n"
"v_exp_f32 v117, v117\n"
"s_waitcnt lgkmcnt(3)\n"
"v_mfma_f32_32x32x16_bf16 v[96:111], v[168:171], v[88:91], v[96:111]\n"
"ds_read_b128 v[168:171], v202 offset:24576\n"
"v_exp_f32 v118, v118\n"
"v_cvt_pk_bf16_f32 v154, v116, v117\n"
"s_waitcnt lgkmcnt(3)\n"
"v_mfma_f32_32x32x16_bf16 v[96:111], v[172:175], v[92:95], v[96:111]\n"
"ds_read_b128 v[172:175], v203 offset:24576\n"
"v_exp_f32 v119, v119\n"
"s_waitcnt lgkmcnt(3)\n"
"v_mfma_f32_32x32x16_bf16 v[128:143], v[160:163], v[64:67], v[176:191]\n"
"ds_read_b128 v[160:163], v204 offset:24576\n"
"v_exp_f32 v120, v120\n"
"v_cvt_pk_bf16_f32 v155, v118, v119\n"
"s_waitcnt lgkmcnt(3)\n"
"v_mfma_f32_32x32x16_bf16 v[128:143], v[164:167], v[68:71], v[128:143]\n"
"ds_read_b128 v[164:167], v205 offset:24576\n"
"v_exp_f32 v121, v121\n"
"s_waitcnt lgkmcnt(3)\n"
"v_mfma_f32_32x32x16_bf16 v[128:143], v[168:171], v[72:75], v[128:143]\n"
"ds_read_b128 v[168:171], v206 offset:24576\n"
"v_exp_f32 v122, v122\n"
"v_cvt_pk_bf16_f32 v156, v120, v121\n"
"v_max3_f32 v226, v96, v97, v98\n"
"v_max3_f32 v227, v99, v100, v101\n"
"s_waitcnt lgkmcnt(3)\n"
"v_mfma_f32_32x32x16_bf16 v[128:143], v[172:175], v[76:79], v[128:143]\n"
"ds_read_b128 v[172:175], v207 offset:24576\n"
"v_exp_f32 v123, v123\n"
"v_max3_f32 v226, v226, v102, v103\n"
"v_max3_f32 v227, v227, v104, v105\n"
"s_waitcnt lgkmcnt(3)\n"
"v_mfma_f32_32x32x16_bf16 v[128:143], v[160:163], v[80:83], v[128:143]\n"
"v_exp_f32 v124, v124\n"
"v_cvt_pk_bf16_f32 v157, v122, v123\n"
"s_waitcnt lgkmcnt(0)\n"
"s_barrier\n"
"v_mfma_f32_32x32x16_bf16 v[128:143], v[164:167], v[84:87], v[128:143]\n"
"v_exp_f32 v125, v125\n"
"ds_read_b64_tr_b16 v[160:161], v208 offset:0\n"
"ds_read_b64_tr_b16 v[162:163], v208 offset:2048\n"
"ds_read_b64_tr_b16 v[164:165], v208 offset:256\n"
"ds_read_b64_tr_b16 v[166:167], v208 offset:2304\n"
"v_max3_f32 v226, v226, v106, v107\n"
"v_max3_f32 v227, v227, v108, v109\n"
"v_mfma_f32_32x32x16_bf16 v[128:143], v[168:171], v[88:91], v[128:143]\n"
"v_exp_f32 v126, v126\n"
"v_cvt_pk_bf16_f32 v158, v124, v125\n"
"ds_read_b64_tr_b16 v[168:169], v208 offset:512\n"
"ds_read_b64_tr_b16 v[170:171], v208 offset:2560\n"
"v_max3_f32 v226, v226, v110, v111\n"
"v_mfma_f32_32x32x16_bf16 v[128:143], v[172:175], v[92:95], v[128:143]\n"
"v_exp_f32 v127, v127\n"
"ds_read_b64_tr_b16 v[172:173], v208 offset:768\n"
"ds_read_b64_tr_b16 v[174:175], v208 offset:2816\n"
"s_waitcnt lgkmcnt(6)\n"
"v_mfma_f32_32x32x16_bf16 v[0:15], v[160:163], v[144:147], 0\n"
"ds_read_b64_tr_b16 v[160:161], v208 offset:4096\n"
"ds_read_b64_tr_b16 v[162:163], v208 offset:6144\n"
"v_cvt_pk_bf16_f32 v159, v126, v127\n"
"s_add_u32 m0, s49, 16384\n"
"s_add_u32 s56, s48, 49152\n"
"buffer_load_dwordx4 v209, s[32:35], s56 offen nt lds\n"
"s_waitcnt lgkmcnt(6)\n"
"v_mfma_f32_32x32x16_bf16 v[16:31], v[164:167], v[144:147], 0\n"
"ds_read_b64_tr_b16 v[164:165], v208 offset:4352\n"
"ds_read_b64_tr_b16 v[166:167], v208 offset:6400\n"
"v_mfma_f32_16x16x32_bf16 v[192:195], v[196:199], v[144:147], 0\n"
"s_waitcnt lgkmcnt(6)\n"
"v_mfma_f32_32x32x16_bf16 v[32:47], v[168:171], v[144:147], 0\n"
"ds_read_b64_tr_b16 v[168:169], v208 offset:4608\n"
"ds_read_b64_tr_b16 v[170:171], v208 offset:6656\n"
"v_max3_f32 v226, v226, v128, v129\n"
"v_max3_f32 v227, v227, v130, v131\n"
"v_max3_f32 v226, v226, v132, v133\n"
"v_max3_f32 v227, v227, v134, v135\n"
"s_waitcnt lgkmcnt(6)\n"
"v_mfma_f32_32x32x16_bf16 v[48:63], v[172:175], v[144:147], 0\n"
"ds_read_b64_tr_b16 v[172:173], v208 offset:4864\n"
"ds_read_b64_tr_b16 v[174:175], v208 offset:6912\n"
"v_max3_f32 v226, v226, v136, v137\n"
"v_max3_f32 v227, v227, v138, v139\n"
"v_max3_f32 v226, v226, v140, v141\n"
"v_max3_f32 v227, v227, v142, v143\n"
"v_max3_f32 v226, v226, v227, v227\n"
"v_mov_b32 v227, v226\n"
"s_nop 1\n"
"v_permlane32_swap_b32 v226, v227\n"
"v_max_f32 v226, v226, v227\n"
"s_mov_b32 s60, 0\n"
"v_cmp_lt_f32 vcc, s64, v226\n"
"s_cbranch_vccnz KF_RA1\n"
"KF_RB1:\n"
"s_waitcnt lgkmcnt(6)\n"
"v_mfma_f32_32x32x16_bf16 v[0:15], v[160:163], v[148:151], v[0:15]\n"
"ds_read_b64_tr_b16 v[160:161], v208 offset:8192\n"
"ds_read_b64_tr_b16 v[162:163], v208 offset:10240\n"
"v_exp_f32 v96, v96\n"
"v_exp_f32 v97, v97\n"
"s_waitcnt lgkmcnt(6)\n"
"v_mfma_f32_32x32x16_bf16 v[16:31], v[164:167], v[148:151], v[16:31]\n"
"ds_read_b64_tr_b16 v[164:165], v208 offset:8448\n"
"ds_read_b64_tr_b16 v[166:167], v208 offset:10496\n"
"v_mfma_f32_16x16x32_bf16 v[192:195], v[196:199], v[148:151], v[192:195]\n"
"v_exp_f32 v98, v98\n"
"v_cvt_pk_bf16_f32 v144, v96, v97\n"
"s_waitcnt lgkmcnt(6)\n"
"v_mfma_f32_32x32x16_bf16 v[32:47], v[168:171], v[148:151], v[32:47]\n"
"ds_read_b64_tr_b16 v[168:169], v208 offset:8704\n"
"ds_read_b64_tr_b16 v[170:171], v208 offset:10752\n"
"buffer_load_dwordx4 v210, s[32:35], s56 offen offset:1024 nt lds\n"
"v_exp_f32 v99, v99\n"
"s_waitcnt lgkmcnt(6)\n"
"v_mfma_f32_32x32x16_bf16 v[48:63], v[172:175], v[148:151], v[48:63]\n"
"ds_read_b64_tr_b16 v[172:173], v208 offset:8960\n"
"ds_read_b64_tr_b16 v[174:175], v208 offset:11008\n"
"v_exp_f32 v100, v100\n"
"v_exp_f32 v101, v101\n"
"v_cvt_pk_bf16_f32 v145, v98, v99\n"
"s_waitcnt lgkmcnt(6)\n"
"v_mfma_f32_32x32x16_bf16 v[0:15], v[160:163], v[152:155], v[0:15]\n"
"ds_read_b64_tr_b16 v[160:161], v208 offset:12288\n"
"ds_read_b64_tr_b16 v[162:163], v208 offset:14336\n"
"buffer_load_dwordx4 v211, s[32:35], s56 offen offset:2048 nt lds\n"
"v_exp_f32 v102, v102\n"
"v_cvt_pk_bf16_f32 v146, v100, v101\n"
"s_waitcnt lgkmcnt(6)\n"
"v_mfma_f32_32x32x16_bf16 v[16:31], v[164:167], v[152:155], v[16:31]\n"
"ds_read_b64_tr_b16 v[164:165], v208 offset:12544\n"
"ds_read_b64_tr_b16 v[166:167], v208 offset:14592\n"
"v_mfma_f32_16x16x32_bf16 v[192:195], v[196:199], v[152:155], v[192:195]\n"
"v_exp_f32 v103, v103\n"
"s_waitcnt lgkmcnt(6)\n"
"v_mfma_f32_32x32x16_bf16 v[32:47], v[168:171], v[152:155], v[32:47]\n"
"ds_read_b64_tr_b16 v[168:169], v208 offset:12800\n"
"ds_read_b64_tr_b16 v[170:171], v208 offset:14848\n"
"v_exp_f32 v104, v104\n"
"v_exp_f32 v105, v105\n"
"v_cvt_pk_bf16_f32 v147, v102, v103\n"
"s_waitcnt lgkmcnt(6)\n"
"v_mfma_f32_32x32x16_bf16 v[48:63], v[172:175], v[152:155], v[48:63]\n"
"ds_read_b64_tr_b16 v[172:173], v208 offset:13056\n"
"ds_read_b64_tr_b16 v[174:175], v208 offset:15104\n"
"buffer_load_dwordx4 v212, s[32:35], s56 offen offset:3072 nt lds\n"
"v_exp_f32 v106, v106\n"
"v_cvt_pk_bf16_f32 v148, v104, v105\n"
"s_waitcnt lgkmcnt(6)\n"
"v_mfma_f32_32x32x16_bf16 v[0:15], v[160:163], v[156:159], v[0:15]\n"
"v_exp_f32 v107, v107\n"
"s_waitcnt vmcnt(4)\n"
"s_waitcnt lgkmcnt(0)\n"
"s_barrier\n"
"v_mfma_f32_32x32x16_bf16 v[16:31], v[164:167], v[156:159], v[16:31]\n"
"v_mfma_f32_16x16x32_bf16 v[192:195], v[196:199], v[156:159], v[192:195]\n"
"v_exp_f32 v108, v108\n"
"v_exp_f32 v109, v109\n"
"v_cvt_pk_bf16_f32 v149, v106, v107\n"
"ds_read_b128 v[160:163], v200 offset:0\n"
"ds_read_b128 v[164:167], v201 offset:0\n"
"v_mfma_f32_32x32x16_bf16 v[32:47], v[168:171], v[156:159], v[32:47]\n"
"v_exp_f32 v110, v110\n"
"v_cvt_pk_bf16_f32 v150, v108, v109\n"
"ds_read_b128 v[168:171], v202 offset:0\n"
"v_mfma_f32_32x32x16_bf16 v[48:63], v[172:175], v[156:159], v[48:63]\n"
"v_exp_f32 v111, v111\n"
"ds_read_b128 v[172:175], v203 offset:0\n"
"s_cmp_lg_u32 s60, 0\n"
"s_cbranch_scc1 KF_RC1\n"
"KF_RD1:\n"
"v_cvt_pk_bf16_f32 v151, v110, v111\n"
"s_waitcnt lgkmcnt(3)\n"
"v_mfma_f32_32x32x16_bf16 v[96:111], v[160:163], v[64:67], v[176:191]\n"
"ds_read_b128 v[160:163], v204 offset:0\n"
"v_exp_f32 v128, v128\n"
"s_waitcnt lgkmcnt(3)\n"
"v_mfma_f32_32x32x16_bf16 v[96:111], v[164:167], v[68:71], v[96:111]\n"
"ds_read_b128 v[164:167], v205 offset:0\n"
"v_exp_f32 v129, v129\n"
"s_add_u32 m0, s49, 32768\n"
"s_add_u32 s56, s48, 49152\n"
"buffer_load_dwordx4 v213, s[36:39], s56 offen nt lds\n"
"s_waitcnt lgkmcnt(3)\n"
"v_mfma_f32_32x32x16_bf16 v[96:111], v[168:171], v[72:75], v[96:111]\n"
"ds_read_b128 v[168:171], v206 offset:0\n"
"v_exp_f32 v130, v130\n"
"v_cvt_pk_bf16_f32 v152, v128, v129\n"
"s_waitcnt lgkmcnt(3)\n"
"v_mfma_f32_32x32x16_bf16 v[96:111], v[172:175], v[76:79], v[96:111]\n"
"ds_read_b128 v[172:175], v207 offset:0\n"
"v_exp_f32 v131, v131\n"
"s_waitcnt lgkmcnt(3)\n"
"v_mfma_f32_32x32x16_bf16 v[96:111], v[160:163], v[80:83], v[96:111]\n"
"ds_read_b128 v[160:163], v200 offset:8192\n"
"v_exp_f32 v132, v132\n"
"v_cvt_pk_bf16_f32 v153, v130, v131\n"
"buffer_load_dwordx4 v213, s[36:39], s56 offen offset:1024 nt lds\n"
"s_waitcnt lgkmcnt(3)\n"
"v_mfma_f32_32x32x16_bf16 v[96:111], v[164:167], v[84:87], v[96:111]\n"
"ds_read_b128 v[164:167], v201 offset:8192\n"
"v_exp_f32 v133, v133\n"
"s_waitcnt lgkmcnt(3)\n"
"v_mfma_f32_32x32x16_bf16 v[96:111], v[168:171], v[88:91], v[96:111]\n"
"ds_read_b128 v[168:171], v202 offset:8192\n"
"v_exp_f32 v134, v134\n"
"v_cvt_pk_bf16_f32 v154, v132, v133\n"
"s_waitcnt lgkmcnt(3)\n"
"v_mfma_f32_32x32x16_bf16 v[96:111], v[172:175], v[92:95], v[96:111]\n"
"ds_read_b128 v[172:175], v203 offset:8192\n"
"v_exp_f32 v135, v135\n"
"buffer_load_dwordx4 v213, s[36:39], s56 offen offset:2048 nt lds\n"
"s_waitcnt lgkmcnt(3)\n"
"v_mfma_f32_32x32x16_bf16 v[112:127], v[160:163], v[64:67], v[176:191]\n"
"ds_read_b128 v[160:163], v204 offset:8192\n"
"v_exp_f32 v136, v136\n"
"v_cvt_pk_bf16_f32 v155, v134, v135\n"
"s_waitcnt lgkmcnt(3)\n"
"v_mfma_f32_32x32x16_bf16 v[112:127], v[164:167], v[68:71], v[112:127]\n"
"ds_read_b128 v[164:167], v205 offset:8192\n"
"v_exp_f32 v137, v137\n"
"buffer_load_dwordx4 v213, s[36:39], s56 offen offset:3072 nt lds\n"
"s_waitcnt lgkmcnt(3)\n"
"v_mfma_f32_32x32x16_bf16 v[112:127], v[168:171], v[72:75], v[112:127]\n"
"ds_read_b128 v[168:171], v206 offset:8192\n"
"v_exp_f32 v138, v138\n"
"v_cvt_pk_bf16_f32 v156, v136, v137\n"
"v_max3_f32 v226, v96, v97, v98\n"
"v_max3_f32 v227, v99, v100, v101\n"
"s_waitcnt lgkmcnt(3)\n"
"v_mfma_f32_32x32x16_bf16 v[112:127], v[172:175], v[76:79], v[112:127]\n"
"ds_read_b128 v[172:175], v207 offset:8192\n"
"v_exp_f32 v139, v139\n"
"v_max3_f32 v226, v226, v102, v103\n"
"v_max3_f32 v227, v227, v104, v105\n"
"s_waitcnt lgkmcnt(3)\n"
"v_mfma_f32_32x32x16_bf16 v[112:127], v[160:163], v[80:83], v[112:127]\n"
"v_exp_f32 v140, v140\n"
"v_cvt_pk_bf16_f32 v157, v138, v139\n"
"s_waitcnt lgkmcnt(0)\n"
"s_barrier\n"
"v_mfma_f32_32x32x16_bf16 v[112:127], v[164:167], v[84:87], v[112:127]\n"
"v_exp_f32 v141, v141\n"
"ds_read_b64_tr_b16 v[160:161], v208 offset:16384\n"
"ds_read_b64_tr_b16 v[162:163], v208 offset:18432\n"
"ds_read_b64_tr_b16 v[164:165], v208 offset:16640\n"
"ds_read_b64_tr_b16 v[166:167], v208 offset:18688\n"
"v_max3_f32 v226, v226, v106, v107\n"
"v_max3_f32 v227, v227, v108, v109\n"
"v_mfma_f32_32x32x16_bf16 v[112:127], v[168:171], v[88:91], v[112:127]\n"
"v_exp_f32 v142, v142\n"
"v_cvt_pk_bf16_f32 v158, v140, v141\n"
"ds_read_b64_tr_b16 v[168:169], v208 offset:16896\n"
"ds_read_b64_tr_b16 v[170:171], v208 offset:18944\n"
"v_max3_f32 v226, v226, v110, v111\n"
"v_mfma_f32_32x32x16_bf16 v[112:127], v[172:175], v[92:95], v[112:127]\n"
"v_exp_f32 v143, v143\n"
"ds_read_b64_tr_b16 v[172:173], v208 offset:17152\n"
"ds_read_b64_tr_b16 v[174:175], v208 offset:19200\n"
"s_waitcnt lgkmcnt(6)\n"
"v_mfma_f32_32x32x16_bf16 v[0:15], v[160:163], v[144:147], v[0:15]\n"
"ds_read_b64_tr_b16 v[160:161], v208 offset:20480\n"
"ds_read_b64_tr_b16 v[162:163], v208 offset:22528\n"
"v_cvt_pk_bf16_f32 v159, v142, v143\n"
"s_add_u32 m0, s49, 0\n"
"s_add_u32 s56, s48, 65536\n"
"buffer_load_dwordx4 v209, s[32:35], s56 offen nt lds\n"
"s_waitcnt lgkmcnt(6)\n"
"v_mfma_f32_32x32x16_bf16 v[16:31], v[164:167], v[144:147], v[16:31]\n"
"ds_read_b64_tr_b16 v[164:165], v208 offset:20736\n"
"ds_read_b64_tr_b16 v[166:167], v208 offset:22784\n"
"v_mfma_f32_16x16x32_bf16 v[192:195], v[196:199], v[144:147], v[192:195]\n"
"s_waitcnt lgkmcnt(6)\n"
"v_mfma_f32_32x32x16_bf16 v[32:47], v[168:171], v[144:147], v[32:47]\n"
"ds_read_b64_tr_b16 v[168:169], v208 offset:20992\n"
"ds_read_b64_tr_b16 v[170:171], v208 offset:23040\n"
"v_max3_f32 v226, v226, v112, v113\n"
"v_max3_f32 v227, v227, v114, v115\n"
"v_max3_f32 v226, v226, v116, v117\n"
"v_max3_f32 v227, v227, v118, v119\n"
"s_waitcnt lgkmcnt(6)\n"
"v_mfma_f32_32x32x16_bf16 v[48:63], v[172:175], v[144:147], v[48:63]\n"
"ds_read_b64_tr_b16 v[172:173], v208 offset:21248\n"
"ds_read_b64_tr_b16 v[174:175], v208 offset:23296\n"
"v_max3_f32 v226, v226, v120, v121\n"
"v_max3_f32 v227, v227, v122, v123\n"
"v_max3_f32 v226, v226, v124, v125\n"
"v_max3_f32 v227, v227, v126, v127\n"
"v_max3_f32 v226, v226, v227, v227\n"
"v_mov_b32 v227, v226\n"
"s_nop 1\n"
"v_permlane32_swap_b32 v226, v227\n"
"v_max_f32 v226, v226, v227\n"
"s_mov_b32 s60, 0\n"
"v_cmp_lt_f32 vcc, s64, v226\n"
"s_cbranch_vccnz KF_RA2\n"
"KF_RB2:\n"
"s_waitcnt lgkmcnt(6)\n"
"v_mfma_f32_32x32x16_bf16 v[0:15], v[160:163], v[148:151], v[0:15]\n"
"ds_read_b64_tr_b16 v[160:161], v208 offset:24576\n"
"ds_read_b64_tr_b16 v[162:163], v208 offset:26624\n"
"v_exp_f32 v96, v96\n"
"v_exp_f32 v97, v97\n"
"s_waitcnt lgkmcnt(6)\n"
"v_mfma_f32_32x32x16_bf16 v[16:31], v[164:167], v[148:151], v[16:31]\n"
"ds_read_b64_tr_b16 v[164:165], v208 offset:24832\n"
"ds_read_b64_tr_b16 v[166:167], v208 offset:26880\n"
"v_mfma_f32_16x16x32_bf16 v[192:195], v[196:199], v[148:151], v[192:195]\n"
"v_exp_f32 v98, v98\n"
"v_cvt_pk_bf16_f32 v144, v96, v97\n"
"s_waitcnt lgkmcnt(6)\n"
"v_mfma_f32_32x32x16_bf16 v[32:47], v[168:171], v[148:151], v[32:47]\n"
"ds_read_b64_tr_b16 v[168:169], v208 offset:25088\n"
"ds_read_b64_tr_b16 v[170:171], v208 offset:27136\n"
"buffer_load_dwordx4 v210, s[32:35], s56 offen offset:1024 nt lds\n"
"v_exp_f32 v99, v99\n"
"s_waitcnt lgkmcnt(6)\n"
"v_mfma_f32_32x32x16_bf16 v[48:63], v[172:175], v[148:151], v[48:63]\n"
"ds_read_b64_tr_b16 v[172:173], v208 offset:25344\n"
"ds_read_b64_tr_b16 v[174:175], v208 offset:27392\n"
"v_exp_f32 v100, v100\n"
"v_exp_f32 v101, v101\n"
"v_cvt_pk_bf16_f32 v145, v98, v99\n"
"s_waitcnt lgkmcnt(6)\n"
"v_mfma_f32_32x32x16_bf16 v[0:15], v[160:163], v[152:155], v[0:15]\n"
"ds_read_b64_tr_b16 v[160:161], v208 offset:28672\n"
"ds_read_b64_tr_b16 v[162:163], v208 offset:30720\n"
"buffer_load_dwordx4 v211, s[32:35], s56 offen offset:2048 nt lds\n"
"v_exp_f32 v102, v102\n"
"v_cvt_pk_bf16_f32 v146, v100, v101\n"
"s_waitcnt lgkmcnt(6)\n"
"v_mfma_f32_32x32x16_bf16 v[16:31], v[164:167], v[152:155], v[16:31]\n"
"ds_read_b64_tr_b16 v[164:165], v208 offset:28928\n"
"ds_read_b64_tr_b16 v[166:167], v208 offset:30976\n"
"v_mfma_f32_16x16x32_bf16 v[192:195], v[196:199], v[152:155], v[192:195]\n"
"v_exp_f32 v103, v103\n"
"s_waitcnt lgkmcnt(6)\n"
"v_mfma_f32_32x32x16_bf16 v[32:47], v[168:171], v[152:155], v[32:47]\n"
"ds_read_b64_tr_b16 v[168:169], v208 offset:29184\n"
"ds_read_b64_tr_b16 v[170:171], v208 offset:31232\n"
"v_exp_f32 v104, v104\n"
"v_exp_f32 v105, v105\n"
"v_cvt_pk_bf16_f32 v147, v102, v103\n"
"s_waitcnt lgkmcnt(6)\n"
"v_mfma_f32_32x32x16_bf16 v[48:63], v[172:175], v[152:155], v[48:63]\n"
"ds_read_b64_tr_b16 v[172:173], v208 offset:29440\n"
"ds_read_b64_tr_b16 v[174:175], v208 offset:31488\n"
"buffer_load_dwordx4 v212, s[32:35], s56 offen offset:3072 nt lds\n"
"v_exp_f32 v106, v106\n"
"v_cvt_pk_bf16_f32 v148, v104, v105\n"
"s_waitcnt lgkmcnt(6)\n"
"v_mfma_f32_32x32x16_bf16 v[0:15], v[160:163], v[156:159], v[0:15]\n"
"v_exp_f32 v107, v107\n"
"s_waitcnt vmcnt(8)\n"
"s_waitcnt lgkmcnt(0)\n"
"s_barrier\n"
"v_mfma_f32_32x32x16_bf16 v[16:31], v[164:167], v[156:159], v[16:31]\n"
"v_mfma_f32_16x16x32_bf16 v[192:195], v[196:199], v[156:159], v[192:195]\n"
"v_exp_f32 v108, v108\n"
"v_exp_f32 v109, v109\n"
"v_cvt_pk_bf16_f32 v149, v106, v107\n"
"ds_read_b128 v[160:163], v200 offset:16384\n"
"ds_read_b128 v[164:167], v201 offset:16384\n"
"v_mfma_f32_32x32x16_bf16 v[32:47], v[168:171], v[156:159], v[32:47]\n"
"v_exp_f32 v110, v110\n"
"v_cvt_pk_bf16_f32 v150, v108, v109\n"
"ds_read_b128 v[168:171], v202 offset:16384\n"
"v_mfma_f32_32x32x16_bf16 v[48:63], v[172:175], v[156:159], v[48:63]\n"
"v_exp_f32 v111, v111\n"
"ds_read_b128 v[172:175], v203 offset:16384\n"
"s_cmp_lg_u32 s60, 0\n"
"s_cbranch_scc1 KF_RC2\n"
"KF_RD2:\n"
"v_cvt_pk_bf16_f32 v151, v110, v111\n"
"s_waitcnt lgkmcnt(3)\n"
"v_mfma_f32_32x32x16_bf16 v[96:111], v[160:163], v[64:67], v[176:191]\n"
"ds_read_b128 v[160:163], v204 offset:16384\n"
"v_exp_f32 v112, v112\n"
"s_waitcnt lgkmcnt(3)\n"
"v_mfma_f32_32x32x16_bf16 v[96:111], v[164:167], v[68:71], v[96:111]\n"
"ds_read_b128 v[164:167], v205 offset:16384\n"
"v_exp_f32 v113, v113\n"
"s_add_u32 m0, s49, 49152\n"
"s_add_u32 s56, s48, 65536\n"
"buffer_load_dwordx4 v213, s[36:39], s56 offen nt lds\n"
"s_waitcnt lgkmcnt(3)\n"
"v_mfma_f32_32x32x16_bf16 v[96:111], v[168:171], v[72:75], v[96:111]\n"
"ds_read_b128 v[168:171], v206 offset:16384\n"
"v_exp_f32 v114, v114\n"
"v_cvt_pk_bf16_f32 v152, v112, v113\n"
"s_waitcnt lgkmcnt(3)\n"
"v_mfma_f32_32x32x16_bf16 v[96:111], v[172:175], v[76:79], v[96:111]\n"
"ds_read_b128 v[172:175], v207 offset:16384\n"
"v_exp_f32 v115, v115\n"
"s_waitcnt lgkmcnt(3)\n"
"v_mfma_f32_32x32x16_bf16 v[96:111], v[160:163], v[80:83], v[96:111]\n"
"ds_read_b128 v[160:163], v200 offset:24576\n"
"v_exp_f32 v116, v116\n"
"v_cvt_pk_bf16_f32 v153, v114, v115\n"
"buffer_load_dwordx4 v213, s[36:39], s56 offen offset:1024 nt lds\n"
"s_waitcnt lgkmcnt(3)\n"
"v_mfma_f32_32x32x16_bf16 v[96:111], v[164:167], v[84:87], v[96:111]\n"
"ds_read_b128 v[164:167], v201 offset:24576\n"
"v_exp_f32 v117, v117\n"
"s_waitcnt lgkmcnt(3)\n"
"v_mfma_f32_32x32x16_bf16 v[96:111], v[168:171], v[88:91], v[96:111]\n"
"ds_read_b128 v[168:171], v202 offset:24576\n"
"v_exp_f32 v118, v118\n"
"v_cvt_pk_bf16_f32 v154, v116, v117\n"
"s_waitcnt lgkmcnt(3)\n"
"v_mfma_f32_32x32x16_bf16 v[96:111], v[172:175], v[92:95], v[96:111]\n"
"ds_read_b128 v[172:175], v203 offset:24576\n"
"v_exp_f32 v119, v119\n"
"buffer_load_dwordx4 v213, s[36:39], s56 offen offset:2048 nt lds\n"
"s_waitcnt lgkmcnt(3)\n"
"v_mfma_f32_32x32x16_bf16 v[128:143], v[160:163], v[64:67], v[176:191]\n"
"ds_read_b128 v[160:163], v204 offset:24576\n"
"v_exp_f32 v120, v120\n"
"v_cvt_pk_bf16_f32 v155, v118, v119\n"
"s_waitcnt lgkmcnt(3)\n"
"v_mfma_f32_32x32x16_bf16 v[128:143], v[164:167], v[68:71], v[128:143]\n"
"ds_read_b128 v[164:167], v205 offset:24576\n"
"v_exp_f32 v121, v121\n"
"buffer_load_dwordx4 v213, s[36:39], s56 offen offset:3072 nt lds\n"
"s_waitcnt lgkmcnt(3)\n"
"v_mfma_f32_32x32x16_bf16 v[128:143], v[168:171], v[72:75], v[128:143]\n"
"ds_read_b128 v[168:171], v206 offset:24576\n"
"v_exp_f32 v122, v122\n"
"v_cvt_pk_bf16_f32 v156, v120, v121\n"
"v_max3_f32 v226, v96, v97, v98\n"
"v_max3_f32 v227, v99, v100, v101\n"
"s_waitcnt lgkmcnt(3)\n"
"v_mfma_f32_32x32x16_bf16 v[128:143], v[172:175], v[76:79], v[128:143]\n"
"ds_read_b128 v[172:175], v207 offset:24576\n"
"v_exp_f32 v123, v123\n"
"v_max3_f32 v226, v226, v102, v103\n"
"v_max3_f32 v227, v227, v104, v105\n"
"s_waitcnt lgkmcnt(3)\n"
"v_mfma_f32_32x32x16_bf16 v[128:143], v[160:163], v[80:83], v[128:143]\n"
"v_exp_f32 v124, v124\n"
"v_cvt_pk_bf16_f32 v157, v122, v123\n"
"s_waitcnt lgkmcnt(0)\n"
"s_barrier\n"
"v_mfma_f32_32x32x16_bf16 v[128:143], v[164:167], v[84:87], v[128:143]\n"
"v_exp_f32 v125, v125\n"
"ds_read_b64_tr_b16 v[160:161], v208 offset:32768\n"
"ds_read_b64_tr_b16 v[162:163], v208 offset:34816\n"
"ds_read_b64_tr_b16 v[164:165], v208 offset:33024\n"
"ds_read_b64_tr_b16 v[166:167], v208 offset:35072\n"
"v_max3_f32 v226, v226, v106, v107\n"
"v_max3_f32 v227, v227, v108, v109\n"
"v_mfma_f32_32x32x16_bf16 v[128:143], v[168:171], v[88:91], v[128:143]\n"
"v_exp_f32 v126, v126\n"
"v_cvt_pk_bf16_f32 v158, v124, v125\n"
"ds_read_b64_tr_b16 v[168:169], v208 offset:33280\n"
"ds_read_b64_tr_b16 v[170:171], v208 offset:35328\n"
"v_max3_f32 v226, v226, v110, v111\n"
"v_mfma_f32_32x32x16_bf16 v[128:143], v[172:175], v[92:95], v[128:143]\n"
"v_exp_f32 v127, v127\n"
"ds_read_b64_tr_b16 v[172:173], v208 offset:33536\n"
"ds_read_b64_tr_b16 v[174:175], v208 offset:35584\n"
"s_waitcnt lgkmcnt(6)\n"
"v_mfma_f32_32x32x16_bf16 v[0:15], v[160:163], v[144:147], v[0:15]\n"
"ds_read_b64_tr_b16 v[160:161], v208 offset:36864\n"
"ds_read_b64_tr_b16 v[162:163], v208 offset:38912\n"
"v_cvt_pk_bf16_f32 v159, v126, v127\n"
"s_add_u32 m0, s49, 16384\n"
"s_add_u32 s56, s48, 81920\n"
"buffer_load_dwordx4 v209, s[32:35], s56 offen nt lds\n"
"s_waitcnt lgkmcnt(6)\n"
"v_mfma_f32_32x32x16_bf16 v[16:31], v[164:167], v[144:147], v[16:31]\n"
"ds_read_b64_tr_b16 v[164:165], v208 offset:37120\n"
"ds_read_b64_tr_b16 v[166:167], v208 offset:39168\n"
"v_mfma_f32_16x16x32_bf16 v[192:195], v[196:199], v[144:147], v[192:195]\n"
"s_waitcnt lgkmcnt(6)\n"
"v_mfma_f32_32x32x16_bf16 v[32:47], v[168:171], v[144:147], v[32:47]\n"
"ds_read_b64_tr_b16 v[168:169], v208 offset:37376\n"
"ds_read_b64_tr_b16 v[170:171], v208 offset:39424\n"
"v_max3_f32 v226, v226, v128, v129\n"
"v_max3_f32 v227, v227, v130, v131\n"
"v_max3_f32 v226, v226, v132, v133\n"
"v_max3_f32 v227, v227, v134, v135\n"
"s_waitcnt lgkmcnt(6)\n"
"v_mfma_f32_32x32x16_bf16 v[48:63], v[172:175], v[144:147], v[48:63]\n"
"ds_read_b64_tr_b16 v[172:173], v208 offset:37632\n"
"ds_read_b64_tr_b16 v[174:175], v208 offset:39680\n"
"v_max3_f32 v226, v226, v136, v137\n"
"v_max3_f32 v227, v227, v138, v139\n"
"v_max3_f32 v226, v226, v140, v141\n"
"v_max3_f32 v227, v227, v142, v143\n"
"v_max3_f32 v226, v226, v227, v227\n"
"v_mov_b32 v227, v226\n"
"s_nop 1\n"
"v_permlane32_swap_b32 v226, v227\n"
"v_max_f32 v226, v226, v227\n"
"s_mov_b32 s60, 0\n"
"v_cmp_lt_f32 vcc, s64, v226\n"
"s_cbranch_vccnz KF_RA3\n"
"KF_RB3:\n"
"s_waitcnt lgkmcnt(6)\n"
"v_mfma_f32_32x32x16_bf16 v[0:15], v[160:163], v[148:151], v[0:15]\n"
"ds_read_b64_tr_b16 v[160:161], v208 offset:40960\n"
"ds_read_b64_tr_b16 v[162:163], v208 offset:43008\n"
"v_exp_f32 v96, v96\n"
"v_exp_f32 v97, v97\n"
"s_waitcnt lgkmcnt(6)\n"
"v_mfma_f32_32x32x16_bf16 v[16:31], v[164:167], v[148:151], v[16:31]\n"
"ds_read_b64_tr_b16 v[164:165], v208 offset:41216\n"
"ds_read_b64_tr_b16 v[166:167], v208 offset:43264\n"
"v_mfma_f32_16x16x32_bf16 v[192:195], v[196:199], v[148:151], v[192:195]\n"
"v_exp_f32 v98, v98\n"
"v_cvt_pk_bf16_f32 v144, v96, v97\n"
"s_waitcnt lgkmcnt(6)\n"
"v_mfma_f32_32x32x16_bf16 v[32:47], v[168:171], v[148:151], v[32:47]\n"
"ds_read_b64_tr_b16 v[168:169], v208 offset:41472\n"
"ds_read_b64_tr_b16 v[170:171], v208 offset:43520\n"
"buffer_load_dwordx4 v210, s[32:35], s56 offen offset:1024 nt lds\n"
"v_exp_f32 v99, v99\n"
"s_waitcnt lgkmcnt(6)\n"
"v_mfma_f32_32x32x16_bf16 v[48:63], v[172:175], v[148:151], v[48:63]\n"
"ds_read_b64_tr_b16 v[172:173], v208 offset:41728\n"
"ds_read_b64_tr_b16 v[174:175], v208 offset:43776\n"
"v_exp_f32 v100, v100\n"
"v_exp_f32 v101, v101\n"
"v_cvt_pk_bf16_f32 v145, v98, v99\n"
"s_waitcnt lgkmcnt(6)\n"
"v_mfma_f32_32x32x16_bf16 v[0:15], v[160:163], v[152:155], v[0:15]\n"
"ds_read_b64_tr_b16 v[160:161], v208 offset:45056\n"
"ds_read_b64_tr_b16 v[162:163], v208 offset:47104\n"
"buffer_load_dwordx4 v211, s[32:35], s56 offen offset:2048 nt lds\n"
"v_exp_f32 v102, v102\n"
"v_cvt_pk_bf16_f32 v146, v100, v101\n"
"s_waitcnt lgkmcnt(6)\n"
"v_mfma_f32_32x32x16_bf16 v[16:31], v[164:167], v[152:155], v[16:31]\n"
"ds_read_b64_tr_b16 v[164:165], v208 offset:45312\n"
"ds_read_b64_tr_b16 v[166:167], v208 offset:47360\n"
"v_mfma_f32_16x16x32_bf16 v[192:195], v[196:199], v[152:155], v[192:195]\n"
"v_exp_f32 v103, v103\n"
"s_waitcnt lgkmcnt(6)\n"
"v_mfma_f32_32x32x16_bf16 v[32:47], v[168:171], v[152:155], v[32:47]\n"
"ds_read_b64_tr_b16 v[168:169], v208 offset:45568\n"
"ds_read_b64_tr_b16 v[170:171], v208 offset:47616\n"
"v_exp_f32 v104, v104\n"
"v_exp_f32 v105, v105\n"
"v_cvt_pk_bf16_f32 v147, v102, v103\n"
"s_waitcnt lgkmcnt(6)\n"
"v_mfma_f32_32x32x16_bf16 v[48:63], v[172:175], v[152:155], v[48:63]\n"
"ds_read_b64_tr_b16 v[172:173], v208 offset:45824\n"
"ds_read_b64_tr_b16 v[174:175], v208 offset:47872\n"
"buffer_load_dwordx4 v212, s[32:35], s56 offen offset:3072 nt lds\n"
"v_exp_f32 v106, v106\n"
"v_cvt_pk_bf16_f32 v148, v104, v105\n"
"s_waitcnt lgkmcnt(6)\n"
"v_mfma_f32_32x32x16_bf16 v[0:15], v[160:163], v[156:159], v[0:15]\n"
"v_exp_f32 v107, v107\n"
"s_waitcnt vmcnt(8)\n"
"s_waitcnt lgkmcnt(0)\n"
"s_barrier\n"
"v_mfma_f32_32x32x16_bf16 v[16:31], v[164:167], v[156:159], v[16:31]\n"
"v_mfma_f32_16x16x32_bf16 v[192:195], v[196:199], v[156:159], v[192:195]\n"
"v_exp_f32 v108, v108\n"
"v_exp_f32 v109, v109\n"
"v_cvt_pk_bf16_f32 v149, v106, v107\n"
"ds_read_b128 v[160:163], v200 offset:0\n"
"ds_read_b128 v[164:167], v201 offset:0\n"
"v_mfma_f32_32x32x16_bf16 v[32:47], v[168:171], v[156:159], v[32:47]\n"
"v_exp_f32 v110, v110\n"
"v_cvt_pk_bf16_f32 v150, v108, v109\n"
"ds_read_b128 v[168:171], v202 offset:0\n"
"v_mfma_f32_32x32x16_bf16 v[48:63], v[172:175], v[156:159], v[48:63]\n"
"v_exp_f32 v111, v111\n"
"ds_read_b128 v[172:175], v203 offset:0\n"
"s_cmp_lg_u32 s60, 0\n"
"s_cbranch_scc1 KF_RC3\n"
"KF_RD3:\n"
"v_cvt_pk_bf16_f32 v151, v110, v111\n"
"s_waitcnt lgkmcnt(3)\n"
"v_mfma_f32_32x32x16_bf16 v[96:111], v[160:163], v[64:67], v[176:191]\n"
"ds_read_b128 v[160:163], v204 offset:0\n"
"v_exp_f32 v128, v128\n"
"s_waitcnt lgkmcnt(3)\n"
"v_mfma_f32_32x32x16_bf16 v[96:111], v[164:167], v[68:71], v[96:111]\n"
"ds_read_b128 v[164:167], v205 offset:0\n"
"v_exp_f32 v129, v129\n"
"s_add_u32 m0, s49, 65536\n"
"s_add_u32 s56, s48, 81920\n"
"buffer_load_dwordx4 v213, s[36:39], s56 offen nt lds\n"
"s_waitcnt lgkmcnt(3)\n"
"v_mfma_f32_32x32x16_bf16 v[96:111], v[168:171], v[72:75], v[96:111]\n"
"ds_read_b128 v[168:171], v206 offset:0\n"
"v_exp_f32 v130, v130\n"
"v_cvt_pk_bf16_f32 v152, v128, v129\n"
"s_waitcnt lgkmcnt(3)\n"
"v_mfma_f32_32x32x16_bf16 v[96:111], v[172:175], v[76:79], v[96:111]\n"
"ds_read_b128 v[172:175], v207 offset:0\n"
"v_exp_f32 v131, v131\n"
"s_waitcnt lgkmcnt(3)\n"
"v_mfma_f32_32x32x16_bf16 v[96:111], v[160:163], v[80:83], v[96:111]\n"
"ds_read_b128 v[160:163], v200 offset:8192\n"
"v_exp_f32 v132, v132\n"
"v_cvt_pk_bf16_f32 v153, v130, v131\n"
"buffer_load_dwordx4 v213, s[36:39], s56 offen offset:1024 nt lds\n"
"s_waitcnt lgkmcnt(3)\n"
"v_mfma_f32_32x32x16_bf16 v[96:111], v[164:167], v[84:87], v[96:111]\n"
"ds_read_b128 v[164:167], v201 offset:8192\n"
"v_exp_f32 v133, v133\n"
"s_waitcnt lgkmcnt(3)\n"
"v_mfma_f32_32x32x16_bf16 v[96:111], v[168:171], v[88:91], v[96:111]\n"
"ds_read_b128 v[168:171], v202 offset:8192\n"
"v_exp_f32 v134, v134\n"
"v_cvt_pk_bf16_f32 v154, v132, v133\n"
"s_waitcnt lgkmcnt(3)\n"
"v_mfma_f32_32x32x16_bf16 v[96:111], v[172:175], v[92:95], v[96:111]\n"
"ds_read_b128 v[172:175], v203 offset:8192\n"
"v_exp_f32 v135, v135\n"
"buffer_load_dwordx4 v213, s[36:39], s56 offen offset:2048 nt lds\n"
"s_waitcnt lgkmcnt(3)\n"
"v_mfma_f32_32x32x16_bf16 v[112:127], v[160:163], v[64:67], v[176:191]\n"
"ds_read_b128 v[160:163], v204 offset:8192\n"
"v_exp_f32 v136, v136\n"
"v_cvt_pk_bf16_f32 v155, v134, v135\n"
"s_waitcnt lgkmcnt(3)\n"
"v_mfma_f32_32x32x16_bf16 v[112:127], v[164:167], v[68:71], v[112:127]\n"
"ds_read_b128 v[164:167], v205 offset:8192\n"
"v_exp_f32 v137, v137\n"
"buffer_load_dwordx4 v213, s[36:39], s56 offen offset:3072 nt lds\n"
"s_waitcnt lgkmcnt(3)\n"
"v_mfma_f32_32x32x16_bf16 v[112:127], v[168:171], v[72:75], v[112:127]\n"
"ds_read_b128 v[168:171], v206 offset:8192\n"
"v_exp_f32 v138, v138\n"
"v_cvt_pk_bf16_f32 v156, v136, v137\n"
"v_max3_f32 v226, v96, v97, v98\n"
"v_max3_f32 v227, v99, v100, v101\n"
"s_waitcnt lgkmcnt(3)\n"
"v_mfma_f32_32x32x16_bf16 v[112:127], v[172:175], v[76:79], v[112:127]\n"
"ds_read_b128 v[172:175], v207 offset:8192\n"
"v_exp_f32 v139, v139\n"
"v_max3_f32 v226, v226, v102, v103\n"
"v_max3_f32 v227, v227, v104, v105\n"
"s_waitcnt lgkmcnt(3)\n"
"v_mfma_f32_32x32x16_bf16 v[112:127], v[160:163], v[80:83], v[112:127]\n"
"v_exp_f32 v140, v140\n"
"v_cvt_pk_bf16_f32 v157, v138, v139\n"
"s_waitcnt lgkmcnt(0)\n"
"s_barrier\n"
"v_mfma_f32_32x32x16_bf16 v[112:127], v[164:167], v[84:87], v[112:127]\n"
"v_exp_f32 v141, v141\n"
"ds_read_b64_tr_b16 v[160:161], v208 offset:0\n"
"ds_read_b64_tr_b16 v[162:163], v208 offset:2048\n"
"ds_read_b64_tr_b16 v[164:165], v208 offset:256\n"
"ds_read_b64_tr_b16 v[166:167], v208 offset:2304\n"
"v_max3_f32 v226, v226, v106, v107\n"
"v_max3_f32 v227, v227, v108, v109\n"
"v_mfma_f32_32x32x16_bf16 v[112:127], v[168:171], v[88:91], v[112:127]\n"
"v_exp_f32 v142, v142\n"
"v_cvt_pk_bf16_f32 v158, v140, v141\n"
"ds_read_b64_tr_b16 v[168:169], v208 offset:512\n"
"ds_read_b64_tr_b16 v[170:171], v208 offset:2560\n"
"v_max3_f32 v226, v226, v110, v111\n"
"v_mfma_f32_32x32x16_bf16 v[112:127], v[172:175], v[92:95], v[112:127]\n"
"v_exp_f32 v143, v143\n"
"ds_read_b64_tr_b16 v[172:173], v208 offset:768\n"
"ds_read_b64_tr_b16 v[174:175], v208 offset:2816\n"
"s_waitcnt lgkmcnt(6)\n"
"v_mfma_f32_32x32x16_bf16 v[0:15], v[160:163], v[144:147], v[0:15]\n"
"ds_read_b64_tr_b16 v[160:161], v208 offset:4096\n"
"ds_read_b64_tr_b16 v[162:163], v208 offset:6144\n"
"v_cvt_pk_bf16_f32 v159, v142, v143\n"
"s_add_u32 m0, s49, 0\n"
"s_add_u32 s56, s48, 98304\n"
"buffer_load_dwordx4 v209, s[32:35], s56 offen nt lds\n"
"s_waitcnt lgkmcnt(6)\n"
"v_mfma_f32_32x32x16_bf16 v[16:31], v[164:167], v[144:147], v[16:31]\n"
"ds_read_b64_tr_b16 v[164:165], v208 offset:4352\n"
"ds_read_b64_tr_b16 v[166:167], v208 offset:6400\n"
"v_mfma_f32_16x16x32_bf16 v[192:195], v[196:199], v[144:147], v[192:195]\n"
"s_waitcnt lgkmcnt(6)\n"
"v_mfma_f32_32x32x16_bf16 v[32:47], v[168:171], v[144:147], v[32:47]\n"
"ds_read_b64_tr_b16 v[168:169], v208 offset:4608\n"
"ds_read_b64_tr_b16 v[170:171], v208 offset:6656\n"
"v_max3_f32 v226, v226, v112, v113\n"
"v_max3_f32 v227, v227, v114, v115\n"
"v_max3_f32 v226, v226, v116, v117\n"
"v_max3_f32 v227, v227, v118, v119\n"
"s_waitcnt lgkmcnt(6)\n"
"v_mfma_f32_32x32x16_bf16 v[48:63], v[172:175], v[144:147], v[48:63]\n"
"ds_read_b64_tr_b16 v[172:173], v208 offset:4864\n"
"ds_read_b64_tr_b16 v[174:175], v208 offset:6912\n"
"v_max3_f32 v226, v226, v120, v121\n"
"v_max3_f32 v227, v227, v122, v123\n"
"v_max3_f32 v226, v226, v124, v125\n"
"v_max3_f32 v227, v227, v126, v127\n"
"v_max3_f32 v226, v226, v227, v227\n"
"v_mov_b32 v227, v226\n"
"s_nop 1\n"
"v_permlane32_swap_b32 v226, v227\n"
"v_max_f32 v226, v226, v227\n"
"s_mov_b32 s60, 0\n"
"v_cmp_lt_f32 vcc, s64, v226\n"
"s_cbranch_vccnz KF_RA4\n"
"KF_RB4:\n"
"s_waitcnt lgkmcnt(6)\n"
"v_mfma_f32_32x32x16_bf16 v[0:15], v[160:163], v[148:151], v[0:15]\n"
"ds_read_b64_tr_b16 v[160:161], v208 offset:8192\n"
"ds_read_b64_tr_b16 v[162:163], v208 offset:10240\n"
"v_exp_f32 v96, v96\n"
"v_exp_f32 v97, v97\n"
"s_waitcnt lgkmcnt(6)\n"
"v_mfma_f32_32x32x16_bf16 v[16:31], v[164:167], v[148:151], v[16:31]\n"
"ds_read_b64_tr_b16 v[164:165], v208 offset:8448\n"
"ds_read_b64_tr_b16 v[166:167], v208 offset:10496\n"
"v_mfma_f32_16x16x32_bf16 v[192:195], v[196:199], v[148:151], v[192:195]\n"
"v_exp_f32 v98, v98\n"
"v_cvt_pk_bf16_f32 v144, v96, v97\n"
"s_waitcnt lgkmcnt(6)\n"
"v_mfma_f32_32x32x16_bf16 v[32:47], v[168:171], v[148:151], v[32:47]\n"
"ds_read_b64_tr_b16 v[168:169], v208 offset:8704\n"
"ds_read_b64_tr_b16 v[170:171], v208 offset:10752\n"
"buffer_load_dwordx4 v210, s[32:35], s56 offen offset:1024 nt lds\n"
"v_exp_f32 v99, v99\n"
"s_waitcnt lgkmcnt(6)\n"
"v_mfma_f32_32x32x16_bf16 v[48:63], v[172:175], v[148:151], v[48:63]\n"
"ds_read_b64_tr_b16 v[172:173], v208 offset:8960\n"
"ds_read_b64_tr_b16 v[174:175], v208 offset:11008\n"
"v_exp_f32 v100, v100\n"
"v_exp_f32 v101, v101\n"
"v_cvt_pk_bf16_f32 v145, v98, v99\n"
"s_waitcnt lgkmcnt(6)\n"
"v_mfma_f32_32x32x16_bf16 v[0:15], v[160:163], v[152:155], v[0:15]\n"
"ds_read_b64_tr_b16 v[160:161], v208 offset:12288\n"
"ds_read_b64_tr_b16 v[162:163], v208 offset:14336\n"
"buffer_load_dwordx4 v211, s[32:35], s56 offen offset:2048 nt lds\n"
"v_exp_f32 v102, v102\n"
"v_cvt_pk_bf16_f32 v146, v100, v101\n"
"s_waitcnt lgkmcnt(6)\n"
"v_mfma_f32_32x32x16_bf16 v[16:31], v[164:167], v[152:155], v[16:31]\n"
"ds_read_b64_tr_b16 v[164:165], v208 offset:12544\n"
"ds_read_b64_tr_b16 v[166:167], v208 offset:14592\n"
"v_mfma_f32_16x16x32_bf16 v[192:195], v[196:199], v[152:155], v[192:195]\n"
"v_exp_f32 v103, v103\n"
"s_waitcnt lgkmcnt(6)\n"
"v_mfma_f32_32x32x16_bf16 v[32:47], v[168:171], v[152:155], v[32:47]\n"
"ds_read_b64_tr_b16 v[168:169], v208 offset:12800\n"
"ds_read_b64_tr_b16 v[170:171], v208 offset:14848\n"
"v_exp_f32 v104, v104\n"
"v_exp_f32 v105, v105\n"
"v_cvt_pk_bf16_f32 v147, v102, v103\n"
"s_waitcnt lgkmcnt(6)\n"
"v_mfma_f32_32x32x16_bf16 v[48:63], v[172:175], v[152:155], v[48:63]\n"
"ds_read_b64_tr_b16 v[172:173], v208 offset:13056\n"
"ds_read_b64_tr_b16 v[174:175], v208 offset:15104\n"
"buffer_load_dwordx4 v212, s[32:35], s56 offen offset:3072 nt lds\n"
"v_exp_f32 v106, v106\n"
"v_cvt_pk_bf16_f32 v148, v104, v105\n"
"s_waitcnt lgkmcnt(6)\n"
"v_mfma_f32_32x32x16_bf16 v[0:15], v[160:163], v[156:159], v[0:15]\n"
"v_exp_f32 v107, v107\n"
"s_waitcnt vmcnt(8)\n"
"s_waitcnt lgkmcnt(0)\n"
"s_barrier\n"
"v_mfma_f32_32x32x16_bf16 v[16:31], v[164:167], v[156:159], v[16:31]\n"
"v_mfma_f32_16x16x32_bf16 v[192:195], v[196:199], v[156:159], v[192:195]\n"
"v_exp_f32 v108, v108\n"
"v_exp_f32 v109, v109\n"
"v_cvt_pk_bf16_f32 v149, v106, v107\n"
"ds_read_b128 v[160:163], v200 offset:16384\n"
"ds_read_b128 v[164:167], v201 offset:16384\n"
"v_mfma_f32_32x32x16_bf16 v[32:47], v[168:171], v[156:159], v[32:47]\n"
"v_exp_f32 v110, v110\n"
"v_cvt_pk_bf16_f32 v150, v108, v109\n"
"ds_read_b128 v[168:171], v202 offset:16384\n"
"v_mfma_f32_32x32x16_bf16 v[48:63], v[172:175], v[156:159], v[48:63]\n"
"v_exp_f32 v111, v111\n"
"ds_read_b128 v[172:175], v203 offset:16384\n"
"s_cmp_lg_u32 s60, 0\n"
"s_cbranch_scc1 KF_RC4\n"
"KF_RD4:\n"
"v_cvt_pk_bf16_f32 v151, v110, v111\n"
"s_waitcnt lgkmcnt(3)\n"
"v_mfma_f32_32x32x16_bf16 v[96:111], v[160:163], v[64:67], v[176:191]\n"
"ds_read_b128 v[160:163], v204 offset:16384\n"
"v_exp_f32 v112, v112\n"
"s_waitcnt lgkmcnt(3)\n"
"v_mfma_f32_32x32x16_bf16 v[96:111], v[164:167], v[68:71], v[96:111]\n"
"ds_read_b128 v[164:167], v205 offset:16384\n"
"v_exp_f32 v113, v113\n"
"s_add_u32 m0, s49, 32768\n"
"s_add_u32 s56, s48, 98304\n"
"buffer_load_dwordx4 v213, s[36:39], s56 offen nt lds\n"
"s_waitcnt lgkmcnt(3)\n"
"v_mfma_f32_32x32x16_bf16 v[96:111], v[168:171], v[72:75], v[96:111]\n"
"ds_read_b128 v[168:171], v206 offset:16384\n"
"v_exp_f32 v114, v114\n"
"v_cvt_pk_bf16_f32 v152, v112, v113\n"
"s_waitcnt lgkmcnt(3)\n"
"v_mfma_f32_32x32x16_bf16 v[96:111], v[172:175], v[76:79], v[96:111]\n"
"ds_read_b128 v[172:175], v207 offset:16384\n"
"v_exp_f32 v115, v115\n"
"s_waitcnt lgkmcnt(3)\n"
"v_mfma_f32_32x32x16_bf16 v[96:111], v[160:163], v[80:83], v[96:111]\n"
"ds_read_b128 v[160:163], v200 offset:24576\n"
"v_exp_f32 v116, v116\n"
"v_cvt_pk_bf16_f32 v153, v114, v115\n"
"buffer_load_dwordx4 v213, s[36:39], s56 offen offset:1024 nt lds\n"
"s_waitcnt lgkmcnt(3)\n"
"v_mfma_f32_32x32x16_bf16 v[96:111], v[164:167], v[84:87], v[96:111]\n"
"ds_read_b128 v[164:167], v201 offset:24576\n"
"v_exp_f32 v117, v117\n"
"s_waitcnt lgkmcnt(3)\n"
"v_mfma_f32_32x32x16_bf16 v[96:111], v[168:171], v[88:91], v[96:111]\n"
"ds_read_b128 v[168:171], v202 offset:24576\n"
"v_exp_f32 v118, v118\n"
"v_cvt_pk_bf16_f32 v154, v116, v117\n"
"s_waitcnt lgkmcnt(3)\n"
"v_mfma_f32_32x32x16_bf16 v[96:111], v[172:175], v[92:95], v[96:111]\n"
"ds_read_b128 v[172:175], v203 offset:24576\n"
"v_exp_f32 v119, v119\n"
"buffer_load_dwordx4 v213, s[36:39], s56 offen offset:2048 nt lds\n"
"s_waitcnt lgkmcnt(3)\n"
"v_mfma_f32_32x32x16_bf16 v[128:143], v[160:163], v[64:67], v[176:191]\n"
"ds_read_b128 v[160:163], v204 offset:24576\n"
"v_exp_f32 v120, v120\n"
"v_cvt_pk_bf16_f32 v155, v118, v119\n"
"s_waitcnt lgkmcnt(3)\n"
"v_mfma_f32_32x32x16_bf16 v[128:143], v[164:167], v[68:71], v[128:143]\n"
"ds_read_b128 v[164:167], v205 offset:24576\n"
"v_exp_f32 v121, v121\n"
"buffer_load_dwordx4 v213, s[36:39], s56 offen offset:3072 nt lds\n"
"s_waitcnt lgkmcnt(3)\n"
"v_mfma_f32_32x32x16_bf16 v[128:143], v[168:171], v[72:75], v[128:143]\n"
"ds_read_b128 v[168:171], v206 offset:24576\n"
"v_exp_f32 v122, v122\n"
"v_cvt_pk_bf16_f32 v156, v120, v121\n"
"v_max3_f32 v226, v96, v97, v98\n"
"v_max3_f32 v227, v99, v100, v101\n"
"s_waitcnt lgkmcnt(3)\n"
"v_mfma_f32_32x32x16_bf16 v[128:143], v[172:175], v[76:79], v[128:143]\n"
"ds_read_b128 v[172:175], v207 offset:24576\n"
"v_exp_f32 v123, v123\n"
"v_max3_f32 v226, v226, v102, v103\n"
"v_max3_f32 v227, v227, v104, v105\n"
"s_waitcnt lgkmcnt(3)\n"
"v_mfma_f32_32x32x16_bf16 v[128:143], v[160:163], v[80:83], v[128:143]\n"
"v_exp_f32 v124, v124\n"
"v_cvt_pk_bf16_f32 v157, v122, v123\n"
"s_waitcnt lgkmcnt(0)\n"
"s_barrier\n"
"v_mfma_f32_32x32x16_bf16 v[128:143], v[164:167], v[84:87], v[128:143]\n"
"v_exp_f32 v125, v125\n"
"ds_read_b64_tr_b16 v[160:161], v208 offset:16384\n"
"ds_read_b64_tr_b16 v[162:163], v208 offset:18432\n"
"ds_read_b64_tr_b16 v[164:165], v208 offset:16640\n"
"ds_read_b64_tr_b16 v[166:167], v208 offset:18688\n"
"v_max3_f32 v226, v226, v106, v107\n"
"v_max3_f32 v227, v227, v108, v109\n"
"v_mfma_f32_32x32x16_bf16 v[128:143], v[168:171], v[88:91], v[128:143]\n"
"v_exp_f32 v126, v126\n"
"v_cvt_pk_bf16_f32 v158, v124, v125\n"
"ds_read_b64_tr_b16 v[168:169], v208 offset:16896\n"
"ds_read_b64_tr_b16 v[170:171], v208 offset:18944\n"
"v_max3_f32 v226, v226, v110, v111\n"
"v_mfma_f32_32x32x16_bf16 v[128:143], v[172:175], v[92:95], v[128:143]\n"
"v_exp_f32 v127, v127\n"
"ds_read_b64_tr_b16 v[172:173], v208 offset:17152\n"
"ds_read_b64_tr_b16 v[174:175], v208 offset:19200\n"
"s_waitcnt lgkmcnt(6)\n"
"v_mfma_f32_32x32x16_bf16 v[0:15], v[160:163], v[144:147], v[0:15]\n"
"ds_read_b64_tr_b16 v[160:161], v208 offset:20480\n"
"ds_read_b64_tr_b16 v[162:163], v208 offset:22528\n"
"v_cvt_pk_bf16_f32 v159, v126, v127\n"
"s_add_u32 m0, s49, 16384\n"
"s_add_u32 s56, s48, 114688\n"
"buffer_load_dwordx4 v209, s[32:35], s56 offen nt lds\n"
"s_waitcnt lgkmcnt(6)\n"
"v_mfma_f32_32x32x16_bf16 v[16:31], v[164:167], v[144:147], v[16:31]\n"
"ds_read_b64_tr_b16 v[164:165], v208 offset:20736\n"
"ds_read_b64_tr_b16 v[166:167], v208 offset:22784\n"
"v_mfma_f32_16x16x32_bf16 v[192:195], v[196:199], v[144:147], v[192:195]\n"
"s_waitcnt lgkmcnt(6)\n"
"v_mfma_f32_32x32x16_bf16 v[32:47], v[168:171], v[144:147], v[32:47]\n"
"ds_read_b64_tr_b16 v[168:169], v208 offset:20992\n"
"ds_read_b64_tr_b16 v[170:171], v208 offset:23040\n"
"v_max3_f32 v226, v226, v128, v129\n"
"v_max3_f32 v227, v227, v130, v131\n"
"v_max3_f32 v226, v226, v132, v133\n"
"v_max3_f32 v227, v227, v134, v135\n"
"s_waitcnt lgkmcnt(6)\n"
"v_mfma_f32_32x32x16_bf16 v[48:63], v[172:175], v[144:147], v[48:63]\n"
"ds_read_b64_tr_b16 v[172:173], v208 offset:21248\n"
"ds_read_b64_tr_b16 v[174:175], v208 offset:23296\n"
"v_max3_f32 v226, v226, v136, v137\n"
"v_max3_f32 v227, v227, v138, v139\n"
"v_max3_f32 v226, v226, v140, v141\n"
"v_max3_f32 v227, v227, v142, v143\n"
"v_max3_f32 v226, v226, v227, v227\n"
"v_mov_b32 v227, v226\n"
"s_nop 1\n"
"v_permlane32_swap_b32 v226, v227\n"
"v_max_f32 v226, v226, v227\n"
"s_mov_b32 s60, 0\n"
"v_cmp_lt_f32 vcc, s64, v226\n"
"s_cbranch_vccnz KF_RA5\n"
"KF_RB5:\n"
"s_waitcnt lgkmcnt(6)\n"
"v_mfma_f32_32x32x16_bf16 v[0:15], v[160:163], v[148:151], v[0:15]\n"
"ds_read_b64_tr_b16 v[160:161], v208 offset:24576\n"
"ds_read_b64_tr_b16 v[162:163], v208 offset:26624\n"
"v_exp_f32 v96, v96\n"
"v_exp_f32 v97, v97\n"
"s_waitcnt lgkmcnt(6)\n"
"v_mfma_f32_32x32x16_bf16 v[16:31], v[164:167], v[148:151], v[16:31]\n"
"ds_read_b64_tr_b16 v[164:165], v208 offset:24832\n"
"ds_read_b64_tr_b16 v[166:167], v208 offset:26880\n"
"v_mfma_f32_16x16x32_bf16 v[192:195], v[196:199], v[148:151], v[192:195]\n"
"v_exp_f32 v98, v98\n"
"v_cvt_pk_bf16_f32 v144, v96, v97\n"
"s_waitcnt lgkmcnt(6)\n"
"v_mfma_f32_32x32x16_bf16 v[32:47], v[168:171], v[148:151], v[32:47]\n"
"ds_read_b64_tr_b16 v[168:169], v208 offset:25088\n"
"ds_read_b64_tr_b16 v[170:171], v208 offset:27136\n"
"buffer_load_dwordx4 v210, s[32:35], s56 offen offset:1024 nt lds\n"
"v_exp_f32 v99, v99\n"
"s_waitcnt lgkmcnt(6)\n"
"v_mfma_f32_32x32x16_bf16 v[48:63], v[172:175], v[148:151], v[48:63]\n"
"ds_read_b64_tr_b16 v[172:173], v208 offset:25344\n"
"ds_read_b64_tr_b16 v[174:175], v208 offset:27392\n"
"v_exp_f32 v100, v100\n"
"v_exp_f32 v101, v101\n"
"v_cvt_pk_bf16_f32 v145, v98, v99\n"
"s_waitcnt lgkmcnt(6)\n"
"v_mfma_f32_32x32x16_bf16 v[0:15], v[160:163], v[152:155], v[0:15]\n"
"ds_read_b64_tr_b16 v[160:161], v208 offset:28672\n"
"ds_read_b64_tr_b16 v[162:163], v208 offset:30720\n"
"buffer_load_dwordx4 v211, s[32:35], s56 offen offset:2048 nt lds\n"
"v_exp_f32 v102, v102\n"
"v_cvt_pk_bf16_f32 v146, v100, v101\n"
"s_waitcnt lgkmcnt(6)\n"
"v_mfma_f32_32x32x16_bf16 v[16:31], v[164:167], v[152:155], v[16:31]\n"
"ds_read_b64_tr_b16 v[164:165], v208 offset:28928\n"
"ds_read_b64_tr_b16 v[166:167], v208 offset:30976\n"
"v_mfma_f32_16x16x32_bf16 v[192:195], v[196:199], v[152:155], v[192:195]\n"
"v_exp_f32 v103, v103\n"
"s_waitcnt lgkmcnt(6)\n"
"v_mfma_f32_32x32x16_bf16 v[32:47], v[168:171], v[152:155], v[32:47]\n"
"ds_read_b64_tr_b16 v[168:169], v208 offset:29184\n"
"ds_read_b64_tr_b16 v[170:171], v208 offset:31232\n"
"v_exp_f32 v104, v104\n"
"v_exp_f32 v105, v105\n"
"v_cvt_pk_bf16_f32 v147, v102, v103\n"
"s_waitcnt lgkmcnt(6)\n"
"v_mfma_f32_32x32x16_bf16 v[48:63], v[172:175], v[152:155], v[48:63]\n"
"ds_read_b64_tr_b16 v[172:173], v208 offset:29440\n"
"ds_read_b64_tr_b16 v[174:175], v208 offset:31488\n"
"buffer_load_dwordx4 v212, s[32:35], s56 offen offset:3072 nt lds\n"
"v_exp_f32 v106, v106\n"
"v_cvt_pk_bf16_f32 v148, v104, v105\n"
"s_waitcnt lgkmcnt(6)\n"
"v_mfma_f32_32x32x16_bf16 v[0:15], v[160:163], v[156:159], v[0:15]\n"
"v_exp_f32 v107, v107\n"
"s_waitcnt vmcnt(8)\n"
"s_waitcnt lgkmcnt(0)\n"
"s_barrier\n"
"v_mfma_f32_32x32x16_bf16 v[16:31], v[164:167], v[156:159], v[16:31]\n"
"v_mfma_f32_16x16x32_bf16 v[192:195], v[196:199], v[156:159], v[192:195]\n"
"v_exp_f32 v108, v108\n"
"v_exp_f32 v109, v109\n"
"v_cvt_pk_bf16_f32 v149, v106, v107\n"
"ds_read_b128 v[160:163], v200 offset:0\n"
"ds_read_b128 v[164:167], v201 offset:0\n"
"v_mfma_f32_32x32x16_bf16 v[32:47], v[168:171], v[156:159], v[32:47]\n"
"v_exp_f32 v110, v110\n"
"v_cvt_pk_bf16_f32 v150, v108, v109\n"
"ds_read_b128 v[168:171], v202 offset:0\n"
"v_mfma_f32_32x32x16_bf16 v[48:63], v[172:175], v[156:159], v[48:63]\n"
"v_exp_f32 v111, v111\n"
"ds_read_b128 v[172:175], v203 offset:0\n"
"s_cmp_lg_u32 s60, 0\n"
"s_cbranch_scc1 KF_RC5\n"
"KF_RD5:\n"
"v_cvt_pk_bf16_f32 v151, v110, v111\n"
"s_waitcnt lgkmcnt(3)\n"
"v_mfma_f32_32x32x16_bf16 v[96:111], v[160:163], v[64:67], v[176:191]\n"
"ds_read_b128 v[160:163], v204 offset:0\n"
"v_exp_f32 v128, v128\n"
"s_waitcnt lgkmcnt(3)\n"
"v_mfma_f32_32x32x16_bf16 v[96:111], v[164:167], v[68:71], v[96:111]\n"
"ds_read_b128 v[164:167], v205 offset:0\n"
"v_exp_f32 v129, v129\n"
"s_add_u32 m0, s49, 49152\n"
"s_add_u32 s56, s48, 114688\n"
"buffer_load_dwordx4 v213, s[36:39], s56 offen nt lds\n"
"s_waitcnt lgkmcnt(3)\n"
"v_mfma_f32_32x32x16_bf16 v[96:111], v[168:171], v[72:75], v[96:111]\n"
"ds_read_b128 v[168:171], v206 offset:0\n"
"v_exp_f32 v130, v130\n"
"v_cvt_pk_bf16_f32 v152, v128, v129\n"
"s_waitcnt lgkmcnt(3)\n"
"v_mfma_f32_32x32x16_bf16 v[96:111], v[172:175], v[76:79], v[96:111]\n"
"ds_read_b128 v[172:175], v207 offset:0\n"
"v_exp_f32 v131, v131\n"
"s_waitcnt lgkmcnt(3)\n"
"v_mfma_f32_32x32x16_bf16 v[96:111], v[160:163], v[80:83], v[96:111]\n"
"ds_read_b128 v[160:163], v200 offset:8192\n"
"v_exp_f32 v132, v132\n"
"v_cvt_pk_bf16_f32 v153, v130, v131\n"
"buffer_load_dwordx4 v213, s[36:39], s56 offen offset:1024 nt lds\n"
"s_waitcnt lgkmcnt(3)\n"
"v_mfma_f32_32x32x16_bf16 v[96:111], v[164:167], v[84:87], v[96:111]\n"
"ds_read_b128 v[164:167], v201 offset:8192\n"
"v_exp_f32 v133, v133\n"
"s_waitcnt lgkmcnt(3)\n"
"v_mfma_f32_32x32x16_bf16 v[96:111], v[168:171], v[88:91], v[96:111]\n"
"ds_read_b128 v[168:171], v202 offset:8192\n"
"v_exp_f32 v134, v134\n"
"v_cvt_pk_bf16_f32 v154, v132, v133\n"
"s_waitcnt lgkmcnt(3)\n"
"v_mfma_f32_32x32x16_bf16 v[96:111], v[172:175], v[92:95], v[96:111]\n"
"ds_read_b128 v[172:175], v203 offset:8192\n"
"v_exp_f32 v135, v135\n"
"buffer_load_dwordx4 v213, s[36:39], s56 offen offset:2048 nt lds\n"
"s_waitcnt lgkmcnt(3)\n"
"v_mfma_f32_32x32x16_bf16 v[112:127], v[160:163], v[64:67], v[176:191]\n"
"ds_read_b128 v[160:163], v204 offset:8192\n"
"v_exp_f32 v136, v136\n"
"v_cvt_pk_bf16_f32 v155, v134, v135\n"
"s_waitcnt lgkmcnt(3)\n"
"v_mfma_f32_32x32x16_bf16 v[112:127], v[164:167], v[68:71], v[112:127]\n"
"ds_read_b128 v[164:167], v205 offset:8192\n"
"v_exp_f32 v137, v137\n"
"buffer_load_dwordx4 v213, s[36:39], s56 offen offset:3072 nt lds\n"
"s_waitcnt lgkmcnt(3)\n"
"v_mfma_f32_32x32x16_bf16 v[112:127], v[168:171], v[72:75], v[112:127]\n"
"ds_read_b128 v[168:171], v206 offset:8192\n"
"v_exp_f32 v138, v138\n"
"v_cvt_pk_bf16_f32 v156, v136, v137\n"
"v_max3_f32 v226, v96, v97, v98\n"
"v_max3_f32 v227, v99, v100, v101\n"
"s_waitcnt lgkmcnt(3)\n"
"v_mfma_f32_32x32x16_bf16 v[112:127], v[172:175], v[76:79], v[112:127]\n"
"ds_read_b128 v[172:175], v207 offset:8192\n"
"v_exp_f32 v139, v139\n"
"v_max3_f32 v226, v226, v102, v103\n"
"v_max3_f32 v227, v227, v104, v105\n"
"s_waitcnt lgkmcnt(3)\n"
"v_mfma_f32_32x32x16_bf16 v[112:127], v[160:163], v[80:83], v[112:127]\n"
"v_exp_f32 v140, v140\n"
"v_cvt_pk_bf16_f32 v157, v138, v139\n"
"s_waitcnt lgkmcnt(0)\n"
"s_barrier\n"
"v_mfma_f32_32x32x16_bf16 v[112:127], v[164:167], v[84:87], v[112:127]\n"
"v_exp_f32 v141, v141\n"
"v_lshrrev_b32 v234, 4, v240\n"
"s_lshl_b32 s12, s44, 6\n"
"v_add_u32 v234, s12, v234\n"
"v_min_u32 v234, 287, v234\n"
"v_mul_u32_u24 v235, 0x1c72, v234\n"
"v_lshrrev_b32 v235, 16, v235\n"
"v_mul_u32_u24 v236, 9, v235\n"
"v_sub_u32 v236, v234, v236\n"
"v_mul_u32_u24 v235, 0x480, v235\n"
"v_lshl_add_u32 v235, v236, 7, v235\n"
"s_lshr_b32 s12, s2, 3\n"
"s_lshl_b32 s12, s12, 5\n"
"s_lshl_b32 s13, s73, 10\n"
"s_add_u32 s12, s12, s13\n"
"s_mul_i32 s12, s12, 1152\n"
"s_add_u32 s12, s12, 2048\n"
"s_lshl_b32 s13, s74, 8\n"
"s_sub_u32 s12, s12, s13\n"
"buffer_load_dword v253, v235, s[76:79], s12 offen\n"
"ds_read_b64_tr_b16 v[160:161], v208 offset:32768\n"
"ds_read_b64_tr_b16 v[162:163], v208 offset:34816\n"
"ds_read_b64_tr_b16 v[164:165], v208 offset:33024\n"
"ds_read_b64_tr_b16 v[166:167], v208 offset:35072\n"
"v_max3_f32 v226, v226, v106, v107\n"
"v_max3_f32 v227, v227, v108, v109\n"
"v_mfma_f32_32x32x16_bf16 v[112:127], v[168:171], v[88:91], v[112:127]\n"
"v_exp_f32 v142, v142\n"
"v_cvt_pk_bf16_f32 v158, v140, v141\n"
"ds_read_b64_tr_b16 v[168:169], v208 offset:33280\n"
"ds_read_b64_tr_b16 v[170:171], v208 offset:35328\n"
"v_max3_f32 v226, v226, v110, v111\n"
"v_mfma_f32_32x32x16_bf16 v[112:127], v[172:175], v[92:95], v[112:127]\n"
"v_exp_f32 v143, v143\n"
"ds_read_b64_tr_b16 v[172:173], v208 offset:33536\n"
"ds_read_b64_tr_b16 v[174:175], v208 offset:35584\n"
"s_waitcnt lgkmcnt(6)\n"
"v_mfma_f32_32x32x16_bf16 v[0:15], v[160:163], v[144:147], v[0:15]\n"
"ds_read_b64_tr_b16 v[160:161], v208 offset:36864\n"
"ds_read_b64_tr_b16 v[162:163], v208 offset:38912\n"
"v_cvt_pk_bf16_f32 v159, v142, v143\n"
"s_waitcnt lgkmcnt(6)\n"
"v_mfma_f32_32x32x16_bf16 v[16:31], v[164:167], v[144:147], v[16:31]\n"
"ds_read_b64_tr_b16 v[164:165], v208 offset:37120\n"
"ds_read_b64_tr_b16 v[166:167], v208 offset:39168\n"
"v_mfma_f32_16x16x32_bf16 v[192:195], v[196:199], v[144:147], v[192:195]\n"
"s_waitcnt lgkmcnt(6)\n"
"v_mfma_f32_32x32x16_bf16 v[32:47], v[168:171], v[144:147], v[32:47]\n"
"ds_read_b64_tr_b16 v[168:169], v208 offset:37376\n"
"ds_read_b64_tr_b16 v[170:171], v208 offset:39424\n"
"v_max3_f32 v226, v226, v112, v113\n"
"v_max3_f32 v227, v227, v114, v115\n"
"v_max3_f32 v226, v226, v116, v117\n"
"v_max3_f32 v227, v227, v118, v119\n"
"s_waitcnt lgkmcnt(6)\n"
"v_mfma_f32_32x32x16_bf16 v[48:63], v[172:175], v[144:147], v[48:63]\n"
"ds_read_b64_tr_b16 v[172:173], v208 offset:37632\n"
"ds_read_b64_tr_b16 v[174:175], v208 offset:39680\n"
"v_max3_f32 v226, v226, v120, v121\n"
"v_max3_f32 v227, v227, v122, v123\n"
"v_max3_f32 v226, v226, v124, v125\n"
"v_max3_f32 v227, v227, v126, v127\n"
"v_max3_f32 v226, v226, v227, v227\n"
"v_mov_b32 v227, v226\n"
"s_nop 1\n"
"v_permlane32_swap_b32 v226, v227\n"
"v_max_f32 v226, v226, v227\n"
"s_mov_b32 s60, 0\n"
"v_cmp_lt_f32 vcc, s64, v226\n"
"s_cbranch_vccnz KF_RA6\n"
"KF_RB6:\n"
"s_waitcnt lgkmcnt(6)\n"
"v_mfma_f32_32x32x16_bf16 v[0:15], v[160:163], v[148:151], v[0:15]\n"
"ds_read_b64_tr_b16 v[160:161], v208 offset:40960\n"
"ds_read_b64_tr_b16 v[162:163], v208 offset:43008\n"
"v_exp_f32 v96, v96\n"
"v_exp_f32 v97, v97\n"
"s_waitcnt lgkmcnt(6)\n"
"v_mfma_f32_32x32x16_bf16 v[16:31], v[164:167], v[148:151], v[16:31]\n"
"ds_read_b64_tr_b16 v[164:165], v208 offset:41216\n"
"ds_read_b64_tr_b16 v[166:167], v208 offset:43264\n"
"v_mfma_f32_16x16x32_bf16 v[192:195], v[196:199], v[148:151], v[192:195]\n"
"v_exp_f32 v98, v98\n"
"v_cvt_pk_bf16_f32 v144, v96, v97\n"
"s_waitcnt lgkmcnt(6)\n"
"v_mfma_f32_32x32x16_bf16 v[32:47], v[168:171], v[148:151], v[32:47]\n"
"ds_read_b64_tr_b16 v[168:169], v208 offset:41472\n"
"ds_read_b64_tr_b16 v[170:171], v208 offset:43520\n"
"v_exp_f32 v99, v99\n"
"s_waitcnt lgkmcnt(6)\n"
"v_mfma_f32_32x32x16_bf16 v[48:63], v[172:175], v[148:151], v[48:63]\n"
"ds_read_b64_tr_b16 v[172:173], v208 offset:41728\n"
"ds_read_b64_tr_b16 v[174:175], v208 offset:43776\n"
"v_exp_f32 v100, v100\n"
"v_exp_f32 v101, v101\n"
"v_cvt_pk_bf16_f32 v145, v98, v99\n"
"s_waitcnt lgkmcnt(6)\n"
"v_mfma_f32_32x32x16_bf16 v[0:15], v[160:163], v[152:155], v[0:15]\n"
"ds_read_b64_tr_b16 v[160:161], v208 offset:45056\n"
"ds_read_b64_tr_b16 v[162:163], v208 offset:47104\n"
"v_exp_f32 v102, v102\n"
"v_cvt_pk_bf16_f32 v146, v100, v101\n"
"s_waitcnt lgkmcnt(6)\n"
"v_mfma_f32_32x32x16_bf16 v[16:31], v[164:167], v[152:155], v[16:31]\n"
"ds_read_b64_tr_b16 v[164:165], v208 offset:45312\n"
"ds_read_b64_tr_b16 v[166:167], v208 offset:47360\n"
"v_mfma_f32_16x16x32_bf16 v[192:195], v[196:199], v[152:155], v[192:195]\n"
"v_exp_f32 v103, v103\n"
"s_waitcnt lgkmcnt(6)\n"
"v_mfma_f32_32x32x16_bf16 v[32:47], v[168:171], v[152:155], v[32:47]\n"
"ds_read_b64_tr_b16 v[168:169], v208 offset:45568\n"
"ds_read_b64_tr_b16 v[170:171], v208 offset:47616\n"
"v_exp_f32 v104, v104\n"
"v_exp_f32 v105, v105\n"
"v_cvt_pk_bf16_f32 v147, v102, v103\n"
"s_waitcnt lgkmcnt(6)\n"
"v_mfma_f32_32x32x16_bf16 v[48:63], v[172:175], v[152:155], v[48:63]\n"
"ds_read_b64_tr_b16 v[172:173], v208 offset:45824\n"
"ds_read_b64_tr_b16 v[174:175], v208 offset:47872\n"
"v_exp_f32 v106, v106\n"
"v_cvt_pk_bf16_f32 v148, v104, v105\n"
"s_waitcnt lgkmcnt(6)\n"
"v_mfma_f32_32x32x16_bf16 v[0:15], v[160:163], v[156:159], v[0:15]\n"
"v_exp_f32 v107, v107\n"
"s_waitcnt vmcnt(5)\n"
"s_waitcnt lgkmcnt(0)\n"
"s_barrier\n"
"v_mfma_f32_32x32x16_bf16 v[16:31], v[164:167], v[156:159], v[16:31]\n"
"v_mfma_f32_16x16x32_bf16 v[192:195], v[196:199], v[156:159], v[192:195]\n"
"v_exp_f32 v108, v108\n"
"v_exp_f32 v109, v109\n"
"v_cvt_pk_bf16_f32 v149, v106, v107\n"
"ds_read_b128 v[160:163], v200 offset:16384\n"
"ds_read_b128 v[164:167], v201 offset:16384\n"
"v_mfma_f32_32x32x16_bf16 v[32:47], v[168:171], v[156:159], v[32:47]\n"
"v_exp_f32 v110, v110\n"
"v_cvt_pk_bf16_f32 v150, v108, v109\n"
"ds_read_b128 v[168:171], v202 offset:16384\n"
"v_mfma_f32_32x32x16_bf16 v[48:63], v[172:175], v[156:159], v[48:63]\n"
"v_exp_f32 v111, v111\n"
"ds_read_b128 v[172:175], v203 offset:16384\n"
"s_cmp_lg_u32 s60, 0\n"
"s_cbranch_scc1 KF_RC6\n"
"KF_RD6:\n"
"v_cvt_pk_bf16_f32 v151, v110, v111\n"
"s_waitcnt lgkmcnt(3)\n"
"v_mfma_f32_32x32x16_bf16 v[96:111], v[160:163], v[64:67], v[176:191]\n"
"ds_read_b128 v[160:163], v204 offset:16384\n"
"v_exp_f32 v112, v112\n"
"s_waitcnt lgkmcnt(3)\n"
"v_mfma_f32_32x32x16_bf16 v[96:111], v[164:167], v[68:71], v[96:111]\n"
"ds_read_b128 v[164:167], v205 offset:16384\n"
"v_exp_f32 v113, v113\n"
"s_waitcnt lgkmcnt(3)\n"
"v_mfma_f32_32x32x16_bf16 v[96:111], v[168:171], v[72:75], v[96:111]\n"
"ds_read_b128 v[168:171], v206 offset:16384\n"
"v_exp_f32 v114, v114\n"
"v_cvt_pk_bf16_f32 v152, v112, v113\n"
"s_waitcnt lgkmcnt(3)\n"
"v_mfma_f32_32x32x16_bf16 v[96:111], v[172:175], v[76:79], v[96:111]\n"
"ds_read_b128 v[172:175], v207 offset:16384\n"
"v_exp_f32 v115, v115\n"
"s_waitcnt lgkmcnt(3)\n"
"v_mfma_f32_32x32x16_bf16 v[96:111], v[160:163], v[80:83], v[96:111]\n"
"ds_read_b128 v[160:163], v200 offset:24576\n"
"v_exp_f32 v116, v116\n"
"v_cvt_pk_bf16_f32 v153, v114, v115\n"
"s_waitcnt lgkmcnt(3)\n"
"v_mfma_f32_32x32x16_bf16 v[96:111], v[164:167], v[84:87], v[96:111]\n"
"ds_read_b128 v[164:167], v201 offset:24576\n"
"v_exp_f32 v117, v117\n"
"s_waitcnt lgkmcnt(3)\n"
"v_mfma_f32_32x32x16_bf16 v[96:111], v[168:171], v[88:91], v[96:111]\n"
"ds_read_b128 v[168:171], v202 offset:24576\n"
"v_exp_f32 v118, v118\n"
"v_cvt_pk_bf16_f32 v154, v116, v117\n"
"s_waitcnt lgkmcnt(3)\n"
"v_mfma_f32_32x32x16_bf16 v[96:111], v[172:175], v[92:95], v[96:111]\n"
"ds_read_b128 v[172:175], v203 offset:24576\n"
"v_exp_f32 v119, v119\n"
"s_waitcnt lgkmcnt(3)\n"
"v_mfma_f32_32x32x16_bf16 v[128:143], v[160:163], v[64:67], v[176:191]\n"
"ds_read_b128 v[160:163], v204 offset:24576\n"
"v_exp_f32 v120, v120\n"
"v_cvt_pk_bf16_f32 v155, v118, v119\n"
"s_waitcnt lgkmcnt(3)\n"
"v_mfma_f32_32x32x16_bf16 v[128:143], v[164:167], v[68:71], v[128:143]\n"
"ds_read_b128 v[164:167], v205 offset:24576\n"
"v_exp_f32 v121, v121\n"
"s_waitcnt lgkmcnt(3)\n"
"v_mfma_f32_32x32x16_bf16 v[128:143], v[168:171], v[72:75], v[128:143]\n"
"ds_read_b128 v[168:171], v206 offset:24576\n"
"v_exp_f32 v122, v122\n"
"v_cvt_pk_bf16_f32 v156, v120, v121\n"
"v_max3_f32 v226, v96, v97, v98\n"
"v_max3_f32 v227, v99, v100, v101\n"
"s_waitcnt lgkmcnt(3)\n"
"v_mfma_f32_32x32x16_bf16 v[128:143], v[172:175], v[76:79], v[128:143]\n"
"ds_read_b128 v[172:175], v207 offset:24576\n"
"v_exp_f32 v123, v123\n"
"v_max3_f32 v226, v226, v102, v103\n"
"v_max3_f32 v227, v227, v104, v105\n"
"s_waitcnt lgkmcnt(3)\n"
"v_mfma_f32_32x32x16_bf16 v[128:143], v[160:163], v[80:83], v[128:143]\n"
"v_exp_f32 v124, v124\n"
"v_cvt_pk_bf16_f32 v157, v122, v123\n"
"s_waitcnt lgkmcnt(0)\n"
"s_barrier\n"
"v_mfma_f32_32x32x16_bf16 v[128:143], v[164:167], v[84:87], v[128:143]\n"
"v_exp_f32 v125, v125\n"
"ds_read_b64_tr_b16 v[160:161], v208 offset:0\n"
"ds_read_b64_tr_b16 v[162:163], v208 offset:2048\n"
"ds_read_b64_tr_b16 v[164:165], v208 offset:256\n"
"ds_read_b64_tr_b16 v[166:167], v208 offset:2304\n"
"v_max3_f32 v226, v226, v106, v107\n"
"v_max3_f32 v227, v227, v108, v109\n"
"v_mfma_f32_32x32x16_bf16 v[128:143], v[168:171], v[88:91], v[128:143]\n"
"v_exp_f32 v126, v126\n"
"v_cvt_pk_bf16_f32 v158, v124, v125\n"
"ds_read_b64_tr_b16 v[168:169], v208 offset:512\n"
"ds_read_b64_tr_b16 v[170:171], v208 offset:2560\n"
"v_max3_f32 v226, v226, v110, v111\n"
"v_mfma_f32_32x32x16_bf16 v[128:143], v[172:175], v[92:95], v[128:143]\n"
"v_exp_f32 v127, v127\n"
"ds_read_b64_tr_b16 v[172:173], v208 offset:768\n"
"ds_read_b64_tr_b16 v[174:175], v208 offset:2816\n"
"s_waitcnt lgkmcnt(6)\n"
"v_mfma_f32_32x32x16_bf16 v[0:15], v[160:163], v[144:147], v[0:15]\n"
"ds_read_b64_tr_b16 v[160:161], v208 offset:4096\n"
"ds_read_b64_tr_b16 v[162:163], v208 offset:6144\n"
"v_cvt_pk_bf16_f32 v159, v126, v127\n"
"s_waitcnt lgkmcnt(6)\n"
"v_mfma_f32_32x32x16_bf16 v[16:31], v[164:167], v[144:147], v[16:31]\n"
"ds_read_b64_tr_b16 v[164:165], v208 offset:4352\n"
"ds_read_b64_tr_b16 v[166:167], v208 offset:6400\n"
"v_mfma_f32_16x16x32_bf16 v[192:195], v[196:199], v[144:147], v[192:195]\n"
"s_waitcnt lgkmcnt(6)\n"
"v_mfma_f32_32x32x16_bf16 v[32:47], v[168:171], v[144:147], v[32:47]\n"
"ds_read_b64_tr_b16 v[168:169], v208 offset:4608\n"
"ds_read_b64_tr_b16 v[170:171], v208 offset:6656\n"
"v_max3_f32 v226, v226, v128, v129\n"
"v_max3_f32 v227, v227, v130, v131\n"
"v_max3_f32 v226, v226, v132, v133\n"
"v_max3_f32 v227, v227, v134, v135\n"
"s_waitcnt lgkmcnt(6)\n"
"v_mfma_f32_32x32x16_bf16 v[48:63], v[172:175], v[144:147], v[48:63]\n"
"ds_read_b64_tr_b16 v[172:173], v208 offset:4864\n"
"ds_read_b64_tr_b16 v[174:175], v208 offset:6912\n"
"v_max3_f32 v226, v226, v136, v137\n"
"v_max3_f32 v227, v227, v138, v139\n"
"v_max3_f32 v226, v226, v140, v141\n"
"v_max3_f32 v227, v227, v142, v143\n"
"v_max3_f32 v226, v226, v227, v227\n"
"v_mov_b32 v227, v226\n"
"s_nop 1\n"
"v_permlane32_swap_b32 v226, v227\n"
"v_max_f32 v226, v226, v227\n"
"s_mov_b32 s60, 0\n"
"v_cmp_lt_f32 vcc, s64, v226\n"
"s_cbranch_vccnz KF_RA7\n"
"KF_RB7:\n"
"s_waitcnt lgkmcnt(6)\n"
"v_mfma_f32_32x32x16_bf16 v[0:15], v[160:163], v[148:151], v[0:15]\n"
"ds_read_b64_tr_b16 v[160:161], v208 offset:8192\n"
"ds_read_b64_tr_b16 v[162:163], v208 offset:10240\n"
"v_exp_f32 v96, v96\n"
"v_exp_f32 v97, v97\n"
"s_waitcnt lgkmcnt(6)\n"
"v_mfma_f32_32x32x16_bf16 v[16:31], v[164:167], v[148:151], v[16:31]\n"
"ds_read_b64_tr_b16 v[164:165], v208 offset:8448\n"
"ds_read_b64_tr_b16 v[166:167], v208 offset:10496\n"
"v_mfma_f32_16x16x32_bf16 v[192:195], v[196:199], v[148:151], v[192:195]\n"
"v_exp_f32 v98, v98\n"
"v_cvt_pk_bf16_f32 v144, v96, v97\n"
"s_waitcnt lgkmcnt(6)\n"
"v_mfma_f32_32x32x16_bf16 v[32:47], v[168:171], v[148:151], v[32:47]\n"
"ds_read_b64_tr_b16 v[168:169], v208 offset:8704\n"
"ds_read_b64_tr_b16 v[170:171], v208 offset:10752\n"
"v_exp_f32 v99, v99\n"
"s_waitcnt lgkmcnt(6)\n"
"v_mfma_f32_32x32x16_bf16 v[48:63], v[172:175], v[148:151], v[48:63]\n"
"ds_read_b64_tr_b16 v[172:173], v208 offset:8960\n"
"ds_read_b64_tr_b16 v[174:175], v208 offset:11008\n"
"v_exp_f32 v100, v100\n"
"v_exp_f32 v101, v101\n"
"v_cvt_pk_bf16_f32 v145, v98, v99\n"
"s_waitcnt lgkmcnt(6)\n"
"v_mfma_f32_32x32x16_bf16 v[0:15], v[160:163], v[152:155], v[0:15]\n"
"ds_read_b64_tr_b16 v[160:161], v208 offset:12288\n"
"ds_read_b64_tr_b16 v[162:163], v208 offset:14336\n"
"v_exp_f32 v102, v102\n"
"v_cvt_pk_bf16_f32 v146, v100, v101\n"
"s_waitcnt lgkmcnt(6)\n"
"v_mfma_f32_32x32x16_bf16 v[16:31], v[164:167], v[152:155], v[16:31]\n"
"ds_read_b64_tr_b16 v[164:165], v208 offset:12544\n"
"ds_read_b64_tr_b16 v[166:167], v208 offset:14592\n"
"v_mfma_f32_16x16x32_bf16 v[192:195], v[196:199], v[152:155], v[192:195]\n"
"v_exp_f32 v103, v103\n"
"s_waitcnt lgkmcnt(6)\n"
"v_mfma_f32_32x32x16_bf16 v[32:47], v[168:171], v[152:155], v[32:47]\n"
"ds_read_b64_tr_b16 v[168:169], v208 offset:12800\n"
"ds_read_b64_tr_b16 v[170:171], v208 offset:14848\n"
"v_exp_f32 v104, v104\n"
"v_exp_f32 v105, v105\n"
"v_cvt_pk_bf16_f32 v147, v102, v103\n"
"s_waitcnt lgkmcnt(6)\n"
"v_mfma_f32_32x32x16_bf16 v[48:63], v[172:175], v[152:155], v[48:63]\n"
"ds_read_b64_tr_b16 v[172:173], v208 offset:13056\n"
"ds_read_b64_tr_b16 v[174:175], v208 offset:15104\n"
"v_exp_f32 v106, v106\n"
"v_cvt_pk_bf16_f32 v148, v104, v105\n"
"s_waitcnt lgkmcnt(6)\n"
"v_mfma_f32_32x32x16_bf16 v[0:15], v[160:163], v[156:159], v[0:15]\n"
"v_exp_f32 v107, v107\n"
"s_waitcnt lgkmcnt(4)\n"
"v_mfma_f32_32x32x16_bf16 v[16:31], v[164:167], v[156:159], v[16:31]\n"
"v_mfma_f32_16x16x32_bf16 v[192:195], v[196:199], v[156:159], v[192:195]\n"
"v_exp_f32 v108, v108\n"
"v_exp_f32 v109, v109\n"
"v_cvt_pk_bf16_f32 v149, v106, v107\n"
"s_waitcnt lgkmcnt(2)\n"
"v_mfma_f32_32x32x16_bf16 v[32:47], v[168:171], v[156:159], v[32:47]\n"
"v_exp_f32 v110, v110\n"
"v_cvt_pk_bf16_f32 v150, v108, v109\n"
"s_waitcnt lgkmcnt(0)\n"
"v_mfma_f32_32x32x16_bf16 v[48:63], v[172:175], v[156:159], v[48:63]\n"
"v_exp_f32 v111, v111\n"
"s_cmp_lg_u32 s60, 0\n"
"s_cbranch_scc1 KF_RC7\n"
"KF_RD7:\n"
"v_cvt_pk_bf16_f32 v151, v110, v111\n"
"v_exp_f32 v128, v128\n"
"v_exp_f32 v129, v129\n"
"v_exp_f32 v130, v130\n"
"v_exp_f32 v131, v131\n"
"v_exp_f32 v132, v132\n"
"v_exp_f32 v133, v133\n"
"v_exp_f32 v134, v134\n"
"v_exp_f32 v135, v135\n"
"v_exp_f32 v136, v136\n"
"v_exp_f32 v137, v137\n"
"v_exp_f32 v138, v138\n"
"v_exp_f32 v139, v139\n"
"v_exp_f32 v140, v140\n"
"v_exp_f32 v141, v141\n"
"v_exp_f32 v142, v142\n"
"v_exp_f32 v143, v143\n"
"v_cvt_pk_bf16_f32 v152, v128, v129\n"
"v_cvt_pk_bf16_f32 v153, v130, v131\n"
"v_cvt_pk_bf16_f32 v154, v132, v133\n"
"v_cvt_pk_bf16_f32 v155, v134, v135\n"
"v_cvt_pk_bf16_f32 v156, v136, v137\n"
"v_cvt_pk_bf16_f32 v157, v138, v139\n"
"v_cvt_pk_bf16_f32 v158, v140, v141\n"
"v_cvt_pk_bf16_f32 v159, v142, v143\n"
"v_mfma_f32_16x16x32_bf16 v[192:195], v[196:199], v[144:147], v[192:195]\n"
"v_mfma_f32_16x16x32_bf16 v[192:195], v[196:199], v[148:151], v[192:195]\n"
"v_mfma_f32_16x16x32_bf16 v[192:195], v[196:199], v[152:155], v[192:195]\n"
"v_mfma_f32_16x16x32_bf16 v[192:195], v[196:199], v[156:159], v[192:195]\n"
"s_waitcnt vmcnt(1)\n"
"s_waitcnt lgkmcnt(0)\n"
"s_barrier\n"
"s_cmp_lg_u32 s44, 0\n"
"s_cbranch_scc1 KF_WA\n"
"v_lshrrev_b32 v232, 4, v240\n"
"v_and_b32 v233, 3, v232\n"
"s_and_b32 s12, s2, 0xe7\n"
"s_and_b32 s13, s2, 0xfc\n"
"v_lshlrev_b32 v234, 3, v233\n"
"v_or_b32 v234, s12, v234\n"
"v_or_b32 v235, s13, v233\n"
"v_cmp_gt_u32 vcc, 4, v232\n"
"s_nop 1\n"
"v_cndmask_b32 v234, v235, v234, vcc\n"
"v_lshlrev_b32 v128, 7, v234\n"
"v_add_u32 v128, 0x3c00000, v128\n"
"v_mov_b32 v235, s73\n"
"v_mov_b32 v236, s74\n"
"v_cndmask_b32 v235, v235, v236, vcc\n"
"v_mov_b32 v236, 0x3c28000\n"
"v_mov_b32 v237, 0x3c08000\n"
"v_cndmask_b32 v236, v236, v237, vcc\n"
"v_lshlrev_b32 v129, 9, v234\n"
"v_lshl_add_u32 v129, v235, 7, v129\n"
"v_add_u32 v129, v129, v236\n"
"s_lshl_b32 s12, s2, 9\n"
"v_lshl_add_u32 v130, v233, 7, v236\n"
"v_add_u32 v130, s12, v130\n"
"s_mov_b64 exec, 0xff\n"
"global_load_dwordx2 v[132:133], v128, s[20:21] sc1\n"
"s_mov_b64 exec, -1\n"
"KF_WA:\n"
"ds_read_b64_tr_b16 v[160:161], v208 offset:16384\n"
"ds_read_b64_tr_b16 v[162:163], v208 offset:18432\n"
"ds_read_b64_tr_b16 v[164:165], v208 offset:20480\n"
"ds_read_b64_tr_b16 v[166:167], v208 offset:22528\n"
"ds_read_b64_tr_b16 v[168:169], v208 offset:24576\n"
"ds_read_b64_tr_b16 v[170:171], v208 offset:26624\n"
"ds_read_b64_tr_b16 v[172:173], v208 offset:28672\n"
"ds_read_b64_tr_b16 v[174:175], v208 offset:30720\n"
"v_rcp_f32 v230, v192\n"
"s_nop 0\n"
"v_mov_b32 v231, v230\n"
"s_mov_b32 s55, 4096\n"
"s_waitcnt lgkmcnt(6)\n"
"v_mfma_f32_32x32x16_bf16 v[0:15], v[160:163], v[144:147], v[0:15]\n"
"ds_read_b64_tr_b16 v[160:161], v208 offset:16640\n"
"ds_read_b64_tr_b16 v[162:163], v208 offset:18688\n"
"s_add_u32 m0, s94, 0\n"
"s_lshl_b32 s54, s74, 1\n"
"s_add_u32 s54, s54, 0\n"
"s_and_b32 s54, s54, 7\n"
"s_mul_i32 s54, s54, 147456\n"
"s_add_u32 s54, s54, s92\n"
"buffer_load_dwordx4 v242, s[76:79], s54 offen lds\n"
"s_waitcnt lgkmcnt(6)\n"
"v_mfma_f32_32x32x16_bf16 v[0:15], v[164:167], v[148:151], v[0:15]\n"
"ds_read_b64_tr_b16 v[164:165], v208 offset:20736\n"
"ds_read_b64_tr_b16 v[166:167], v208 offset:22784\n"
"buffer_load_dwordx4 v243, s[76:79], s54 offen offset:1024 lds\n"
"s_waitcnt lgkmcnt(6)\n"
"v_mfma_f32_32x32x16_bf16 v[0:15], v[168:171], v[152:155], v[0:15]\n"
"ds_read_b64_tr_b16 v[168:169], v208 offset:24832\n"
"ds_read_b64_tr_b16 v[170:171], v208 offset:26880\n"
"buffer_load_dwordx4 v244, s[76:79], s54 offen offset:2048 lds\n"
"s_waitcnt lgkmcnt(6)\n"
"v_mfma_f32_32x32x16_bf16 v[0:15], v[172:175], v[156:159], v[0:15]\n"
"ds_read_b64_tr_b16 v[172:173], v208 offset:28928\n"
"ds_read_b64_tr_b16 v[174:175], v208 offset:30976\n"
"s_waitcnt lgkmcnt(6)\n"
"v_mfma_f32_32x32x16_bf16 v[16:31], v[160:163], v[144:147], v[16:31]\n"
"ds_read_b64_tr_b16 v[160:161], v208 offset:16896\n"
"ds_read_b64_tr_b16 v[162:163], v208 offset:18944\n"
"s_add_u32 m0, s94, 81920\n"
"s_lshl_b32 s54, s74, 1\n"
"s_add_u32 s54, s54, 1\n"
"s_and_b32 s54, s54, 7\n"
"s_mul_i32 s54, s54, 147456\n"
"s_add_u32 s54, s54, s92\n"
"buffer_load_dwordx4 v242, s[76:79], s54 offen lds\n"
"s_waitcnt lgkmcnt(6)\n"
"v_mfma_f32_32x32x16_bf16 v[16:31], v[164:167], v[148:151], v[16:31]\n"
"ds_read_b64_tr_b16 v[164:165], v208 offset:20992\n"
"ds_read_b64_tr_b16 v[166:167], v208 offset:23040\n"
"buffer_load_dwordx4 v243, s[76:79], s54 offen offset:1024 lds\n"
"v_pk_mul_f32 v[0:1], v[0:1], v[230:231]\n"
"v_pk_mul_f32 v[2:3], v[2:3], v[230:231]\n"
"v_pk_mul_f32 v[4:5], v[4:5], v[230:231]\n"
"v_pk_mul_f32 v[6:7], v[6:7], v[230:231]\n"
"v_pk_mul_f32 v[8:9], v[8:9], v[230:231]\n"
"v_pk_mul_f32 v[10:11], v[10:11], v[230:231]\n"
"v_pk_mul_f32 v[12:13], v[12:13], v[230:231]\n"
"s_waitcnt lgkmcnt(6)\n"
"v_mfma_f32_32x32x16_bf16 v[16:31], v[168:171], v[152:155], v[16:31]\n"
"ds_read_b64_tr_b16 v[168:169], v208 offset:25088\n"
"ds_read_b64_tr_b16 v[170:171], v208 offset:27136\n"
"buffer_load_dwordx4 v244, s[76:79], s54 offen offset:2048 lds\n"
"v_pk_mul_f32 v[14:15], v[14:15], v[230:231]\n"
"v_cvt_pk_bf16_f32 v64, v0, v1\n"
"v_cvt_pk_bf16_f32 v65, v2, v3\n"
"v_cvt_pk_bf16_f32 v66, v4, v5\n"
"v_cvt_pk_bf16_f32 v67, v6, v7\n"
"ds_write_b128 v255, v[64:67] offset:0\n"
"v_cvt_pk_bf16_f32 v68, v8, v9\n"
"s_waitcnt lgkmcnt(7)\n"
"v_mfma_f32_32x32x16_bf16 v[16:31], v[172:175], v[156:159], v[16:31]\n"
"ds_read_b64_tr_b16 v[172:173], v208 offset:29184\n"
"ds_read_b64_tr_b16 v[174:175], v208 offset:31232\n"
"v_cvt_pk_bf16_f32 v69, v10, v11\n"
"v_cvt_pk_bf16_f32 v70, v12, v13\n"
"v_cvt_pk_bf16_f32 v71, v14, v15\n"
"ds_write_b128 v255, v[68:71] offset:1024\n"
"s_waitcnt lgkmcnt(8)\n"
"v_mfma_f32_32x32x16_bf16 v[32:47], v[160:163], v[144:147], v[32:47]\n"
"ds_read_b64_tr_b16 v[160:161], v208 offset:17152\n"
"ds_read_b64_tr_b16 v[162:163], v208 offset:19200\n"
"s_waitcnt lgkmcnt(8)\n"
"v_mfma_f32_32x32x16_bf16 v[32:47], v[164:167], v[148:151], v[32:47]\n"
"ds_read_b64_tr_b16 v[164:165], v208 offset:21248\n"
"ds_read_b64_tr_b16 v[166:167], v208 offset:23296\n"
"v_pk_mul_f32 v[16:17], v[16:17], v[230:231]\n"
"v_pk_mul_f32 v[18:19], v[18:19], v[230:231]\n"
"v_pk_mul_f32 v[20:21], v[20:21], v[230:231]\n"
"v_pk_mul_f32 v[22:23], v[22:23], v[230:231]\n"
"v_pk_mul_f32 v[24:25], v[24:25], v[230:231]\n"
"v_pk_mul_f32 v[26:27], v[26:27], v[230:231]\n"
"v_pk_mul_f32 v[28:29], v[28:29], v[230:231]\n"
"s_waitcnt lgkmcnt(8)\n"
"v_mfma_f32_32x32x16_bf16 v[32:47], v[168:171], v[152:155], v[32:47]\n"
"ds_read_b64_tr_b16 v[168:169], v208 offset:25344\n"
"ds_read_b64_tr_b16 v[170:171], v208 offset:27392\n"
"v_pk_mul_f32 v[30:31], v[30:31], v[230:231]\n"
"v_cvt_pk_bf16_f32 v72, v16, v17\n"
"v_cvt_pk_bf16_f32 v73, v18, v19\n"
"v_cvt_pk_bf16_f32 v74, v20, v21\n"
"v_cvt_pk_bf16_f32 v75, v22, v23\n"
"ds_write_b128 v255, v[72:75] offset:2048\n"
"v_cvt_pk_bf16_f32 v76, v24, v25\n"
"s_waitcnt lgkmcnt(8)\n"
"v_mfma_f32_32x32x16_bf16 v[32:47], v[172:175], v[156:159], v[32:47]\n"
"ds_read_b64_tr_b16 v[172:173], v208 offset:29440\n"
"ds_read_b64_tr_b16 v[174:175], v208 offset:31488\n"
"v_cvt_pk_bf16_f32 v77, v26, v27\n"
"v_cvt_pk_bf16_f32 v78, v28, v29\n"
"v_cvt_pk_bf16_f32 v79, v30, v31\n"
"ds_write_b128 v255, v[76:79] offset:3072\n"
"s_waitcnt lgkmcnt(8)\n"
"v_mfma_f32_32x32x16_bf16 v[48:63], v[160:163], v[144:147], v[48:63]\n"
"s_waitcnt lgkmcnt(0)\n"
"s_barrier\n"
"v_mfma_f32_32x32x16_bf16 v[48:63], v[164:167], v[148:151], v[48:63]\n"
"s_add_u32 m0, s94, 40960\n"
"s_lshl_b32 s54, s74, 1\n"
"s_add_u32 s54, s54, 0\n"
"s_and_b32 s54, s54, 7\n"
"s_mul_i32 s54, s54, 147456\n"
"s_add_u32 s54, s54, s92\n"
"s_add_u32 s54, s54, 73728\n"
"buffer_load_dwordx4 v242, s[76:79], s54 offen lds\n"
"buffer_load_dwordx4 v243, s[76:79], s54 offen offset:1024 lds\n"
"v_pk_mul_f32 v[32:33], v[32:33], v[230:231]\n"
"v_pk_mul_f32 v[34:35], v[34:35], v[230:231]\n"
"v_pk_mul_f32 v[36:37], v[36:37], v[230:231]\n"
"v_pk_mul_f32 v[38:39], v[38:39], v[230:231]\n"
"v_pk_mul_f32 v[40:41], v[40:41], v[230:231]\n"
"v_pk_mul_f32 v[42:43], v[42:43], v[230:231]\n"
"v_pk_mul_f32 v[44:45], v[44:45], v[230:231]\n"
"v_mfma_f32_32x32x16_bf16 v[48:63], v[168:171], v[152:155], v[48:63]\n"
"buffer_load_dwordx4 v244, s[76:79], s54 offen offset:2048 lds\n"
"s_add_u32 m0, s94, 122880\n"
"s_lshl_b32 s54, s74, 1\n"
"s_add_u32 s54, s54, 1\n"
"s_and_b32 s54, s54, 7\n"
"s_mul_i32 s54, s54, 147456\n"
"s_add_u32 s54, s54, s92\n"
"s_add_u32 s54, s54, 73728\n"
"buffer_load_dwordx4 v242, s[76:79], s54 offen lds\n"
"v_pk_mul_f32 v[46:47], v[46:47], v[230:231]\n"
"v_cvt_pk_bf16_f32 v80, v32, v33\n"
"v_cvt_pk_bf16_f32 v81, v34, v35\n"
"v_cvt_pk_bf16_f32 v82, v36, v37\n"
"v_cvt_pk_bf16_f32 v83, v38, v39\n"
"ds_write_b128 v255, v[80:83] offset:40960\n"
"v_cvt_pk_bf16_f32 v84, v40, v41\n"
"v_mfma_f32_32x32x16_bf16 v[48:63], v[172:175], v[156:159], v[48:63]\n"
"buffer_load_dwordx4 v243, s[76:79], s54 offen offset:1024 lds\n"
"buffer_load_dwordx4 v244, s[76:79], s54 offen offset:2048 lds\n"
"v_cvt_pk_bf16_f32 v85, v42, v43\n"
"v_cvt_pk_bf16_f32 v86, v44, v45\n"
"v_cvt_pk_bf16_f32 v87, v46, v47\n"
"ds_write_b128 v255, v[84:87] offset:41984\n"
"buffer_store_dwordx4 v[64:67], v240, s[40:43], 0 offen sc1\n"
"buffer_store_dwordx4 v[68:71], v240, s[40:43], 0 offen offset:1024 sc1\n"
"buffer_store_dwordx4 v[72:75], v240, s[40:43], 0 offen offset:2048 sc1\n"
"buffer_store_dwordx4 v[76:79], v240, s[40:43], 0 offen offset:3072 sc1\n"
"buffer_store_dwordx4 v[80:83], v240, s[40:43], s55 offen sc1\n"
"buffer_store_dwordx4 v[84:87], v240, s[40:43], s55 offen offset:1024 sc1\n"
"v_pk_mul_f32 v[48:49], v[48:49], v[230:231]\n"
"v_pk_mul_f32 v[50:51], v[50:51], v[230:231]\n"
"v_pk_mul_f32 v[52:53], v[52:53], v[230:231]\n"
"v_pk_mul_f32 v[54:55], v[54:55], v[230:231]\n"
"v_pk_mul_f32 v[56:57], v[56:57], v[230:231]\n"
"v_pk_mul_f32 v[58:59], v[58:59], v[230:231]\n"
"v_pk_mul_f32 v[60:61], v[60:61], v[230:231]\n"
"v_pk_mul_f32 v[62:63], v[62:63], v[230:231]\n"
"v_cvt_pk_bf16_f32 v88, v48, v49\n"
"v_cvt_pk_bf16_f32 v89, v50, v51\n"
"v_cvt_pk_bf16_f32 v90, v52, v53\n"
"v_cvt_pk_bf16_f32 v91, v54, v55\n"
"ds_write_b128 v255, v[88:91] offset:43008\n"
"v_cvt_pk_bf16_f32 v92, v56, v57\n"
"v_cvt_pk_bf16_f32 v93, v58, v59\n"
"v_cvt_pk_bf16_f32 v94, v60, v61\n"
"v_cvt_pk_bf16_f32 v95, v62, v63\n"
"ds_write_b128 v255, v[92:95] offset:44032\n"
"buffer_store_dwordx4 v[88:91], v240, s[40:43], s55 offen offset:2048 sc1\n"
"buffer_store_dwordx4 v[92:95], v240, s[40:43], s55 offen offset:3072 sc1\n"
"s_waitcnt vmcnt(17)\n"
"s_waitcnt lgkmcnt(0)\n"
"s_barrier\n"
"s_cmp_lg_u32 s45, 0\n"
"s_cbranch_scc1 KF_NH1\n"
"ds_read_b128 v[120:123], v251 offset:0\n"
"ds_read_b64_tr_b16 v[96:97], v245 offset:0\n"
"ds_read_b64_tr_b16 v[98:99], v245 offset:512\n"
"ds_read_b64_tr_b16 v[100:101], v246 offset:0\n"
"ds_read_b64_tr_b16 v[102:103], v246 offset:512\n"
"ds_read_b64_tr_b16 v[104:105], v247 offset:0\n"
"ds_read_b64_tr_b16 v[106:107], v247 offset:512\n"
"s_waitcnt lgkmcnt(4)\n"
"v_mfma_f32_32x32x16_bf16 v[0:15], v[96:99], v[120:123], 0\n"
"ds_read_b128 v[124:127], v251 offset:1024\n"
"ds_read_b64_tr_b16 v[108:109], v245 offset:6144\n"
"ds_read_b64_tr_b16 v[110:111], v245 offset:6656\n"
"s_waitcnt lgkmcnt(5)\n"
"v_mfma_f32_32x32x16_bf16 v[16:31], v[100:103], v[120:123], 0\n"
"ds_read_b64_tr_b16 v[112:113], v246 offset:6144\n"
"ds_read_b64_tr_b16 v[114:115], v246 offset:6656\n"
"s_waitcnt lgkmcnt(5)\n"
"v_mfma_f32_32x32x16_bf16 v[32:47], v[104:107], v[120:123], 0\n"
"ds_read_b64_tr_b16 v[116:117], v247 offset:6144\n"
"ds_read_b64_tr_b16 v[118:119], v247 offset:6656\n"
"s_waitcnt lgkmcnt(4)\n"
"v_mfma_f32_32x32x16_bf16 v[0:15], v[108:111], v[124:127], v[0:15]\n"
"ds_read_b128 v[120:123], v251 offset:2048\n"
"ds_read_b64_tr_b16 v[96:97], v245 offset:12288\n"
"ds_read_b64_tr_b16 v[98:99], v245 offset:12800\n"
"s_waitcnt lgkmcnt(5)\n"
"v_mfma_f32_32x32x16_bf16 v[16:31], v[112:115], v[124:127], v[16:31]\n"
"ds_read_b64_tr_b16 v[100:101], v246 offset:12288\n"
"ds_read_b64_tr_b16 v[102:103], v246 offset:12800\n"
"s_waitcnt lgkmcnt(5)\n"
"v_mfma_f32_32x32x16_bf16 v[32:47], v[116:119], v[124:127], v[32:47]\n"
"ds_read_b64_tr_b16 v[104:105], v247 offset:12288\n"
"ds_read_b64_tr_b16 v[106:107], v247 offset:12800\n"
"s_waitcnt lgkmcnt(4)\n"
"v_mfma_f32_32x32x16_bf16 v[0:15], v[96:99], v[120:123], v[0:15]\n"
"ds_read_b128 v[124:127], v251 offset:3072\n"
"ds_read_b64_tr_b16 v[108:109], v245 offset:18432\n"
"ds_read_b64_tr_b16 v[110:111], v245 offset:18944\n"
"s_waitcnt lgkmcnt(5)\n"
"v_mfma_f32_32x32x16_bf16 v[16:31], v[100:103], v[120:123], v[16:31]\n"
"ds_read_b64_tr_b16 v[112:113], v246 offset:18432\n"
"ds_read_b64_tr_b16 v[114:115], v246 offset:18944\n"
"s_waitcnt lgkmcnt(5)\n"
"v_mfma_f32_32x32x16_bf16 v[32:47], v[104:107], v[120:123], v[32:47]\n"
"ds_read_b64_tr_b16 v[116:117], v247 offset:18432\n"
"ds_read_b64_tr_b16 v[118:119], v247 offset:18944\n"
"s_waitcnt vmcnt(11)\n"
"s_waitcnt lgkmcnt(0)\n"
"s_barrier\n"
"v_mfma_f32_32x32x16_bf16 v[0:15], v[108:111], v[124:127], v[0:15]\n"
"ds_read_b128 v[120:123], v251 offset:40960\n"
"ds_read_b64_tr_b16 v[96:97], v245 offset:40960\n"
"ds_read_b64_tr_b16 v[98:99], v245 offset:41472\n"
"v_mfma_f32_32x32x16_bf16 v[16:31], v[112:115], v[124:127], v[16:31]\n"
"ds_read_b64_tr_b16 v[100:101], v246 offset:40960\n"
"ds_read_b64_tr_b16 v[102:103], v246 offset:41472\n"
"v_mfma_f32_32x32x16_bf16 v[32:47], v[116:119], v[124:127], v[32:47]\n"
"ds_read_b64_tr_b16 v[104:105], v247 offset:40960\n"
"ds_read_b64_tr_b16 v[106:107], v247 offset:41472\n"
"s_add_u32 m0, s94, 0\n"
"s_lshl_b32 s54, s74, 1\n"
"s_add_u32 s54, s54, 2\n"
"s_and_b32 s54, s54, 7\n"
"s_mul_i32 s54, s54, 147456\n"
"s_add_u32 s54, s54, s92\n"
"buffer_load_dwordx4 v242, s[76:79], s54 offen lds\n"
"buffer_load_dwordx4 v243, s[76:79], s54 offen offset:1024 lds\n"
"s_waitcnt lgkmcnt(4)\n"
"v_mfma_f32_32x32x16_bf16 v[0:15], v[96:99], v[120:123], v[0:15]\n"
"ds_read_b128 v[124:127], v251 offset:41984\n"
"ds_read_b64_tr_b16 v[108:109], v245 offset:47104\n"
"ds_read_b64_tr_b16 v[110:111], v245 offset:47616\n"
"s_waitcnt lgkmcnt(5)\n"
"v_mfma_f32_32x32x16_bf16 v[16:31], v[100:103], v[120:123], v[16:31]\n"
"ds_read_b64_tr_b16 v[112:113], v246 offset:47104\n"
"ds_read_b64_tr_b16 v[114:115], v246 offset:47616\n"
"s_waitcnt lgkmcnt(5)\n"
"v_mfma_f32_32x32x16_bf16 v[32:47], v[104:107], v[120:123], v[32:47]\n"
"ds_read_b64_tr_b16 v[116:117], v247 offset:47104\n"
"ds_read_b64_tr_b16 v[118:119], v247 offset:47616\n"
"buffer_load_dwordx4 v244, s[76:79], s54 offen offset:2048 lds\n"
"s_waitcnt lgkmcnt(4)\n"
"v_mfma_f32_32x32x16_bf16 v[0:15], v[108:111], v[124:127], v[0:15]\n"
"ds_read_b128 v[120:123], v251 offset:43008\n"
"ds_read_b64_tr_b16 v[96:97], v245 offset:53248\n"
"ds_read_b64_tr_b16 v[98:99], v245 offset:53760\n"
"s_waitcnt lgkmcnt(5)\n"
"v_mfma_f32_32x32x16_bf16 v[16:31], v[112:115], v[124:127], v[16:31]\n"
"ds_read_b64_tr_b16 v[100:101], v246 offset:53248\n"
"ds_read_b64_tr_b16 v[102:103], v246 offset:53760\n"
"s_waitcnt lgkmcnt(5)\n"
"v_mfma_f32_32x32x16_bf16 v[32:47], v[116:119], v[124:127], v[32:47]\n"
"ds_read_b64_tr_b16 v[104:105], v247 offset:53248\n"
"ds_read_b64_tr_b16 v[106:107], v247 offset:53760\n"
"s_waitcnt lgkmcnt(4)\n"
"v_mfma_f32_32x32x16_bf16 v[0:15], v[96:99], v[120:123], v[0:15]\n"
"ds_read_b128 v[124:127], v251 offset:44032\n"
"ds_read_b64_tr_b16 v[108:109], v245 offset:59392\n"
"ds_read_b64_tr_b16 v[110:111], v245 offset:59904\n"
"s_waitcnt lgkmcnt(5)\n"
"v_mfma_f32_32x32x16_bf16 v[16:31], v[100:103], v[120:123], v[16:31]\n"
"ds_read_b64_tr_b16 v[112:113], v246 offset:59392\n"
"ds_read_b64_tr_b16 v[114:115], v246 offset:59904\n"
"s_waitcnt lgkmcnt(5)\n"
"v_mfma_f32_32x32x16_bf16 v[32:47], v[104:107], v[120:123], v[32:47]\n"
"ds_read_b64_tr_b16 v[116:117], v247 offset:59392\n"
"ds_read_b64_tr_b16 v[118:119], v247 offset:59904\n"
"s_waitcnt vmcnt(3)\n"
"s_waitcnt lgkmcnt(0)\n"
"s_barrier\n"
"s_cmp_lg_u32 s44, 0\n"
"s_cbranch_scc1 KF_WB\n"
"s_lshl_b32 s12, 1, s74\n"
"s_xor_b32 s12, s12, 15\n"
"s_mov_b32 s13, 0\n"
"s_mov_b64 exec, s[12:13]\n"
"global_store_dwordx2 v129, v[132:133], s[20:21] sc1\n"
"s_mov_b64 exec, -1\n"
"KF_WB:\n"
"v_mfma_f32_32x32x16_bf16 v[0:15], v[108:111], v[124:127], v[0:15]\n"
"ds_read_b128 v[120:123], v252 offset:0\n"
"ds_read_b64_tr_b16 v[96:97], v248 offset:0\n"
"ds_read_b64_tr_b16 v[98:99], v248 offset:512\n"
"v_mfma_f32_32x32x16_bf16 v[16:31], v[112:115], v[124:127], v[16:31]\n"
"ds_read_b64_tr_b16 v[100:101], v249 offset:0\n"
"ds_read_b64_tr_b16 v[102:103], v249 offset:512\n"
"v_mfma_f32_32x32x16_bf16 v[32:47], v[116:119], v[124:127], v[32:47]\n"
"ds_read_b64_tr_b16 v[104:105], v250 offset:0\n"
"ds_read_b64_tr_b16 v[106:107], v250 offset:512\n"
"s_add_u32 m0, s94, 40960\n"
"s_lshl_b32 s54, s74, 1\n"
"s_add_u32 s54, s54, 2\n"
"s_and_b32 s54, s54, 7\n"
"s_mul_i32 s54, s54, 147456\n"
"s_add_u32 s54, s54, s92\n"
"s_add_u32 s54, s54, 73728\n"
"buffer_load_dwordx4 v242, s[76:79], s54 offen lds\n"
"buffer_load_dwordx4 v243, s[76:79], s54 offen offset:1024 lds\n"
"s_waitcnt lgkmcnt(4)\n"
"v_mfma_f32_32x32x16_bf16 v[0:15], v[96:99], v[120:123], v[0:15]\n"
"ds_read_b128 v[124:127], v252 offset:1024\n"
"ds_read_b64_tr_b16 v[108:109], v248 offset:6144\n"
"ds_read_b64_tr_b16 v[110:111], v248 offset:6656\n"
"s_waitcnt lgkmcnt(5)\n"
"v_mfma_f32_32x32x16_bf16 v[16:31], v[100:103], v[120:123], v[16:31]\n"
"ds_read_b64_tr_b16 v[112:113], v249 offset:6144\n"
"ds_read_b64_tr_b16 v[114:115], v249 offset:6656\n"
"s_waitcnt lgkmcnt(5)\n"
"v_mfma_f32_32x32x16_bf16 v[32:47], v[104:107], v[120:123], v[32:47]\n"
"ds_read_b64_tr_b16 v[116:117], v250 offset:6144\n"
"ds_read_b64_tr_b16 v[118:119], v250 offset:6656\n"
"buffer_load_dwordx4 v244, s[76:79], s54 offen offset:2048 lds\n"
"s_waitcnt lgkmcnt(4)\n"
"v_mfma_f32_32x32x16_bf16 v[0:15], v[108:111], v[124:127], v[0:15]\n"
"ds_read_b128 v[120:123], v252 offset:2048\n"
"ds_read_b64_tr_b16 v[96:97], v248 offset:12288\n"
"ds_read_b64_tr_b16 v[98:99], v248 offset:12800\n"
"s_waitcnt lgkmcnt(5)\n"
"v_mfma_f32_32x32x16_bf16 v[16:31], v[112:115], v[124:127], v[16:31]\n"
"ds_read_b64_tr_b16 v[100:101], v249 offset:12288\n"
"ds_read_b64_tr_b16 v[102:103], v249 offset:12800\n"
"s_waitcnt lgkmcnt(5)\n"
"v_mfma_f32_32x32x16_bf16 v[32:47], v[116:119], v[124:127], v[32:47]\n"
"ds_read_b64_tr_b16 v[104:105], v250 offset:12288\n"
"ds_read_b64_tr_b16 v[106:107], v250 offset:12800\n"
"s_waitcnt lgkmcnt(4)\n"
"v_mfma_f32_32x32x16_bf16 v[0:15], v[96:99], v[120:123], v[0:15]\n"
"ds_read_b128 v[124:127], v252 offset:3072\n"
"ds_read_b64_tr_b16 v[108:109], v248 offset:18432\n"
"ds_read_b64_tr_b16 v[110:111], v248 offset:18944\n"
"s_waitcnt lgkmcnt(5)\n"
"v_mfma_f32_32x32x16_bf16 v[16:31], v[100:103], v[120:123], v[16:31]\n"
"ds_read_b64_tr_b16 v[112:113], v249 offset:18432\n"
"ds_read_b64_tr_b16 v[114:115], v249 offset:18944\n"
"s_waitcnt lgkmcnt(5)\n"
"v_mfma_f32_32x32x16_bf16 v[32:47], v[104:107], v[120:123], v[32:47]\n"
"ds_read_b64_tr_b16 v[116:117], v250 offset:18432\n"
"ds_read_b64_tr_b16 v[118:119], v250 offset:18944\n"
"s_waitcnt lgkmcnt(0)\n"
"s_barrier\n"
"v_mfma_f32_32x32x16_bf16 v[0:15], v[108:111], v[124:127], v[0:15]\n"
"ds_read_b128 v[120:123], v252 offset:40960\n"
"ds_read_b64_tr_b16 v[96:97], v248 offset:40960\n"
"ds_read_b64_tr_b16 v[98:99], v248 offset:41472\n"
"v_mfma_f32_32x32x16_bf16 v[16:31], v[112:115], v[124:127], v[16:31]\n"
"ds_read_b64_tr_b16 v[100:101], v249 offset:40960\n"
"ds_read_b64_tr_b16 v[102:103], v249 offset:41472\n"
"v_mfma_f32_32x32x16_bf16 v[32:47], v[116:119], v[124:127], v[32:47]\n"
"ds_read_b64_tr_b16 v[104:105], v250 offset:40960\n"
"ds_read_b64_tr_b16 v[106:107], v250 offset:41472\n"
"s_add_u32 m0, s94, 81920\n"
"s_lshl_b32 s54, s74, 1\n"
"s_add_u32 s54, s54, 3\n"
"s_and_b32 s54, s54, 7\n"
"s_mul_i32 s54, s54, 147456\n"
"s_add_u32 s54, s54, s92\n"
"buffer_load_dwordx4 v242, s[76:79], s54 offen lds\n"
"buffer_load_dwordx4 v243, s[76:79], s54 offen offset:1024 lds\n"
"s_waitcnt lgkmcnt(4)\n"
"v_mfma_f32_32x32x16_bf16 v[0:15], v[96:99], v[120:123], v[0:15]\n"
"ds_read_b128 v[124:127], v252 offset:41984\n"
"ds_read_b64_tr_b16 v[108:109], v248 offset:47104\n"
"ds_read_b64_tr_b16 v[110:111], v248 offset:47616\n"
"s_waitcnt lgkmcnt(5)\n"
"v_mfma_f32_32x32x16_bf16 v[16:31], v[100:103], v[120:123], v[16:31]\n"
"ds_read_b64_tr_b16 v[112:113], v249 offset:47104\n"
"ds_read_b64_tr_b16 v[114:115], v249 offset:47616\n"
"s_waitcnt lgkmcnt(5)\n"
"v_mfma_f32_32x32x16_bf16 v[32:47], v[104:107], v[120:123], v[32:47]\n"
"ds_read_b64_tr_b16 v[116:117], v250 offset:47104\n"
"ds_read_b64_tr_b16 v[118:119], v250 offset:47616\n"
"buffer_load_dwordx4 v244, s[76:79], s54 offen offset:2048 lds\n"
"s_waitcnt lgkmcnt(4)\n"
"v_mfma_f32_32x32x16_bf16 v[0:15], v[108:111], v[124:127], v[0:15]\n"
"ds_read_b128 v[120:123], v252 offset:43008\n"
"ds_read_b64_tr_b16 v[96:97], v248 offset:53248\n"
"ds_read_b64_tr_b16 v[98:99], v248 offset:53760\n"
"s_waitcnt lgkmcnt(5)\n"
"v_mfma_f32_32x32x16_bf16 v[16:31], v[112:115], v[124:127], v[16:31]\n"
"ds_read_b64_tr_b16 v[100:101], v249 offset:53248\n"
"ds_read_b64_tr_b16 v[102:103], v249 offset:53760\n"
"s_waitcnt lgkmcnt(5)\n"
"v_mfma_f32_32x32x16_bf16 v[32:47], v[116:119], v[124:127], v[32:47]\n"
"ds_read_b64_tr_b16 v[104:105], v250 offset:53248\n"
"ds_read_b64_tr_b16 v[106:107], v250 offset:53760\n"
"s_waitcnt lgkmcnt(4)\n"
"v_mfma_f32_32x32x16_bf16 v[0:15], v[96:99], v[120:123], v[0:15]\n"
"ds_read_b128 v[124:127], v252 offset:44032\n"
"ds_read_b64_tr_b16 v[108:109], v248 offset:59392\n"
"ds_read_b64_tr_b16 v[110:111], v248 offset:59904\n"
"s_waitcnt lgkmcnt(5)\n"
"v_mfma_f32_32x32x16_bf16 v[16:31], v[100:103], v[120:123], v[16:31]\n"
"ds_read_b64_tr_b16 v[112:113], v249 offset:59392\n"
"ds_read_b64_tr_b16 v[114:115], v249 offset:59904\n"
"s_waitcnt lgkmcnt(5)\n"
"v_mfma_f32_32x32x16_bf16 v[32:47], v[104:107], v[120:123], v[32:47]\n"
"ds_read_b64_tr_b16 v[116:117], v250 offset:59392\n"
"ds_read_b64_tr_b16 v[118:119], v250 offset:59904\n"
"s_cmp_lg_u32 s44, 0\n"
"s_cbranch_scc1 KF_WC\n"
"s_mov_b32 s58, 0\n"
"KF_POLLG:\n"
"s_mov_b64 exec, 0xff\n"
"global_load_dwordx2 v[136:137], v130, s[20:21] sc1\n"
"global_load_dwordx2 v[134:135], v128, s[20:21] sc1\n"
"s_mov_b64 exec, -1\n"
"s_waitcnt vmcnt(0)\n"
"s_lshl_b32 s12, 1, s74\n"
"s_xor_b32 s12, s12, 15\n"
"s_mov_b32 s13, 0\n"
"v_cmp_ne_u64 vcc, v[134:135], v[132:133]\n"
"s_nop 4\n"
"s_and_b64 s[12:13], s[12:13], vcc\n"
"s_cbranch_scc0 KF_RCGa\n"
"v_mov_b32 v132, v134\n"
"v_mov_b32 v133, v135\n"
"s_mov_b64 exec, s[12:13]\n"
"global_store_dwordx2 v129, v[132:133], s[20:21] sc1\n"
"s_mov_b64 exec, -1\n"
"KF_RCGa:\n"
"s_lshl_b32 s12, 1, s74\n"
"s_xor_b32 s12, s12, 15\n"
"s_mov_b32 s13, 0\n"
"v_cmp_eq_u64 vcc, s[96:97], v[136:137]\n"
"s_nop 4\n"
"s_and_b64 vcc, vcc, s[12:13]\n"
"s_cmp_eq_u64 vcc, s[12:13]\n"
"s_cbranch_scc1 KF_GOTG\n"
"s_add_u32 s58, s58, 1\n"
"s_cmp_lt_u32 s58, 0x200000\n"
"s_cbranch_scc1 KF_POLLG\n"
"KF_GOTG:\n"
"s_mov_b64 exec, 0xff\n"
"global_load_dwordx2 v[134:135], v128, s[20:21] sc1\n"
"s_mov_b64 exec, -1\n"
"KF_WC:\n"
"s_waitcnt vmcnt(6)\n"
"s_waitcnt lgkmcnt(0)\n"
"s_barrier\n"
"s_add_u32 m0, s95, 0\n"
"s_lshl_b32 s55, s74, 1\n"
"s_add_u32 s55, s55, 2\n"
"s_and_b32 s55, s55, 7\n"
"s_lshl_b32 s55, s55, 15\n"
"s_add_u32 s55, s55, s93\n"
"buffer_load_dwordx4 v240, s[80:83], s55 offen sc1 lds\n"
"buffer_load_dwordx4 v240, s[80:83], s55 offen offset:1024 sc1 lds\n"
"v_mfma_f32_32x32x16_bf16 v[0:15], v[108:111], v[124:127], v[0:15]\n"
"s_add_u32 m0, s95, 40960\n"
"s_lshl_b32 s55, s74, 1\n"
"s_add_u32 s55, s55, 2\n"
"s_and_b32 s55, s55, 7\n"
"s_lshl_b32 s55, s55, 15\n"
"s_add_u32 s55, s55, s93\n"
"s_add_u32 s55, s55, 4096\n"
"buffer_load_dwordx4 v240, s[80:83], s55 offen sc1 lds\n"
"buffer_load_dwordx4 v240, s[80:83], s55 offen offset:1024 sc1 lds\n"
"v_mfma_f32_32x32x16_bf16 v[16:31], v[112:115], v[124:127], v[16:31]\n"
"v_mfma_f32_32x32x16_bf16 v[32:47], v[116:119], v[124:127], v[32:47]\n"
"s_add_u32 m0, s94, 122880\n"
"s_lshl_b32 s54, s74, 1\n"
"s_add_u32 s54, s54, 3\n"
"s_and_b32 s54, s54, 7\n"
"s_mul_i32 s54, s54, 147456\n"
"s_add_u32 s54, s54, s92\n"
"s_add_u32 s54, s54, 73728\n"
"buffer_load_dwordx4 v242, s[76:79], s54 offen lds\n"
"buffer_load_dwordx4 v243, s[76:79], s54 offen offset:1024 lds\n"
"s_waitcnt vmcnt(4)\n"
"s_waitcnt lgkmcnt(0)\n"
"s_barrier\n"
"s_cmp_lg_u32 s44, 0\n"
"s_cbranch_scc1 KF_WD\n"
"s_lshl_b32 s12, 1, s74\n"
"s_xor_b32 s12, s12, 15\n"
"s_mov_b32 s13, 0\n"
"v_cmp_ne_u64 vcc, v[134:135], v[132:133]\n"
"s_nop 4\n"
"s_and_b64 s[12:13], s[12:13], vcc\n"
"s_cbranch_scc0 KF_RCGb\n"
"v_mov_b32 v132, v134\n"
"v_mov_b32 v133, v135\n"
"s_mov_b64 exec, s[12:13]\n"
"global_store_dwordx2 v129, v[132:133], s[20:21] sc1\n"
"s_mov_b64 exec, -1\n"
"KF_RCGb:\n"
"KF_WD:\n"
"ds_read_b128 v[120:123], v251 offset:0\n"
"ds_read_b64_tr_b16 v[96:97], v245 offset:0\n"
"ds_read_b64_tr_b16 v[98:99], v245 offset:512\n"
"ds_read_b64_tr_b16 v[100:101], v246 offset:0\n"
"ds_read_b64_tr_b16 v[102:103], v246 offset:512\n"
"ds_read_b64_tr_b16 v[104:105], v247 offset:0\n"
"ds_read_b64_tr_b16 v[106:107], v247 offset:512\n"
"s_waitcnt lgkmcnt(4)\n"
"v_mfma_f32_32x32x16_bf16 v[0:15], v[96:99], v[120:123], v[0:15]\n"
"ds_read_b128 v[124:127], v251 offset:1024\n"
"ds_read_b64_tr_b16 v[108:109], v245 offset:6144\n"
"ds_read_b64_tr_b16 v[110:111], v245 offset:6656\n"
"s_waitcnt lgkmcnt(5)\n"
"v_mfma_f32_32x32x16_bf16 v[16:31], v[100:103], v[120:123], v[16:31]\n"
"ds_read_b64_tr_b16 v[112:113], v246 offset:6144\n"
"ds_read_b64_tr_b16 v[114:115], v246 offset:6656\n"
"s_waitcnt lgkmcnt(5)\n"
"v_mfma_f32_32x32x16_bf16 v[32:47], v[104:107], v[120:123], v[32:47]\n"
"ds_read_b64_tr_b16 v[116:117], v247 offset:6144\n"
"ds_read_b64_tr_b16 v[118:119], v247 offset:6656\n"
"buffer_load_dwordx4 v244, s[76:79], s54 offen offset:2048 lds\n"
"s_waitcnt lgkmcnt(4)\n"
"v_mfma_f32_32x32x16_bf16 v[0:15], v[108:111], v[124:127], v[0:15]\n"
"ds_read_b128 v[120:123], v251 offset:2048\n"
"ds_read_b64_tr_b16 v[96:97], v245 offset:12288\n"
"ds_read_b64_tr_b16 v[98:99], v245 offset:12800\n"
"s_waitcnt lgkmcnt(5)\n"
"v_mfma_f32_32x32x16_bf16 v[16:31], v[112:115], v[124:127], v[16:31]\n"
"ds_read_b64_tr_b16 v[100:101], v246 offset:12288\n"
"ds_read_b64_tr_b16 v[102:103], v246 offset:12800\n"
"s_waitcnt lgkmcnt(5)\n"
"v_mfma_f32_32x32x16_bf16 v[32:47], v[116:119], v[124:127], v[32:47]\n"
"ds_read_b64_tr_b16 v[104:105], v247 offset:12288\n"
"ds_read_b64_tr_b16 v[106:107], v247 offset:12800\n"
"s_add_u32 m0, s95, 81920\n"
"s_lshl_b32 s55, s74, 1\n"
"s_add_u32 s55, s55, 3\n"
"s_and_b32 s55, s55, 7\n"
"s_lshl_b32 s55, s55, 15\n"
"s_add_u32 s55, s55, s93\n"
"buffer_load_dwordx4 v240, s[80:83], s55 offen sc1 lds\n"
"buffer_load_dwordx4 v240, s[80:83], s55 offen offset:1024 sc1 lds\n"
"s_waitcnt lgkmcnt(4)\n"
"v_mfma_f32_32x32x16_bf16 v[0:15], v[96:99], v[120:123], v[0:15]\n"
"ds_read_b128 v[124:127], v251 offset:3072\n"
"ds_read_b64_tr_b16 v[108:109], v245 offset:18432\n"
"ds_read_b64_tr_b16 v[110:111], v245 offset:18944\n"
"s_waitcnt lgkmcnt(5)\n"
"v_mfma_f32_32x32x16_bf16 v[16:31], v[100:103], v[120:123], v[16:31]\n"
"ds_read_b64_tr_b16 v[112:113], v246 offset:18432\n"
"ds_read_b64_tr_b16 v[114:115], v246 offset:18944\n"
"s_waitcnt lgkmcnt(5)\n"
"v_mfma_f32_32x32x16_bf16 v[32:47], v[104:107], v[120:123], v[32:47]\n"
"ds_read_b64_tr_b16 v[116:117], v247 offset:18432\n"
"ds_read_b64_tr_b16 v[118:119], v247 offset:18944\n"
"s_add_u32 m0, s95, 122880\n"
"s_lshl_b32 s55, s74, 1\n"
"s_add_u32 s55, s55, 3\n"
"s_and_b32 s55, s55, 7\n"
"s_lshl_b32 s55, s55, 15\n"
"s_add_u32 s55, s55, s93\n"
"s_add_u32 s55, s55, 4096\n"
"buffer_load_dwordx4 v240, s[80:83], s55 offen sc1 lds\n"
"buffer_load_dwordx4 v240, s[80:83], s55 offen offset:1024 sc1 lds\n"
"s_waitcnt vmcnt(7)\n"
"s_waitcnt lgkmcnt(0)\n"
"s_barrier\n"
"v_mfma_f32_32x32x16_bf16 v[0:15], v[108:111], v[124:127], v[0:15]\n"
"ds_read_b128 v[120:123], v251 offset:40960\n"
"ds_read_b64_tr_b16 v[96:97], v245 offset:40960\n"
"ds_read_b64_tr_b16 v[98:99], v245 offset:41472\n"
"v_mfma_f32_32x32x16_bf16 v[16:31], v[112:115], v[124:127], v[16:31]\n"
"ds_read_b64_tr_b16 v[100:101], v246 offset:40960\n"
"ds_read_b64_tr_b16 v[102:103], v246 offset:41472\n"
"v_mfma_f32_32x32x16_bf16 v[32:47], v[116:119], v[124:127], v[32:47]\n"
"ds_read_b64_tr_b16 v[104:105], v247 offset:40960\n"
"ds_read_b64_tr_b16 v[106:107], v247 offset:41472\n"
"s_add_u32 m0, s94, 0\n"
"s_lshl_b32 s54, s74, 1\n"
"s_add_u32 s54, s54, 4\n"
"s_and_b32 s54, s54, 7\n"
"s_mul_i32 s54, s54, 147456\n"
"s_add_u32 s54, s54, s92\n"
"buffer_load_dwordx4 v242, s[76:79], s54 offen lds\n"
"buffer_load_dwordx4 v243, s[76:79], s54 offen offset:1024 lds\n"
"s_waitcnt lgkmcnt(4)\n"
"v_mfma_f32_32x32x16_bf16 v[0:15], v[96:99], v[120:123], v[0:15]\n"
"ds_read_b128 v[124:127], v251 offset:41984\n"
"ds_read_b64_tr_b16 v[108:109], v245 offset:47104\n"
"ds_read_b64_tr_b16 v[110:111], v245 offset:47616\n"
"s_waitcnt lgkmcnt(5)\n"
"v_mfma_f32_32x32x16_bf16 v[16:31], v[100:103], v[120:123], v[16:31]\n"
"ds_read_b64_tr_b16 v[112:113], v246 offset:47104\n"
"ds_read_b64_tr_b16 v[114:115], v246 offset:47616\n"
"s_waitcnt lgkmcnt(5)\n"
"v_mfma_f32_32x32x16_bf16 v[32:47], v[104:107], v[120:123], v[32:47]\n"
"ds_read_b64_tr_b16 v[116:117], v247 offset:47104\n"
"ds_read_b64_tr_b16 v[118:119], v247 offset:47616\n"
"buffer_load_dwordx4 v244, s[76:79], s54 offen offset:2048 lds\n"
"s_waitcnt lgkmcnt(4)\n"
"v_mfma_f32_32x32x16_bf16 v[0:15], v[108:111], v[124:127], v[0:15]\n"
"ds_read_b128 v[120:123], v251 offset:43008\n"
"ds_read_b64_tr_b16 v[96:97], v245 offset:53248\n"
"ds_read_b64_tr_b16 v[98:99], v245 offset:53760\n"
"s_waitcnt lgkmcnt(5)\n"
"v_mfma_f32_32x32x16_bf16 v[16:31], v[112:115], v[124:127], v[16:31]\n"
"ds_read_b64_tr_b16 v[100:101], v246 offset:53248\n"
"ds_read_b64_tr_b16 v[102:103], v246 offset:53760\n"
"s_waitcnt lgkmcnt(5)\n"
"v_mfma_f32_32x32x16_bf16 v[32:47], v[116:119], v[124:127], v[32:47]\n"
"ds_read_b64_tr_b16 v[104:105], v247 offset:53248\n"
"ds_read_b64_tr_b16 v[106:107], v247 offset:53760\n"
"s_add_u32 m0, s95, 0\n"
"s_lshl_b32 s55, s74, 1\n"
"s_add_u32 s55, s55, 4\n"
"s_and_b32 s55, s55, 7\n"
"s_lshl_b32 s55, s55, 15\n"
"s_add_u32 s55, s55, s93\n"
"buffer_load_dwordx4 v240, s[80:83], s55 offen sc1 lds\n"
"s_waitcnt lgkmcnt(4)\n"
"v_mfma_f32_32x32x16_bf16 v[0:15], v[96:99], v[120:123], v[0:15]\n"
"ds_read_b128 v[124:127], v251 offset:44032\n"
"ds_read_b64_tr_b16 v[108:109], v245 offset:59392\n"
"ds_read_b64_tr_b16 v[110:111], v245 offset:59904\n"
"s_waitcnt lgkmcnt(5)\n"
"v_mfma_f32_32x32x16_bf16 v[16:31], v[100:103], v[120:123], v[16:31]\n"
"ds_read_b64_tr_b16 v[112:113], v246 offset:59392\n"
"ds_read_b64_tr_b16 v[114:115], v246 offset:59904\n"
"s_waitcnt lgkmcnt(5)\n"
"v_mfma_f32_32x32x16_bf16 v[32:47], v[104:107], v[120:123], v[32:47]\n"
"ds_read_b64_tr_b16 v[116:117], v247 offset:59392\n"
"ds_read_b64_tr_b16 v[118:119], v247 offset:59904\n"
"buffer_load_dwordx4 v240, s[80:83], s55 offen offset:1024 sc1 lds\n"
"s_waitcnt vmcnt(7)\n"
"s_waitcnt lgkmcnt(0)\n"
"s_barrier\n"
"v_mfma_f32_32x32x16_bf16 v[0:15], v[108:111], v[124:127], v[0:15]\n"
"ds_read_b128 v[120:123], v252 offset:0\n"
"ds_read_b64_tr_b16 v[96:97], v248 offset:0\n"
"ds_read_b64_tr_b16 v[98:99], v248 offset:512\n"
"v_mfma_f32_32x32x16_bf16 v[16:31], v[112:115], v[124:127], v[16:31]\n"
"ds_read_b64_tr_b16 v[100:101], v249 offset:0\n"
"ds_read_b64_tr_b16 v[102:103], v249 offset:512\n"
"v_mfma_f32_32x32x16_bf16 v[32:47], v[116:119], v[124:127], v[32:47]\n"
"ds_read_b64_tr_b16 v[104:105], v250 offset:0\n"
"ds_read_b64_tr_b16 v[106:107], v250 offset:512\n"
"s_add_u32 m0, s94, 40960\n"
"s_lshl_b32 s54, s74, 1\n"
"s_add_u32 s54, s54, 4\n"
"s_and_b32 s54, s54, 7\n"
"s_mul_i32 s54, s54, 147456\n"
"s_add_u32 s54, s54, s92\n"
"s_add_u32 s54, s54, 73728\n"
"buffer_load_dwordx4 v242, s[76:79], s54 offen lds\n"
"buffer_load_dwordx4 v243, s[76:79], s54 offen offset:1024 lds\n"
"s_waitcnt lgkmcnt(4)\n"
"v_mfma_f32_32x32x16_bf16 v[0:15], v[96:99], v[120:123], v[0:15]\n"
"ds_read_b128 v[124:127], v252 offset:1024\n"
"ds_read_b64_tr_b16 v[108:109], v248 offset:6144\n"
"ds_read_b64_tr_b16 v[110:111], v248 offset:6656\n"
"s_waitcnt lgkmcnt(5)\n"
"v_mfma_f32_32x32x16_bf16 v[16:31], v[100:103], v[120:123], v[16:31]\n"
"ds_read_b64_tr_b16 v[112:113], v249 offset:6144\n"
"ds_read_b64_tr_b16 v[114:115], v249 offset:6656\n"
"s_waitcnt lgkmcnt(5)\n"
"v_mfma_f32_32x32x16_bf16 v[32:47], v[104:107], v[120:123], v[32:47]\n"
"ds_read_b64_tr_b16 v[116:117], v250 offset:6144\n"
"ds_read_b64_tr_b16 v[118:119], v250 offset:6656\n"
"buffer_load_dwordx4 v244, s[76:79], s54 offen offset:2048 lds\n"
"s_waitcnt lgkmcnt(4)\n"
"v_mfma_f32_32x32x16_bf16 v[0:15], v[108:111], v[124:127], v[0:15]\n"
"ds_read_b128 v[120:123], v252 offset:2048\n"
"ds_read_b64_tr_b16 v[96:97], v248 offset:12288\n"
"ds_read_b64_tr_b16 v[98:99], v248 offset:12800\n"
"s_waitcnt lgkmcnt(5)\n"
"v_mfma_f32_32x32x16_bf16 v[16:31], v[112:115], v[124:127], v[16:31]\n"
"ds_read_b64_tr_b16 v[100:101], v249 offset:12288\n"
"ds_read_b64_tr_b16 v[102:103], v249 offset:12800\n"
"s_waitcnt lgkmcnt(5)\n"
"v_mfma_f32_32x32x16_bf16 v[32:47], v[116:119], v[124:127], v[32:47]\n"
"ds_read_b64_tr_b16 v[104:105], v250 offset:12288\n"
"ds_read_b64_tr_b16 v[106:107], v250 offset:12800\n"
"s_add_u32 m0, s95, 40960\n"
"s_lshl_b32 s55, s74, 1\n"
"s_add_u32 s55, s55, 4\n"
"s_and_b32 s55, s55, 7\n"
"s_lshl_b32 s55, s55, 15\n"
"s_add_u32 s55, s55, s93\n"
"s_add_u32 s55, s55, 4096\n"
"buffer_load_dwordx4 v240, s[80:83], s55 offen sc1 lds\n"
"s_waitcnt lgkmcnt(4)\n"
"v_mfma_f32_32x32x16_bf16 v[0:15], v[96:99], v[120:123], v[0:15]\n"
"ds_read_b128 v[124:127], v252 offset:3072\n"
"ds_read_b64_tr_b16 v[108:109], v248 offset:18432\n"
"ds_read_b64_tr_b16 v[110:111], v248 offset:18944\n"
"s_waitcnt lgkmcnt(5)\n"
"v_mfma_f32_32x32x16_bf16 v[16:31], v[100:103], v[120:123], v[16:31]\n"
"ds_read_b64_tr_b16 v[112:113], v249 offset:18432\n"
"ds_read_b64_tr_b16 v[114:115], v249 offset:18944\n"
"s_waitcnt lgkmcnt(5)\n"
"v_mfma_f32_32x32x16_bf16 v[32:47], v[104:107], v[120:123], v[32:47]\n"
"ds_read_b64_tr_b16 v[116:117], v250 offset:18432\n"
"ds_read_b64_tr_b16 v[118:119], v250 offset:18944\n"
"buffer_load_dwordx4 v240, s[80:83], s55 offen offset:1024 sc1 lds\n"
"s_waitcnt vmcnt(10)\n"
"s_waitcnt lgkmcnt(0)\n"
"s_barrier\n"
"v_mfma_f32_32x32x16_bf16 v[0:15], v[108:111], v[124:127], v[0:15]\n"
"ds_read_b128 v[120:123], v252 offset:40960\n"
"ds_read_b64_tr_b16 v[96:97], v248 offset:40960\n"
"ds_read_b64_tr_b16 v[98:99], v248 offset:41472\n"
"v_mfma_f32_32x32x16_bf16 v[16:31], v[112:115], v[124:127], v[16:31]\n"
"ds_read_b64_tr_b16 v[100:101], v249 offset:40960\n"
"ds_read_b64_tr_b16 v[102:103], v249 offset:41472\n"
"v_mfma_f32_32x32x16_bf16 v[32:47], v[116:119], v[124:127], v[32:47]\n"
"ds_read_b64_tr_b16 v[104:105], v250 offset:40960\n"
"ds_read_b64_tr_b16 v[106:107], v250 offset:41472\n"
"s_add_u32 m0, s94, 81920\n"
"s_lshl_b32 s54, s74, 1\n"
"s_add_u32 s54, s54, 5\n"
"s_and_b32 s54, s54, 7\n"
"s_mul_i32 s54, s54, 147456\n"
"s_add_u32 s54, s54, s92\n"
"buffer_load_dwordx4 v242, s[76:79], s54 offen lds\n"
"buffer_load_dwordx4 v243, s[76:79], s54 offen offset:1024 lds\n"
"s_waitcnt lgkmcnt(4)\n"
"v_mfma_f32_32x32x16_bf16 v[0:15], v[96:99], v[120:123], v[0:15]\n"
"ds_read_b128 v[124:127], v252 offset:41984\n"
"ds_read_b64_tr_b16 v[108:109], v248 offset:47104\n"
"ds_read_b64_tr_b16 v[110:111], v248 offset:47616\n"
"s_waitcnt lgkmcnt(5)\n"
"v_mfma_f32_32x32x16_bf16 v[16:31], v[100:103], v[120:123], v[16:31]\n"
"ds_read_b64_tr_b16 v[112:113], v249 offset:47104\n"
"ds_read_b64_tr_b16 v[114:115], v249 offset:47616\n"
"s_waitcnt lgkmcnt(5)\n"
"v_mfma_f32_32x32x16_bf16 v[32:47], v[104:107], v[120:123], v[32:47]\n"
"ds_read_b64_tr_b16 v[116:117], v250 offset:47104\n"
"ds_read_b64_tr_b16 v[118:119], v250 offset:47616\n"
"buffer_load_dwordx4 v244, s[76:79], s54 offen offset:2048 lds\n"
"s_waitcnt lgkmcnt(4)\n"
"v_mfma_f32_32x32x16_bf16 v[0:15], v[108:111], v[124:127], v[0:15]\n"
"ds_read_b128 v[120:123], v252 offset:43008\n"
"ds_read_b64_tr_b16 v[96:97], v248 offset:53248\n"
"ds_read_b64_tr_b16 v[98:99], v248 offset:53760\n"
"s_waitcnt lgkmcnt(5)\n"
"v_mfma_f32_32x32x16_bf16 v[16:31], v[112:115], v[124:127], v[16:31]\n"
"ds_read_b64_tr_b16 v[100:101], v249 offset:53248\n"
"ds_read_b64_tr_b16 v[102:103], v249 offset:53760\n"
"s_waitcnt lgkmcnt(5)\n"
"v_mfma_f32_32x32x16_bf16 v[32:47], v[116:119], v[124:127], v[32:47]\n"
"ds_read_b64_tr_b16 v[104:105], v250 offset:53248\n"
"ds_read_b64_tr_b16 v[106:107], v250 offset:53760\n"
"s_add_u32 m0, s95, 81920\n"
"s_lshl_b32 s55, s74, 1\n"
"s_add_u32 s55, s55, 5\n"
"s_and_b32 s55, s55, 7\n"
"s_lshl_b32 s55, s55, 15\n"
"s_add_u32 s55, s55, s93\n"
"buffer_load_dwordx4 v240, s[80:83], s55 offen sc1 lds\n"
"s_waitcnt lgkmcnt(4)\n"
"v_mfma_f32_32x32x16_bf16 v[0:15], v[96:99], v[120:123], v[0:15]\n"
"ds_read_b128 v[124:127], v252 offset:44032\n"
"ds_read_b64_tr_b16 v[108:109], v248 offset:59392\n"
"ds_read_b64_tr_b16 v[110:111], v248 offset:59904\n"
"s_waitcnt lgkmcnt(5)\n"
"v_mfma_f32_32x32x16_bf16 v[16:31], v[100:103], v[120:123], v[16:31]\n"
"ds_read_b64_tr_b16 v[112:113], v249 offset:59392\n"
"ds_read_b64_tr_b16 v[114:115], v249 offset:59904\n"
"s_waitcnt lgkmcnt(5)\n"
"v_mfma_f32_32x32x16_bf16 v[32:47], v[104:107], v[120:123], v[32:47]\n"
"ds_read_b64_tr_b16 v[116:117], v250 offset:59392\n"
"ds_read_b64_tr_b16 v[118:119], v250 offset:59904\n"
"buffer_load_dwordx4 v240, s[80:83], s55 offen offset:1024 sc1 lds\n"
"s_waitcnt vmcnt(10)\n"
"s_waitcnt lgkmcnt(0)\n"
"s_barrier\n"
"v_mfma_f32_32x32x16_bf16 v[0:15], v[108:111], v[124:127], v[0:15]\n"
"ds_read_b128 v[120:123], v251 offset:0\n"
"ds_read_b64_tr_b16 v[96:97], v245 offset:0\n"
"ds_read_b64_tr_b16 v[98:99], v245 offset:512\n"
"v_mfma_f32_32x32x16_bf16 v[16:31], v[112:115], v[124:127], v[16:31]\n"
"ds_read_b64_tr_b16 v[100:101], v246 offset:0\n"
"ds_read_b64_tr_b16 v[102:103], v246 offset:512\n"
"v_mfma_f32_32x32x16_bf16 v[32:47], v[116:119], v[124:127], v[32:47]\n"
"ds_read_b64_tr_b16 v[104:105], v247 offset:0\n"
"ds_read_b64_tr_b16 v[106:107], v247 offset:512\n"
"s_add_u32 m0, s94, 122880\n"
"s_lshl_b32 s54, s74, 1\n"
"s_add_u32 s54, s54, 5\n"
"s_and_b32 s54, s54, 7\n"
"s_mul_i32 s54, s54, 147456\n"
"s_add_u32 s54, s54, s92\n"
"s_add_u32 s54, s54, 73728\n"
"buffer_load_dwordx4 v242, s[76:79], s54 offen lds\n"
"buffer_load_dwordx4 v243, s[76:79], s54 offen offset:1024 lds\n"
"s_waitcnt lgkmcnt(4)\n"
"v_mfma_f32_32x32x16_bf16 v[0:15], v[96:99], v[120:123], v[0:15]\n"
"ds_read_b128 v[124:127], v251 offset:1024\n"
"ds_read_b64_tr_b16 v[108:109], v245 offset:6144\n"
"ds_read_b64_tr_b16 v[110:111], v245 offset:6656\n"
"s_waitcnt lgkmcnt(5)\n"
"v_mfma_f32_32x32x16_bf16 v[16:31], v[100:103], v[120:123], v[16:31]\n"
"ds_read_b64_tr_b16 v[112:113], v246 offset:6144\n"
"ds_read_b64_tr_b16 v[114:115], v246 offset:6656\n"
"s_waitcnt lgkmcnt(5)\n"
"v_mfma_f32_32x32x16_bf16 v[32:47], v[104:107], v[120:123], v[32:47]\n"
"ds_read_b64_tr_b16 v[116:117], v247 offset:6144\n"
"ds_read_b64_tr_b16 v[118:119], v247 offset:6656\n"
"buffer_load_dwordx4 v244, s[76:79], s54 offen offset:2048 lds\n"
"s_waitcnt lgkmcnt(4)\n"
"v_mfma_f32_32x32x16_bf16 v[0:15], v[108:111], v[124:127], v[0:15]\n"
"ds_read_b128 v[120:123], v251 offset:2048\n"
"ds_read_b64_tr_b16 v[96:97], v245 offset:12288\n"
"ds_read_b64_tr_b16 v[98:99], v245 offset:12800\n"
"s_waitcnt lgkmcnt(5)\n"
"v_mfma_f32_32x32x16_bf16 v[16:31], v[112:115], v[124:127], v[16:31]\n"
"ds_read_b64_tr_b16 v[100:101], v246 offset:12288\n"
"ds_read_b64_tr_b16 v[102:103], v246 offset:12800\n"
"s_waitcnt lgkmcnt(5)\n"
"v_mfma_f32_32x32x16_bf16 v[32:47], v[116:119], v[124:127], v[32:47]\n"
"ds_read_b64_tr_b16 v[104:105], v247 offset:12288\n"
"ds_read_b64_tr_b16 v[106:107], v247 offset:12800\n"
"s_add_u32 m0, s95, 122880\n"
"s_lshl_b32 s55, s74, 1\n"
"s_add_u32 s55, s55, 5\n"
"s_and_b32 s55, s55, 7\n"
"s_lshl_b32 s55, s55, 15\n"
"s_add_u32 s55, s55, s93\n"
"s_add_u32 s55, s55, 4096\n"
"buffer_load_dwordx4 v240, s[80:83], s55 offen sc1 lds\n"
"s_waitcnt lgkmcnt(4)\n"
"v_mfma_f32_32x32x16_bf16 v[0:15], v[96:99], v[120:123], v[0:15]\n"
"ds_read_b128 v[124:127], v251 offset:3072\n"
"ds_read_b64_tr_b16 v[108:109], v245 offset:18432\n"
"ds_read_b64_tr_b16 v[110:111], v245 offset:18944\n"
"s_waitcnt lgkmcnt(5)\n"
"v_mfma_f32_32x32x16_bf16 v[16:31], v[100:103], v[120:123], v[16:31]\n"
"ds_read_b64_tr_b16 v[112:113], v246 offset:18432\n"
"ds_read_b64_tr_b16 v[114:115], v246 offset:18944\n"
"s_waitcnt lgkmcnt(5)\n"
"v_mfma_f32_32x32x16_bf16 v[32:47], v[104:107], v[120:123], v[32:47]\n"
"ds_read_b64_tr_b16 v[116:117], v247 offset:18432\n"
"ds_read_b64_tr_b16 v[118:119], v247 offset:18944\n"
"buffer_load_dwordx4 v240, s[80:83], s55 offen offset:1024 sc1 lds\n"
"s_waitcnt vmcnt(10)\n"
"s_waitcnt lgkmcnt(0)\n"
"s_barrier\n"
"v_mfma_f32_32x32x16_bf16 v[0:15], v[108:111], v[124:127], v[0:15]\n"
"ds_read_b128 v[120:123], v251 offset:40960\n"
"ds_read_b64_tr_b16 v[96:97], v245 offset:40960\n"
"ds_read_b64_tr_b16 v[98:99], v245 offset:41472\n"
"v_mfma_f32_32x32x16_bf16 v[16:31], v[112:115], v[124:127], v[16:31]\n"
"ds_read_b64_tr_b16 v[100:101], v246 offset:40960\n"
"ds_read_b64_tr_b16 v[102:103], v246 offset:41472\n"
"v_mfma_f32_32x32x16_bf16 v[32:47], v[116:119], v[124:127], v[32:47]\n"
"ds_read_b64_tr_b16 v[104:105], v247 offset:40960\n"
"ds_read_b64_tr_b16 v[106:107], v247 offset:41472\n"
"s_add_u32 m0, s94, 0\n"
"s_lshl_b32 s54, s74, 1\n"
"s_add_u32 s54, s54, 6\n"
"s_and_b32 s54, s54, 7\n"
"s_mul_i32 s54, s54, 147456\n"
"s_add_u32 s54, s54, s92\n"
"buffer_load_dwordx4 v242, s[76:79], s54 offen lds\n"
"buffer_load_dwordx4 v243, s[76:79], s54 offen offset:1024 lds\n"
"s_waitcnt lgkmcnt(4)\n"
"v_mfma_f32_32x32x16_bf16 v[0:15], v[96:99], v[120:123], v[0:15]\n"
"ds_read_b128 v[124:127], v251 offset:41984\n"
"ds_read_b64_tr_b16 v[108:109], v245 offset:47104\n"
"ds_read_b64_tr_b16 v[110:111], v245 offset:47616\n"
"s_waitcnt lgkmcnt(5)\n"
"v_mfma_f32_32x32x16_bf16 v[16:31], v[100:103], v[120:123], v[16:31]\n"
"ds_read_b64_tr_b16 v[112:113], v246 offset:47104\n"
"ds_read_b64_tr_b16 v[114:115], v246 offset:47616\n"
"s_waitcnt lgkmcnt(5)\n"
"v_mfma_f32_32x32x16_bf16 v[32:47], v[104:107], v[120:123], v[32:47]\n"
"ds_read_b64_tr_b16 v[116:117], v247 offset:47104\n"
"ds_read_b64_tr_b16 v[118:119], v247 offset:47616\n"
"buffer_load_dwordx4 v244, s[76:79], s54 offen offset:2048 lds\n"
"s_waitcnt lgkmcnt(4)\n"
"v_mfma_f32_32x32x16_bf16 v[0:15], v[108:111], v[124:127], v[0:15]\n"
"ds_read_b128 v[120:123], v251 offset:43008\n"
"ds_read_b64_tr_b16 v[96:97], v245 offset:53248\n"
"ds_read_b64_tr_b16 v[98:99], v245 offset:53760\n"
"s_waitcnt lgkmcnt(5)\n"
"v_mfma_f32_32x32x16_bf16 v[16:31], v[112:115], v[124:127], v[16:31]\n"
"ds_read_b64_tr_b16 v[100:101], v246 offset:53248\n"
"ds_read_b64_tr_b16 v[102:103], v246 offset:53760\n"
"s_waitcnt lgkmcnt(5)\n"
"v_mfma_f32_32x32x16_bf16 v[32:47], v[116:119], v[124:127], v[32:47]\n"
"ds_read_b64_tr_b16 v[104:105], v247 offset:53248\n"
"ds_read_b64_tr_b16 v[106:107], v247 offset:53760\n"
"s_add_u32 m0, s95, 0\n"
"s_lshl_b32 s55, s74, 1\n"
"s_add_u32 s55, s55, 6\n"
"s_and_b32 s55, s55, 7\n"
"s_lshl_b32 s55, s55, 15\n"
"s_add_u32 s55, s55, s93\n"
"buffer_load_dwordx4 v240, s[80:83], s55 offen sc1 lds\n"
"s_waitcnt lgkmcnt(4)\n"
"v_mfma_f32_32x32x16_bf16 v[0:15], v[96:99], v[120:123], v[0:15]\n"
"ds_read_b128 v[124:127], v251 offset:44032\n"
"ds_read_b64_tr_b16 v[108:109], v245 offset:59392\n"
"ds_read_b64_tr_b16 v[110:111], v245 offset:59904\n"
"s_waitcnt lgkmcnt(5)\n"
"v_mfma_f32_32x32x16_bf16 v[16:31], v[100:103], v[120:123], v[16:31]\n"
"ds_read_b64_tr_b16 v[112:113], v246 offset:59392\n"
"ds_read_b64_tr_b16 v[114:115], v246 offset:59904\n"
"s_waitcnt lgkmcnt(5)\n"
"v_mfma_f32_32x32x16_bf16 v[32:47], v[104:107], v[120:123], v[32:47]\n"
"ds_read_b64_tr_b16 v[116:117], v247 offset:59392\n"
"ds_read_b64_tr_b16 v[118:119], v247 offset:59904\n"
"buffer_load_dwordx4 v240, s[80:83], s55 offen offset:1024 sc1 lds\n"
"s_waitcnt vmcnt(10)\n"
"s_waitcnt lgkmcnt(0)\n"
"s_barrier\n"
"v_mfma_f32_32x32x16_bf16 v[0:15], v[108:111], v[124:127], v[0:15]\n"
"ds_read_b128 v[120:123], v252 offset:0\n"
"ds_read_b64_tr_b16 v[96:97], v248 offset:0\n"
"ds_read_b64_tr_b16 v[98:99], v248 offset:512\n"
"v_mfma_f32_32x32x16_bf16 v[16:31], v[112:115], v[124:127], v[16:31]\n"
"ds_read_b64_tr_b16 v[100:101], v249 offset:0\n"
"ds_read_b64_tr_b16 v[102:103], v249 offset:512\n"
"v_mfma_f32_32x32x16_bf16 v[32:47], v[116:119], v[124:127], v[32:47]\n"
"ds_read_b64_tr_b16 v[104:105], v250 offset:0\n"
"ds_read_b64_tr_b16 v[106:107], v250 offset:512\n"
"s_add_u32 m0, s94, 40960\n"
"s_lshl_b32 s54, s74, 1\n"
"s_add_u32 s54, s54, 6\n"
"s_and_b32 s54, s54, 7\n"
"s_mul_i32 s54, s54, 147456\n"
"s_add_u32 s54, s54, s92\n"
"s_add_u32 s54, s54, 73728\n"
"buffer_load_dwordx4 v242, s[76:79], s54 offen lds\n"
"buffer_load_dwordx4 v243, s[76:79], s54 offen offset:1024 lds\n"
"s_waitcnt lgkmcnt(4)\n"
"v_mfma_f32_32x32x16_bf16 v[0:15], v[96:99], v[120:123], v[0:15]\n"
"ds_read_b128 v[124:127], v252 offset:1024\n"
"ds_read_b64_tr_b16 v[108:109], v248 offset:6144\n"
"ds_read_b64_tr_b16 v[110:111], v248 offset:6656\n"
"s_waitcnt lgkmcnt(5)\n"
"v_mfma_f32_32x32x16_bf16 v[16:31], v[100:103], v[120:123], v[16:31]\n"
"ds_read_b64_tr_b16 v[112:113], v249 offset:6144\n"
"ds_read_b64_tr_b16 v[114:115], v249 offset:6656\n"
"s_waitcnt lgkmcnt(5)\n"
"v_mfma_f32_32x32x16_bf16 v[32:47], v[104:107], v[120:123], v[32:47]\n"
"ds_read_b64_tr_b16 v[116:117], v250 offset:6144\n"
"ds_read_b64_tr_b16 v[118:119], v250 offset:6656\n"
"buffer_load_dwordx4 v244, s[76:79], s54 offen offset:2048 lds\n"
"s_waitcnt lgkmcnt(4)\n"
"v_mfma_f32_32x32x16_bf16 v[0:15], v[108:111], v[124:127], v[0:15]\n"
"ds_read_b128 v[120:123], v252 offset:2048\n"
"ds_read_b64_tr_b16 v[96:97], v248 offset:12288\n"
"ds_read_b64_tr_b16 v[98:99], v248 offset:12800\n"
"s_waitcnt lgkmcnt(5)\n"
"v_mfma_f32_32x32x16_bf16 v[16:31], v[112:115], v[124:127], v[16:31]\n"
"ds_read_b64_tr_b16 v[100:101], v249 offset:12288\n"
"ds_read_b64_tr_b16 v[102:103], v249 offset:12800\n"
"s_waitcnt lgkmcnt(5)\n"
"v_mfma_f32_32x32x16_bf16 v[32:47], v[116:119], v[124:127], v[32:47]\n"
"ds_read_b64_tr_b16 v[104:105], v250 offset:12288\n"
"ds_read_b64_tr_b16 v[106:107], v250 offset:12800\n"
"s_add_u32 m0, s95, 40960\n"
"s_lshl_b32 s55, s74, 1\n"
"s_add_u32 s55, s55, 6\n"
"s_and_b32 s55, s55, 7\n"
"s_lshl_b32 s55, s55, 15\n"
"s_add_u32 s55, s55, s93\n"
"s_add_u32 s55, s55, 4096\n"
"buffer_load_dwordx4 v240, s[80:83], s55 offen sc1 lds\n"
"s_waitcnt lgkmcnt(4)\n"
"v_mfma_f32_32x32x16_bf16 v[0:15], v[96:99], v[120:123], v[0:15]\n"
"ds_read_b128 v[124:127], v252 offset:3072\n"
"ds_read_b64_tr_b16 v[108:109], v248 offset:18432\n"
"ds_read_b64_tr_b16 v[110:111], v248 offset:18944\n"
"s_waitcnt lgkmcnt(5)\n"
"v_mfma_f32_32x32x16_bf16 v[16:31], v[100:103], v[120:123], v[16:31]\n"
"ds_read_b64_tr_b16 v[112:113], v249 offset:18432\n"
"ds_read_b64_tr_b16 v[114:115], v249 offset:18944\n"
"s_waitcnt lgkmcnt(5)\n"
"v_mfma_f32_32x32x16_bf16 v[32:47], v[104:107], v[120:123], v[32:47]\n"
"ds_read_b64_tr_b16 v[116:117], v250 offset:18432\n"
"ds_read_b64_tr_b16 v[118:119], v250 offset:18944\n"
"buffer_load_dwordx4 v240, s[80:83], s55 offen offset:1024 sc1 lds\n"
"s_waitcnt vmcnt(10)\n"
"s_waitcnt lgkmcnt(0)\n"
"s_barrier\n"
"v_mfma_f32_32x32x16_bf16 v[0:15], v[108:111], v[124:127], v[0:15]\n"
"ds_read_b128 v[120:123], v252 offset:40960\n"
"ds_read_b64_tr_b16 v[96:97], v248 offset:40960\n"
"ds_read_b64_tr_b16 v[98:99], v248 offset:41472\n"
"v_mfma_f32_32x32x16_bf16 v[16:31], v[112:115], v[124:127], v[16:31]\n"
"ds_read_b64_tr_b16 v[100:101], v249 offset:40960\n"
"ds_read_b64_tr_b16 v[102:103], v249 offset:41472\n"
"v_mfma_f32_32x32x16_bf16 v[32:47], v[116:119], v[124:127], v[32:47]\n"
"ds_read_b64_tr_b16 v[104:105], v250 offset:40960\n"
"ds_read_b64_tr_b16 v[106:107], v250 offset:41472\n"
"s_add_u32 m0, s94, 81920\n"
"s_lshl_b32 s54, s74, 1\n"
"s_add_u32 s54, s54, 7\n"
"s_and_b32 s54, s54, 7\n"
"s_mul_i32 s54, s54, 147456\n"
"s_add_u32 s54, s54, s92\n"
"buffer_load_dwordx4 v242, s[76:79], s54 offen lds\n"
"buffer_load_dwordx4 v243, s[76:79], s54 offen offset:1024 lds\n"
"s_waitcnt lgkmcnt(4)\n"
"v_mfma_f32_32x32x16_bf16 v[0:15], v[96:99], v[120:123], v[0:15]\n"
"ds_read_b128 v[124:127], v252 offset:41984\n"
"ds_read_b64_tr_b16 v[108:109], v248 offset:47104\n"
"ds_read_b64_tr_b16 v[110:111], v248 offset:47616\n"
"s_waitcnt lgkmcnt(5)\n"
"v_mfma_f32_32x32x16_bf16 v[16:31], v[100:103], v[120:123], v[16:31]\n"
"ds_read_b64_tr_b16 v[112:113], v249 offset:47104\n"
"ds_read_b64_tr_b16 v[114:115], v249 offset:47616\n"
"s_waitcnt lgkmcnt(5)\n"
"v_mfma_f32_32x32x16_bf16 v[32:47], v[104:107], v[120:123], v[32:47]\n"
"ds_read_b64_tr_b16 v[116:117], v250 offset:47104\n"
"ds_read_b64_tr_b16 v[118:119], v250 offset:47616\n"
"buffer_load_dwordx4 v244, s[76:79], s54 offen offset:2048 lds\n"
"s_waitcnt lgkmcnt(4)\n"
"v_mfma_f32_32x32x16_bf16 v[0:15], v[108:111], v[124:127], v[0:15]\n"
"ds_read_b128 v[120:123], v252 offset:43008\n"
"ds_read_b64_tr_b16 v[96:97], v248 offset:53248\n"
"ds_read_b64_tr_b16 v[98:99], v248 offset:53760\n"
"s_waitcnt lgkmcnt(5)\n"
"v_mfma_f32_32x32x16_bf16 v[16:31], v[112:115], v[124:127], v[16:31]\n"
"ds_read_b64_tr_b16 v[100:101], v249 offset:53248\n"
"ds_read_b64_tr_b16 v[102:103], v249 offset:53760\n"
"s_waitcnt lgkmcnt(5)\n"
"v_mfma_f32_32x32x16_bf16 v[32:47], v[116:119], v[124:127], v[32:47]\n"
"ds_read_b64_tr_b16 v[104:105], v250 offset:53248\n"
"ds_read_b64_tr_b16 v[106:107], v250 offset:53760\n"
"s_add_u32 m0, s95, 81920\n"
"s_lshl_b32 s55, s74, 1\n"
"s_add_u32 s55, s55, 7\n"
"s_and_b32 s55, s55, 7\n"
"s_lshl_b32 s55, s55, 15\n"
"s_add_u32 s55, s55, s93\n"
"buffer_load_dwordx4 v240, s[80:83], s55 offen sc1 lds\n"
"s_waitcnt lgkmcnt(4)\n"
"v_mfma_f32_32x32x16_bf16 v[0:15], v[96:99], v[120:123], v[0:15]\n"
"ds_read_b128 v[124:127], v252 offset:44032\n"
"ds_read_b64_tr_b16 v[108:109], v248 offset:59392\n"
"ds_read_b64_tr_b16 v[110:111], v248 offset:59904\n"
"s_waitcnt lgkmcnt(5)\n"
"v_mfma_f32_32x32x16_bf16 v[16:31], v[100:103], v[120:123], v[16:31]\n"
"ds_read_b64_tr_b16 v[112:113], v249 offset:59392\n"
"ds_read_b64_tr_b16 v[114:115], v249 offset:59904\n"
"s_waitcnt lgkmcnt(5)\n"
"v_mfma_f32_32x32x16_bf16 v[32:47], v[104:107], v[120:123], v[32:47]\n"
"ds_read_b64_tr_b16 v[116:117], v250 offset:59392\n"
"ds_read_b64_tr_b16 v[118:119], v250 offset:59904\n"
"buffer_load_dwordx4 v240, s[80:83], s55 offen offset:1024 sc1 lds\n"
"s_waitcnt vmcnt(10)\n"
"s_waitcnt lgkmcnt(0)\n"
"s_barrier\n"
"v_mfma_f32_32x32x16_bf16 v[0:15], v[108:111], v[124:127], v[0:15]\n"
"ds_read_b128 v[120:123], v251 offset:0\n"
"ds_read_b64_tr_b16 v[96:97], v245 offset:0\n"
"ds_read_b64_tr_b16 v[98:99], v245 offset:512\n"
"v_mfma_f32_32x32x16_bf16 v[16:31], v[112:115], v[124:127], v[16:31]\n"
"ds_read_b64_tr_b16 v[100:101], v246 offset:0\n"
"ds_read_b64_tr_b16 v[102:103], v246 offset:512\n"
"v_mfma_f32_32x32x16_bf16 v[32:47], v[116:119], v[124:127], v[32:47]\n"
"ds_read_b64_tr_b16 v[104:105], v247 offset:0\n"
"ds_read_b64_tr_b16 v[106:107], v247 offset:512\n"
"s_add_u32 m0, s94, 122880\n"
"s_lshl_b32 s54, s74, 1\n"
"s_add_u32 s54, s54, 7\n"
"s_and_b32 s54, s54, 7\n"
"s_mul_i32 s54, s54, 147456\n"
"s_add_u32 s54, s54, s92\n"
"s_add_u32 s54, s54, 73728\n"
"buffer_load_dwordx4 v242, s[76:79], s54 offen lds\n"
"buffer_load_dwordx4 v243, s[76:79], s54 offen offset:1024 lds\n"
"s_waitcnt lgkmcnt(4)\n"
"v_mfma_f32_32x32x16_bf16 v[0:15], v[96:99], v[120:123], v[0:15]\n"
"ds_read_b128 v[124:127], v251 offset:1024\n"
"ds_read_b64_tr_b16 v[108:109], v245 offset:6144\n"
"ds_read_b64_tr_b16 v[110:111], v245 offset:6656\n"
"s_waitcnt lgkmcnt(5)\n"
"v_mfma_f32_32x32x16_bf16 v[16:31], v[100:103], v[120:123], v[16:31]\n"
"ds_read_b64_tr_b16 v[112:113], v246 offset:6144\n"
"ds_read_b64_tr_b16 v[114:115], v246 offset:6656\n"
"s_waitcnt lgkmcnt(5)\n"
"v_mfma_f32_32x32x16_bf16 v[32:47], v[104:107], v[120:123], v[32:47]\n"
"ds_read_b64_tr_b16 v[116:117], v247 offset:6144\n"
"ds_read_b64_tr_b16 v[118:119], v247 offset:6656\n"
"buffer_load_dwordx4 v244, s[76:79], s54 offen offset:2048 lds\n"
"s_waitcnt lgkmcnt(4)\n"
"v_mfma_f32_32x32x16_bf16 v[0:15], v[108:111], v[124:127], v[0:15]\n"
"ds_read_b128 v[120:123], v251 offset:2048\n"
"ds_read_b64_tr_b16 v[96:97], v245 offset:12288\n"
"ds_read_b64_tr_b16 v[98:99], v245 offset:12800\n"
"s_waitcnt lgkmcnt(5)\n"
"v_mfma_f32_32x32x16_bf16 v[16:31], v[112:115], v[124:127], v[16:31]\n"
"ds_read_b64_tr_b16 v[100:101], v246 offset:12288\n"
"ds_read_b64_tr_b16 v[102:103], v246 offset:12800\n"
"s_waitcnt lgkmcnt(5)\n"
"v_mfma_f32_32x32x16_bf16 v[32:47], v[116:119], v[124:127], v[32:47]\n"
"ds_read_b64_tr_b16 v[104:105], v247 offset:12288\n"
"ds_read_b64_tr_b16 v[106:107], v247 offset:12800\n"
"s_add_u32 m0, s95, 122880\n"
"s_lshl_b32 s55, s74, 1\n"
"s_add_u32 s55, s55, 7\n"
"s_and_b32 s55, s55, 7\n"
"s_lshl_b32 s55, s55, 15\n"
"s_add_u32 s55, s55, s93\n"
"s_add_u32 s55, s55, 4096\n"
"buffer_load_dwordx4 v240, s[80:83], s55 offen sc1 lds\n"
"s_waitcnt lgkmcnt(4)\n"
"v_mfma_f32_32x32x16_bf16 v[0:15], v[96:99], v[120:123], v[0:15]\n"
"ds_read_b128 v[124:127], v251 offset:3072\n"
"ds_read_b64_tr_b16 v[108:109], v245 offset:18432\n"
"ds_read_b64_tr_b16 v[110:111], v245 offset:18944\n"
"s_waitcnt lgkmcnt(5)\n"
"v_mfma_f32_32x32x16_bf16 v[16:31], v[100:103], v[120:123], v[16:31]\n"
"ds_read_b64_tr_b16 v[112:113], v246 offset:18432\n"
"ds_read_b64_tr_b16 v[114:115], v246 offset:18944\n"
"s_waitcnt lgkmcnt(5)\n"
"v_mfma_f32_32x32x16_bf16 v[32:47], v[104:107], v[120:123], v[32:47]\n"
"ds_read_b64_tr_b16 v[116:117], v247 offset:18432\n"
"ds_read_b64_tr_b16 v[118:119], v247 offset:18944\n"
"buffer_load_dwordx4 v240, s[80:83], s55 offen offset:1024 sc1 lds\n"
"s_waitcnt vmcnt(10)\n"
"s_waitcnt lgkmcnt(0)\n"
"s_barrier\n"
"v_mfma_f32_32x32x16_bf16 v[0:15], v[108:111], v[124:127], v[0:15]\n"
"ds_read_b128 v[120:123], v251 offset:40960\n"
"ds_read_b64_tr_b16 v[96:97], v245 offset:40960\n"
"ds_read_b64_tr_b16 v[98:99], v245 offset:41472\n"
"v_mfma_f32_32x32x16_bf16 v[16:31], v[112:115], v[124:127], v[16:31]\n"
"ds_read_b64_tr_b16 v[100:101], v246 offset:40960\n"
"ds_read_b64_tr_b16 v[102:103], v246 offset:41472\n"
"v_mfma_f32_32x32x16_bf16 v[32:47], v[116:119], v[124:127], v[32:47]\n"
"ds_read_b64_tr_b16 v[104:105], v247 offset:40960\n"
"ds_read_b64_tr_b16 v[106:107], v247 offset:41472\n"
"s_waitcnt lgkmcnt(4)\n"
"v_mfma_f32_32x32x16_bf16 v[0:15], v[96:99], v[120:123], v[0:15]\n"
"ds_read_b128 v[124:127], v251 offset:41984\n"
"ds_read_b64_tr_b16 v[108:109], v245 offset:47104\n"
"ds_read_b64_tr_b16 v[110:111], v245 offset:47616\n"
"s_waitcnt lgkmcnt(5)\n"
"v_mfma_f32_32x32x16_bf16 v[16:31], v[100:103], v[120:123], v[16:31]\n"
"ds_read_b64_tr_b16 v[112:113], v246 offset:47104\n"
"ds_read_b64_tr_b16 v[114:115], v246 offset:47616\n"
"s_waitcnt lgkmcnt(5)\n"
"v_mfma_f32_32x32x16_bf16 v[32:47], v[104:107], v[120:123], v[32:47]\n"
"ds_read_b64_tr_b16 v[116:117], v247 offset:47104\n"
"ds_read_b64_tr_b16 v[118:119], v247 offset:47616\n"
"s_waitcnt lgkmcnt(4)\n"
"v_mfma_f32_32x32x16_bf16 v[0:15], v[108:111], v[124:127], v[0:15]\n"
"ds_read_b128 v[120:123], v251 offset:43008\n"
"ds_read_b64_tr_b16 v[96:97], v245 offset:53248\n"
"ds_read_b64_tr_b16 v[98:99], v245 offset:53760\n"
"s_waitcnt lgkmcnt(5)\n"
"v_mfma_f32_32x32x16_bf16 v[16:31], v[112:115], v[124:127], v[16:31]\n"
"ds_read_b64_tr_b16 v[100:101], v246 offset:53248\n"
"ds_read_b64_tr_b16 v[102:103], v246 offset:53760\n"
"s_waitcnt lgkmcnt(5)\n"
"v_mfma_f32_32x32x16_bf16 v[32:47], v[116:119], v[124:127], v[32:47]\n"
"ds_read_b64_tr_b16 v[104:105], v247 offset:53248\n"
"ds_read_b64_tr_b16 v[106:107], v247 offset:53760\n"
"s_waitcnt lgkmcnt(4)\n"
"v_mfma_f32_32x32x16_bf16 v[0:15], v[96:99], v[120:123], v[0:15]\n"
"ds_read_b128 v[124:127], v251 offset:44032\n"
"ds_read_b64_tr_b16 v[108:109], v245 offset:59392\n"
"ds_read_b64_tr_b16 v[110:111], v245 offset:59904\n"
"s_waitcnt lgkmcnt(5)\n"
"v_mfma_f32_32x32x16_bf16 v[16:31], v[100:103], v[120:123], v[16:31]\n"
"ds_read_b64_tr_b16 v[112:113], v246 offset:59392\n"
"ds_read_b64_tr_b16 v[114:115], v246 offset:59904\n"
"s_waitcnt lgkmcnt(5)\n"
"v_mfma_f32_32x32x16_bf16 v[32:47], v[104:107], v[120:123], v[32:47]\n"
"ds_read_b64_tr_b16 v[116:117], v247 offset:59392\n"
"ds_read_b64_tr_b16 v[118:119], v247 offset:59904\n"
"s_waitcnt vmcnt(5)\n"
"s_waitcnt lgkmcnt(0)\n"
"s_barrier\n"
"v_mfma_f32_32x32x16_bf16 v[0:15], v[108:111], v[124:127], v[0:15]\n"
"ds_read_b128 v[120:123], v252 offset:0\n"
"ds_read_b64_tr_b16 v[96:97], v248 offset:0\n"
"ds_read_b64_tr_b16 v[98:99], v248 offset:512\n"
"v_mfma_f32_32x32x16_bf16 v[16:31], v[112:115], v[124:127], v[16:31]\n"
"ds_read_b64_tr_b16 v[100:101], v249 offset:0\n"
"ds_read_b64_tr_b16 v[102:103], v249 offset:512\n"
"v_mfma_f32_32x32x16_bf16 v[32:47], v[116:119], v[124:127], v[32:47]\n"
"ds_read_b64_tr_b16 v[104:105], v250 offset:0\n"
"ds_read_b64_tr_b16 v[106:107], v250 offset:512\n"
"s_waitcnt lgkmcnt(4)\n"
"v_mfma_f32_32x32x16_bf16 v[0:15], v[96:99], v[120:123], v[0:15]\n"
"ds_read_b128 v[124:127], v252 offset:1024\n"
"ds_read_b64_tr_b16 v[108:109], v248 offset:6144\n"
"ds_read_b64_tr_b16 v[110:111], v248 offset:6656\n"
"s_waitcnt lgkmcnt(5)\n"
"v_mfma_f32_32x32x16_bf16 v[16:31], v[100:103], v[120:123], v[16:31]\n"
"ds_read_b64_tr_b16 v[112:113], v249 offset:6144\n"
"ds_read_b64_tr_b16 v[114:115], v249 offset:6656\n"
"s_waitcnt lgkmcnt(5)\n"
"v_mfma_f32_32x32x16_bf16 v[32:47], v[104:107], v[120:123], v[32:47]\n"
"ds_read_b64_tr_b16 v[116:117], v250 offset:6144\n"
"ds_read_b64_tr_b16 v[118:119], v250 offset:6656\n"
"s_waitcnt lgkmcnt(4)\n"
"v_mfma_f32_32x32x16_bf16 v[0:15], v[108:111], v[124:127], v[0:15]\n"
"ds_read_b128 v[120:123], v252 offset:2048\n"
"ds_read_b64_tr_b16 v[96:97], v248 offset:12288\n"
"ds_read_b64_tr_b16 v[98:99], v248 offset:12800\n"
"s_waitcnt lgkmcnt(5)\n"
"v_mfma_f32_32x32x16_bf16 v[16:31], v[112:115], v[124:127], v[16:31]\n"
"ds_read_b64_tr_b16 v[100:101], v249 offset:12288\n"
"ds_read_b64_tr_b16 v[102:103], v249 offset:12800\n"
"s_waitcnt lgkmcnt(5)\n"
"v_mfma_f32_32x32x16_bf16 v[32:47], v[116:119], v[124:127], v[32:47]\n"
"ds_read_b64_tr_b16 v[104:105], v250 offset:12288\n"
"ds_read_b64_tr_b16 v[106:107], v250 offset:12800\n"
"s_waitcnt lgkmcnt(4)\n"
"v_mfma_f32_32x32x16_bf16 v[0:15], v[96:99], v[120:123], v[0:15]\n"
"ds_read_b128 v[124:127], v252 offset:3072\n"
"ds_read_b64_tr_b16 v[108:109], v248 offset:18432\n"
"ds_read_b64_tr_b16 v[110:111], v248 offset:18944\n"
"s_waitcnt lgkmcnt(5)\n"
"v_mfma_f32_32x32x16_bf16 v[16:31], v[100:103], v[120:123], v[16:31]\n"
"ds_read_b64_tr_b16 v[112:113], v249 offset:18432\n"
"ds_read_b64_tr_b16 v[114:115], v249 offset:18944\n"
"s_waitcnt lgkmcnt(5)\n"
"v_mfma_f32_32x32x16_bf16 v[32:47], v[104:107], v[120:123], v[32:47]\n"
"ds_read_b64_tr_b16 v[116:117], v250 offset:18432\n"
"ds_read_b64_tr_b16 v[118:119], v250 offset:18944\n"
"s_waitcnt vmcnt(0)\n"
"s_waitcnt lgkmcnt(0)\n"
"s_barrier\n"
"v_mfma_f32_32x32x16_bf16 v[0:15], v[108:111], v[124:127], v[0:15]\n"
"ds_read_b128 v[120:123], v252 offset:40960\n"
"ds_read_b64_tr_b16 v[96:97], v248 offset:40960\n"
"ds_read_b64_tr_b16 v[98:99], v248 offset:41472\n"
"v_mfma_f32_32x32x16_bf16 v[16:31], v[112:115], v[124:127], v[16:31]\n"
"ds_read_b64_tr_b16 v[100:101], v249 offset:40960\n"
"ds_read_b64_tr_b16 v[102:103], v249 offset:41472\n"
"v_mfma_f32_32x32x16_bf16 v[32:47], v[116:119], v[124:127], v[32:47]\n"
"ds_read_b64_tr_b16 v[104:105], v250 offset:40960\n"
"ds_read_b64_tr_b16 v[106:107], v250 offset:41472\n"
"s_waitcnt lgkmcnt(4)\n"
"v_mfma_f32_32x32x16_bf16 v[0:15], v[96:99], v[120:123], v[0:15]\n"
"ds_read_b128 v[124:127], v252 offset:41984\n"
"ds_read_b64_tr_b16 v[108:109], v248 offset:47104\n"
"ds_read_b64_tr_b16 v[110:111], v248 offset:47616\n"
"s_waitcnt lgkmcnt(5)\n"
"v_mfma_f32_32x32x16_bf16 v[16:31], v[100:103], v[120:123], v[16:31]\n"
"ds_read_b64_tr_b16 v[112:113], v249 offset:47104\n"
"ds_read_b64_tr_b16 v[114:115], v249 offset:47616\n"
"s_waitcnt lgkmcnt(5)\n"
"v_mfma_f32_32x32x16_bf16 v[32:47], v[104:107], v[120:123], v[32:47]\n"
"ds_read_b64_tr_b16 v[116:117], v250 offset:47104\n"
"ds_read_b64_tr_b16 v[118:119], v250 offset:47616\n"
"s_waitcnt lgkmcnt(4)\n"
"v_mfma_f32_32x32x16_bf16 v[0:15], v[108:111], v[124:127], v[0:15]\n"
"ds_read_b128 v[120:123], v252 offset:43008\n"
"ds_read_b64_tr_b16 v[96:97], v248 offset:53248\n"
"ds_read_b64_tr_b16 v[98:99], v248 offset:53760\n"
"s_waitcnt lgkmcnt(5)\n"
"v_mfma_f32_32x32x16_bf16 v[16:31], v[112:115], v[124:127], v[16:31]\n"
"ds_read_b64_tr_b16 v[100:101], v249 offset:53248\n"
"ds_read_b64_tr_b16 v[102:103], v249 offset:53760\n"
"s_waitcnt lgkmcnt(5)\n"
"v_mfma_f32_32x32x16_bf16 v[32:47], v[116:119], v[124:127], v[32:47]\n"
"ds_read_b64_tr_b16 v[104:105], v250 offset:53248\n"
"ds_read_b64_tr_b16 v[106:107], v250 offset:53760\n"
"s_waitcnt lgkmcnt(4)\n"
"v_mfma_f32_32x32x16_bf16 v[0:15], v[96:99], v[120:123], v[0:15]\n"
"ds_read_b128 v[124:127], v252 offset:44032\n"
"ds_read_b64_tr_b16 v[108:109], v248 offset:59392\n"
"ds_read_b64_tr_b16 v[110:111], v248 offset:59904\n"
"s_waitcnt lgkmcnt(5)\n"
"v_mfma_f32_32x32x16_bf16 v[16:31], v[100:103], v[120:123], v[16:31]\n"
"ds_read_b64_tr_b16 v[112:113], v249 offset:59392\n"
"ds_read_b64_tr_b16 v[114:115], v249 offset:59904\n"
"s_waitcnt lgkmcnt(5)\n"
"v_mfma_f32_32x32x16_bf16 v[32:47], v[104:107], v[120:123], v[32:47]\n"
"ds_read_b64_tr_b16 v[116:117], v250 offset:59392\n"
"ds_read_b64_tr_b16 v[118:119], v250 offset:59904\n"
"s_waitcnt lgkmcnt(4)\n"
"v_mfma_f32_32x32x16_bf16 v[0:15], v[108:111], v[124:127], v[0:15]\n"
"s_waitcnt lgkmcnt(2)\n"
"v_mfma_f32_32x32x16_bf16 v[16:31], v[112:115], v[124:127], v[16:31]\n"
"s_waitcnt lgkmcnt(0)\n"
"v_mfma_f32_32x32x16_bf16 v[32:47], v[116:119], v[124:127], v[32:47]\n"
"s_nop 11\n"
"s_cmp_eq_u32 s46, s73\n"
"s_cbranch_scc1 KF_NS0\n"
"s_lshl_b32 s12, s46, 2\n"
"s_add_u32 s12, s12, s73\n"
"s_mul_i32 s12, s12, 9216\n"
"s_add_u32 s13, s12, 4096\n"
"v_cvt_pk_bf16_f32 v48, v0, v1\n"
"v_cvt_pk_bf16_f32 v49, v2, v3\n"
"v_cvt_pk_bf16_f32 v50, v4, v5\n"
"v_cvt_pk_bf16_f32 v51, v6, v7\n"
"buffer_store_dwordx4 v[48:51], v240, s[84:87], s12 offen sc1\n"
"v_cvt_pk_bf16_f32 v52, v8, v9\n"
"v_cvt_pk_bf16_f32 v53, v10, v11\n"
"v_cvt_pk_bf16_f32 v54, v12, v13\n"
"v_cvt_pk_bf16_f32 v55, v14, v15\n"
"buffer_store_dwordx4 v[52:55], v240, s[84:87], s12 offen offset:1024 sc1\n"
"v_cvt_pk_bf16_f32 v56, v16, v17\n"
"v_cvt_pk_bf16_f32 v57, v18, v19\n"
"v_cvt_pk_bf16_f32 v58, v20, v21\n"
"v_cvt_pk_bf16_f32 v59, v22, v23\n"
"buffer_store_dwordx4 v[56:59], v240, s[84:87], s12 offen offset:2048 sc1\n"
"v_cvt_pk_bf16_f32 v60, v24, v25\n"
"v_cvt_pk_bf16_f32 v61, v26, v27\n"
"v_cvt_pk_bf16_f32 v62, v28, v29\n"
"v_cvt_pk_bf16_f32 v63, v30, v31\n"
"buffer_store_dwordx4 v[60:63], v240, s[84:87], s12 offen offset:3072 sc1\n"
"v_cvt_pk_bf16_f32 v64, v32, v33\n"
"v_cvt_pk_bf16_f32 v65, v34, v35\n"
"v_cvt_pk_bf16_f32 v66, v36, v37\n"
"v_cvt_pk_bf16_f32 v67, v38, v39\n"
"buffer_store_dwordx4 v[64:67], v240, s[84:87], s13 offen sc1\n"
"v_cvt_pk_bf16_f32 v68, v40, v41\n"
"v_cvt_pk_bf16_f32 v69, v42, v43\n"
"v_cvt_pk_bf16_f32 v70, v44, v45\n"
"v_cvt_pk_bf16_f32 v71, v46, v47\n"
"buffer_store_dwordx4 v[68:71], v240, s[84:87], s13 offen offset:1024 sc1\n"
"KF_NS0:\n"
"s_waitcnt vmcnt(0)\n"
"s_waitcnt lgkmcnt(0)\n"
"s_barrier\n"
"s_cmp_lg_u32 s44, 0\n"
"s_cbranch_scc1 KF_WE\n"
"s_lshl_b32 s12, 1, s73\n"
"s_xor_b32 s12, s12, 15\n"
"s_lshl_b32 s12, s12, 4\n"
"s_mov_b32 s13, 0\n"
"s_mov_b64 exec, s[12:13]\n"
"global_store_dwordx2 v129, v[132:133], s[20:21] sc1\n"
"s_mov_b64 exec, -1\n"
"s_mov_b32 s58, 0\n"
"KF_POLLR:\n"
"s_mov_b64 exec, 0xff\n"
"global_load_dwordx2 v[136:137], v130, s[20:21] sc1\n"
"global_load_dwordx2 v[134:135], v128, s[20:21] sc1\n"
"s_mov_b64 exec, -1\n"
"s_waitcnt vmcnt(0)\n"
"s_lshl_b32 s12, 1, s73\n"
"s_xor_b32 s12, s12, 15\n"
"s_lshl_b32 s12, s12, 4\n"
"s_mov_b32 s13, 0\n"
"v_cmp_ne_u64 vcc, v[134:135], v[132:133]\n"
"s_nop 4\n"
"s_and_b64 s[12:13], s[12:13], vcc\n"
"s_cbranch_scc0 KF_RCRa\n"
"v_mov_b32 v132, v134\n"
"v_mov_b32 v133, v135\n"
"s_mov_b64 exec, s[12:13]\n"
"global_store_dwordx2 v129, v[132:133], s[20:21] sc1\n"
"s_mov_b64 exec, -1\n"
"KF_RCRa:\n"
"s_lshl_b32 s12, 1, s73\n"
"s_xor_b32 s12, s12, 15\n"
"s_lshl_b32 s12, s12, 4\n"
"s_mov_b32 s13, 0\n"
"v_cmp_eq_u64 vcc, s[96:97], v[136:137]\n"
"s_nop 4\n"
"s_and_b64 vcc, vcc, s[12:13]\n"
"s_cmp_eq_u64 vcc, s[12:13]\n"
"s_cbranch_scc1 KF_GOTR\n"
"s_add_u32 s58, s58, 1\n"
"s_cmp_lt_u32 s58, 0x200000\n"
"s_cbranch_scc1 KF_POLLR\n"
"KF_GOTR:\n"
"s_mov_b64 exec, 0xff\n"
"global_load_dwordx2 v[134:135], v128, s[20:21] sc1\n"
"s_mov_b64 exec, -1\n"
"KF_WE:\n"
"s_waitcnt lgkmcnt(0)\n"
"s_barrier\n"
"s_cmp_eq_u32 s46, s73\n"
"s_cbranch_scc0 KF_LV0\n"
"s_lshl_b32 s17, s73, 2\n"
"s_add_u32 s12, s73, 1\n"
"s_and_b32 s12, s12, 3\n"
"s_add_u32 s12, s12, s17\n"
"s_mul_i32 s12, s12, 9216\n"
"s_add_u32 s13, s12, 4096\n"
"buffer_load_dwordx4 v[48:51], v240, s[84:87], s12 offen sc1\n"
"buffer_load_dwordx4 v[52:55], v240, s[84:87], s12 offen offset:1024 sc1\n"
"buffer_load_dwordx4 v[56:59], v240, s[84:87], s12 offen offset:2048 sc1\n"
"buffer_load_dwordx4 v[60:63], v240, s[84:87], s12 offen offset:3072 sc1\n"
"buffer_load_dwordx4 v[64:67], v240, s[84:87], s13 offen sc1\n"
"buffer_load_dwordx4 v[68:71], v240, s[84:87], s13 offen offset:1024 sc1\n"
"s_add_u32 s12, s73, 2\n"
"s_and_b32 s12, s12, 3\n"
"s_add_u32 s12, s12, s17\n"
"s_mul_i32 s12, s12, 9216\n"
"s_add_u32 s13, s12, 4096\n"
"buffer_load_dwordx4 v[72:75], v240, s[84:87], s12 offen sc1\n"
"buffer_load_dwordx4 v[76:79], v240, s[84:87], s12 offen offset:1024 sc1\n"
"buffer_load_dwordx4 v[80:83], v240, s[84:87], s12 offen offset:2048 sc1\n"
"buffer_load_dwordx4 v[84:87], v240, s[84:87], s12 offen offset:3072 sc1\n"
"buffer_load_dwordx4 v[88:91], v240, s[84:87], s13 offen sc1\n"
"buffer_load_dwordx4 v[92:95], v240, s[84:87], s13 offen offset:1024 sc1\n"
"s_add_u32 s12, s73, 3\n"
"s_and_b32 s12, s12, 3\n"
"s_add_u32 s12, s12, s17\n"
"s_mul_i32 s12, s12, 9216\n"
"s_add_u32 s13, s12, 4096\n"
"buffer_load_dwordx4 v[96:99], v240, s[84:87], s12 offen sc1\n"
"buffer_load_dwordx4 v[100:103], v240, s[84:87], s12 offen offset:1024 sc1\n"
"buffer_load_dwordx4 v[104:107], v240, s[84:87], s12 offen offset:2048 sc1\n"
"buffer_load_dwordx4 v[108:111], v240, s[84:87], s12 offen offset:3072 sc1\n"
"buffer_load_dwordx4 v[112:115], v240, s[84:87], s13 offen sc1\n"
"buffer_load_dwordx4 v[116:119], v240, s[84:87], s13 offen offset:1024 sc1\n"
"v_lshrrev_b32 v230, 4, v240\n"
"v_and_b32 v231, 31, v230\n"
"v_mul_u32_u24 v231, 0x900, v231\n"
"v_lshrrev_b32 v230, 5, v230\n"
"v_lshl_add_u32 v231, v230, 4, v231\n"
"s_mul_i32 s59, s46, 73728\n"
"s_waitcnt vmcnt(17)\n"
"v_lshlrev_b32 v234, 16, v48\n"
"v_and_b32 v235, 0xffff0000, v48\n"
"v_add_f32 v0, v0, v234\n"
"v_add_f32 v1, v1, v235\n"
"v_lshlrev_b32 v236, 16, v49\n"
"v_and_b32 v237, 0xffff0000, v49\n"
"v_add_f32 v2, v2, v236\n"
"v_add_f32 v3, v3, v237\n"
"v_lshlrev_b32 v234, 16, v50\n"
"v_and_b32 v235, 0xffff0000, v50\n"
"v_add_f32 v4, v4, v234\n"
"v_add_f32 v5, v5, v235\n"
"v_lshlrev_b32 v236, 16, v51\n"
"v_and_b32 v237, 0xffff0000, v51\n"
"v_add_f32 v6, v6, v236\n"
"v_add_f32 v7, v7, v237\n"
"s_waitcnt vmcnt(16)\n"
"v_lshlrev_b32 v234, 16, v52\n"
"v_and_b32 v235, 0xffff0000, v52\n"
"v_add_f32 v8, v8, v234\n"
"v_add_f32 v9, v9, v235\n"
"v_lshlrev_b32 v236, 16, v53\n"
"v_and_b32 v237, 0xffff0000, v53\n"
"v_add_f32 v10, v10, v236\n"
"v_add_f32 v11, v11, v237\n"
"v_lshlrev_b32 v234, 16, v54\n"
"v_and_b32 v235, 0xffff0000, v54\n"
"v_add_f32 v12, v12, v234\n"
"v_add_f32 v13, v13, v235\n"
"v_lshlrev_b32 v236, 16, v55\n"
"v_and_b32 v237, 0xffff0000, v55\n"
"v_add_f32 v14, v14, v236\n"
"v_add_f32 v15, v15, v237\n"
"s_waitcnt vmcnt(15)\n"
"v_lshlrev_b32 v234, 16, v56\n"
"v_and_b32 v235, 0xffff0000, v56\n"
"v_add_f32 v16, v16, v234\n"
"v_add_f32 v17, v17, v235\n"
"v_lshlrev_b32 v236, 16, v57\n"
"v_and_b32 v237, 0xffff0000, v57\n"
"v_add_f32 v18, v18, v236\n"
"v_add_f32 v19, v19, v237\n"
"v_lshlrev_b32 v234, 16, v58\n"
"v_and_b32 v235, 0xffff0000, v58\n"
"v_add_f32 v20, v20, v234\n"
"v_add_f32 v21, v21, v235\n"
"v_lshlrev_b32 v236, 16, v59\n"
"v_and_b32 v237, 0xffff0000, v59\n"
"v_add_f32 v22, v22, v236\n"
"v_add_f32 v23, v23, v237\n"
"s_waitcnt vmcnt(14)\n"
"v_lshlrev_b32 v234, 16, v60\n"
"v_and_b32 v235, 0xffff0000, v60\n"
"v_add_f32 v24, v24, v234\n"
"v_add_f32 v25, v25, v235\n"
"v_lshlrev_b32 v236, 16, v61\n"
"v_and_b32 v237, 0xffff0000, v61\n"
"v_add_f32 v26, v26, v236\n"
"v_add_f32 v27, v27, v237\n"
"v_lshlrev_b32 v234, 16, v62\n"
"v_and_b32 v235, 0xffff0000, v62\n"
"v_add_f32 v28, v28, v234\n"
"v_add_f32 v29, v29, v235\n"
"v_lshlrev_b32 v236, 16, v63\n"
"v_and_b32 v237, 0xffff0000, v63\n"
"v_add_f32 v30, v30, v236\n"
"v_add_f32 v31, v31, v237\n"
"s_waitcnt vmcnt(13)\n"
"v_lshlrev_b32 v234, 16, v64\n"
"v_and_b32 v235, 0xffff0000, v64\n"
"v_add_f32 v32, v32, v234\n"
"v_add_f32 v33, v33, v235\n"
"v_lshlrev_b32 v236, 16, v65\n"
"v_and_b32 v237, 0xffff0000, v65\n"
"v_add_f32 v34, v34, v236\n"
"v_add_f32 v35, v35, v237\n"
"v_lshlrev_b32 v234, 16, v66\n"
"v_and_b32 v235, 0xffff0000, v66\n"
"v_add_f32 v36, v36, v234\n"
"v_add_f32 v37, v37, v235\n"
"v_lshlrev_b32 v236, 16, v67\n"
"v_and_b32 v237, 0xffff0000, v67\n"
"v_add_f32 v38, v38, v236\n"
"v_add_f32 v39, v39, v237\n"
"s_waitcnt vmcnt(12)\n"
"v_lshlrev_b32 v234, 16, v68\n"
"v_and_b32 v235, 0xffff0000, v68\n"
"v_add_f32 v40, v40, v234\n"
"v_add_f32 v41, v41, v235\n"
"v_lshlrev_b32 v236, 16, v69\n"
"v_and_b32 v237, 0xffff0000, v69\n"
"v_add_f32 v42, v42, v236\n"
"v_add_f32 v43, v43, v237\n"
"v_lshlrev_b32 v234, 16, v70\n"
"v_and_b32 v235, 0xffff0000, v70\n"
"v_add_f32 v44, v44, v234\n"
"v_add_f32 v45, v45, v235\n"
"v_lshlrev_b32 v236, 16, v71\n"
"v_and_b32 v237, 0xffff0000, v71\n"
"v_add_f32 v46, v46, v236\n"
"v_add_f32 v47, v47, v237\n"
"s_waitcnt vmcnt(11)\n"
"v_lshlrev_b32 v234, 16, v72\n"
"v_and_b32 v235, 0xffff0000, v72\n"
"v_add_f32 v0, v0, v234\n"
"v_add_f32 v1, v1, v235\n"
"v_lshlrev_b32 v236, 16, v73\n"
"v_and_b32 v237, 0xffff0000, v73\n"
"v_add_f32 v2, v2, v236\n"
"v_add_f32 v3, v3, v237\n"
"v_lshlrev_b32 v234, 16, v74\n"
"v_and_b32 v235, 0xffff0000, v74\n"
"v_add_f32 v4, v4, v234\n"
"v_add_f32 v5, v5, v235\n"
"v_lshlrev_b32 v236, 16, v75\n"
"v_and_b32 v237, 0xffff0000, v75\n"
"v_add_f32 v6, v6, v236\n"
"v_add_f32 v7, v7, v237\n"
"s_waitcnt vmcnt(10)\n"
"v_lshlrev_b32 v234, 16, v76\n"
"v_and_b32 v235, 0xffff0000, v76\n"
"v_add_f32 v8, v8, v234\n"
"v_add_f32 v9, v9, v235\n"
"v_lshlrev_b32 v236, 16, v77\n"
"v_and_b32 v237, 0xffff0000, v77\n"
"v_add_f32 v10, v10, v236\n"
"v_add_f32 v11, v11, v237\n"
"v_lshlrev_b32 v234, 16, v78\n"
"v_and_b32 v235, 0xffff0000, v78\n"
"v_add_f32 v12, v12, v234\n"
"v_add_f32 v13, v13, v235\n"
"v_lshlrev_b32 v236, 16, v79\n"
"v_and_b32 v237, 0xffff0000, v79\n"
"v_add_f32 v14, v14, v236\n"
"v_add_f32 v15, v15, v237\n"
"s_waitcnt vmcnt(9)\n"
"v_lshlrev_b32 v234, 16, v80\n"
"v_and_b32 v235, 0xffff0000, v80\n"
"v_add_f32 v16, v16, v234\n"
"v_add_f32 v17, v17, v235\n"
"v_lshlrev_b32 v236, 16, v81\n"
"v_and_b32 v237, 0xffff0000, v81\n"
"v_add_f32 v18, v18, v236\n"
"v_add_f32 v19, v19, v237\n"
"v_lshlrev_b32 v234, 16, v82\n"
"v_and_b32 v235, 0xffff0000, v82\n"
"v_add_f32 v20, v20, v234\n"
"v_add_f32 v21, v21, v235\n"
"v_lshlrev_b32 v236, 16, v83\n"
"v_and_b32 v237, 0xffff0000, v83\n"
"v_add_f32 v22, v22, v236\n"
"v_add_f32 v23, v23, v237\n"
"s_waitcnt vmcnt(8)\n"
"v_lshlrev_b32 v234, 16, v84\n"
"v_and_b32 v235, 0xffff0000, v84\n"
"v_add_f32 v24, v24, v234\n"
"v_add_f32 v25, v25, v235\n"
"v_lshlrev_b32 v236, 16, v85\n"
"v_and_b32 v237, 0xffff0000, v85\n"
"v_add_f32 v26, v26, v236\n"
"v_add_f32 v27, v27, v237\n"
"v_lshlrev_b32 v234, 16, v86\n"
"v_and_b32 v235, 0xffff0000, v86\n"
"v_add_f32 v28, v28, v234\n"
"v_add_f32 v29, v29, v235\n"
"v_lshlrev_b32 v236, 16, v87\n"
"v_and_b32 v237, 0xffff0000, v87\n"
"v_add_f32 v30, v30, v236\n"
"v_add_f32 v31, v31, v237\n"
"s_waitcnt vmcnt(7)\n"
"v_lshlrev_b32 v234, 16, v88\n"
"v_and_b32 v235, 0xffff0000, v88\n"
"v_add_f32 v32, v32, v234\n"
"v_add_f32 v33, v33, v235\n"
"v_lshlrev_b32 v236, 16, v89\n"
"v_and_b32 v237, 0xffff0000, v89\n"
"v_add_f32 v34, v34, v236\n"
"v_add_f32 v35, v35, v237\n"
"v_lshlrev_b32 v234, 16, v90\n"
"v_and_b32 v235, 0xffff0000, v90\n"
"v_add_f32 v36, v36, v234\n"
"v_add_f32 v37, v37, v235\n"
"v_lshlrev_b32 v236, 16, v91\n"
"v_and_b32 v237, 0xffff0000, v91\n"
"v_add_f32 v38, v38, v236\n"
"v_add_f32 v39, v39, v237\n"
"s_waitcnt vmcnt(6)\n"
"v_lshlrev_b32 v234, 16, v92\n"
"v_and_b32 v235, 0xffff0000, v92\n"
"v_add_f32 v40, v40, v234\n"
"v_add_f32 v41, v41, v235\n"
"v_lshlrev_b32 v236, 16, v93\n"
"v_and_b32 v237, 0xffff0000, v93\n"
"v_add_f32 v42, v42, v236\n"
"v_add_f32 v43, v43, v237\n"
"v_lshlrev_b32 v234, 16, v94\n"
"v_and_b32 v235, 0xffff0000, v94\n"
"v_add_f32 v44, v44, v234\n"
"v_add_f32 v45, v45, v235\n"
"v_lshlrev_b32 v236, 16, v95\n"
"v_and_b32 v237, 0xffff0000, v95\n"
"v_add_f32 v46, v46, v236\n"
"v_add_f32 v47, v47, v237\n"
"s_waitcnt vmcnt(5)\n"
"v_lshlrev_b32 v234, 16, v96\n"
"v_and_b32 v235, 0xffff0000, v96\n"
"v_add_f32 v0, v0, v234\n"
"v_add_f32 v1, v1, v235\n"
"v_lshlrev_b32 v236, 16, v97\n"
"v_and_b32 v237, 0xffff0000, v97\n"
"v_add_f32 v2, v2, v236\n"
"v_add_f32 v3, v3, v237\n"
"v_lshlrev_b32 v234, 16, v98\n"
"v_and_b32 v235, 0xffff0000, v98\n"
"v_add_f32 v4, v4, v234\n"
"v_add_f32 v5, v5, v235\n"
"v_lshlrev_b32 v236, 16, v99\n"
"v_and_b32 v237, 0xffff0000, v99\n"
"v_add_f32 v6, v6, v236\n"
"v_add_f32 v7, v7, v237\n"
"s_waitcnt vmcnt(4)\n"
"v_lshlrev_b32 v234, 16, v100\n"
"v_and_b32 v235, 0xffff0000, v100\n"
"v_add_f32 v8, v8, v234\n"
"v_add_f32 v9, v9, v235\n"
"v_lshlrev_b32 v236, 16, v101\n"
"v_and_b32 v237, 0xffff0000, v101\n"
"v_add_f32 v10, v10, v236\n"
"v_add_f32 v11, v11, v237\n"
"v_lshlrev_b32 v234, 16, v102\n"
"v_and_b32 v235, 0xffff0000, v102\n"
"v_add_f32 v12, v12, v234\n"
"v_add_f32 v13, v13, v235\n"
"v_lshlrev_b32 v236, 16, v103\n"
"v_and_b32 v237, 0xffff0000, v103\n"
"v_add_f32 v14, v14, v236\n"
"v_add_f32 v15, v15, v237\n"
"s_waitcnt vmcnt(3)\n"
"v_lshlrev_b32 v234, 16, v104\n"
"v_and_b32 v235, 0xffff0000, v104\n"
"v_add_f32 v16, v16, v234\n"
"v_add_f32 v17, v17, v235\n"
"v_lshlrev_b32 v236, 16, v105\n"
"v_and_b32 v237, 0xffff0000, v105\n"
"v_add_f32 v18, v18, v236\n"
"v_add_f32 v19, v19, v237\n"
"v_lshlrev_b32 v234, 16, v106\n"
"v_and_b32 v235, 0xffff0000, v106\n"
"v_add_f32 v20, v20, v234\n"
"v_add_f32 v21, v21, v235\n"
"v_lshlrev_b32 v236, 16, v107\n"
"v_and_b32 v237, 0xffff0000, v107\n"
"v_add_f32 v22, v22, v236\n"
"v_add_f32 v23, v23, v237\n"
"s_waitcnt vmcnt(2)\n"
"v_lshlrev_b32 v234, 16, v108\n"
"v_and_b32 v235, 0xffff0000, v108\n"
"v_add_f32 v24, v24, v234\n"
"v_add_f32 v25, v25, v235\n"
"v_lshlrev_b32 v236, 16, v109\n"
"v_and_b32 v237, 0xffff0000, v109\n"
"v_add_f32 v26, v26, v236\n"
"v_add_f32 v27, v27, v237\n"
"v_lshlrev_b32 v234, 16, v110\n"
"v_and_b32 v235, 0xffff0000, v110\n"
"v_add_f32 v28, v28, v234\n"
"v_add_f32 v29, v29, v235\n"
"v_lshlrev_b32 v236, 16, v111\n"
"v_and_b32 v237, 0xffff0000, v111\n"
"v_add_f32 v30, v30, v236\n"
"v_add_f32 v31, v31, v237\n"
"s_waitcnt vmcnt(1)\n"
"v_lshlrev_b32 v234, 16, v112\n"
"v_and_b32 v235, 0xffff0000, v112\n"
"v_add_f32 v32, v32, v234\n"
"v_add_f32 v33, v33, v235\n"
"v_lshlrev_b32 v236, 16, v113\n"
"v_and_b32 v237, 0xffff0000, v113\n"
"v_add_f32 v34, v34, v236\n"
"v_add_f32 v35, v35, v237\n"
"v_lshlrev_b32 v234, 16, v114\n"
"v_and_b32 v235, 0xffff0000, v114\n"
"v_add_f32 v36, v36, v234\n"
"v_add_f32 v37, v37, v235\n"
"v_lshlrev_b32 v236, 16, v115\n"
"v_and_b32 v237, 0xffff0000, v115\n"
"v_add_f32 v38, v38, v236\n"
"v_add_f32 v39, v39, v237\n"
"s_waitcnt vmcnt(0)\n"
"v_lshlrev_b32 v234, 16, v116\n"
"v_and_b32 v235, 0xffff0000, v116\n"
"v_add_f32 v40, v40, v234\n"
"v_add_f32 v41, v41, v235\n"
"v_lshlrev_b32 v236, 16, v117\n"
"v_and_b32 v237, 0xffff0000, v117\n"
"v_add_f32 v42, v42, v236\n"
"v_add_f32 v43, v43, v237\n"
"v_lshlrev_b32 v234, 16, v118\n"
"v_and_b32 v235, 0xffff0000, v118\n"
"v_add_f32 v44, v44, v234\n"
"v_add_f32 v45, v45, v235\n"
"v_lshlrev_b32 v236, 16, v119\n"
"v_and_b32 v237, 0xffff0000, v119\n"
"v_add_f32 v46, v46, v236\n"
"v_add_f32 v47, v47, v237\n"
"v_cvt_pk_bf16_f32 v140, v0, v1\n"
"v_cvt_pk_bf16_f32 v141, v2, v3\n"
"v_cvt_pk_bf16_f32 v142, v4, v5\n"
"v_cvt_pk_bf16_f32 v143, v6, v7\n"
"s_nop 0\n"
"v_permlane32_swap_b32 v140, v142\n"
"v_permlane32_swap_b32 v141, v143\n"
"buffer_store_dwordx4 v[140:143], v231, s[88:91], s59 offen\n"
"v_cvt_pk_bf16_f32 v144, v8, v9\n"
"v_cvt_pk_bf16_f32 v145, v10, v11\n"
"v_cvt_pk_bf16_f32 v146, v12, v13\n"
"v_cvt_pk_bf16_f32 v147, v14, v15\n"
"s_nop 0\n"
"v_permlane32_swap_b32 v144, v146\n"
"v_permlane32_swap_b32 v145, v147\n"
"buffer_store_dwordx4 v[144:147], v231, s[88:91], s59 offen offset:32\n"
"v_cvt_pk_bf16_f32 v148, v16, v17\n"
"v_cvt_pk_bf16_f32 v149, v18, v19\n"
"v_cvt_pk_bf16_f32 v150, v20, v21\n"
"v_cvt_pk_bf16_f32 v151, v22, v23\n"
"s_nop 0\n"
"v_permlane32_swap_b32 v148, v150\n"
"v_permlane32_swap_b32 v149, v151\n"
"buffer_store_dwordx4 v[148:151], v231, s[88:91], s59 offen offset:64\n"
"v_cvt_pk_bf16_f32 v152, v24, v25\n"
"v_cvt_pk_bf16_f32 v153, v26, v27\n"
"v_cvt_pk_bf16_f32 v154, v28, v29\n"
"v_cvt_pk_bf16_f32 v155, v30, v31\n"
"s_nop 0\n"
"v_permlane32_swap_b32 v152, v154\n"
"v_permlane32_swap_b32 v153, v155\n"
"buffer_store_dwordx4 v[152:155], v231, s[88:91], s59 offen offset:96\n"
"v_cvt_pk_bf16_f32 v156, v32, v33\n"
"v_cvt_pk_bf16_f32 v157, v34, v35\n"
"v_cvt_pk_bf16_f32 v158, v36, v37\n"
"v_cvt_pk_bf16_f32 v159, v38, v39\n"
"s_nop 0\n"
"v_permlane32_swap_b32 v156, v158\n"
"v_permlane32_swap_b32 v157, v159\n"
"buffer_store_dwordx4 v[156:159], v231, s[88:91], s59 offen offset:128\n"
"v_cvt_pk_bf16_f32 v160, v40, v41\n"
"v_cvt_pk_bf16_f32 v161, v42, v43\n"
"v_cvt_pk_bf16_f32 v162, v44, v45\n"
"v_cvt_pk_bf16_f32 v163, v46, v47\n"
"s_nop 0\n"
"v_permlane32_swap_b32 v160, v162\n"
"v_permlane32_swap_b32 v161, v163\n"
"buffer_store_dwordx4 v[160:163], v231, s[88:91], s59 offen offset:160\n"
"KF_LV0:\n"
"s_cmp_lg_u32 s44, 0\n"
"s_cbranch_scc1 KF_WF\n"
"s_waitcnt vmcnt(0)\n"
"s_lshl_b32 s12, 1, s73\n"
"s_xor_b32 s12, s12, 15\n"
"s_lshl_b32 s12, s12, 4\n"
"s_mov_b32 s13, 0\n"
"v_cmp_ne_u64 vcc, v[134:135], v[132:133]\n"
"s_nop 4\n"
"s_and_b64 s[12:13], s[12:13], vcc\n"
"s_cbranch_scc0 KF_RCRb\n"
"v_mov_b32 v132, v134\n"
"v_mov_b32 v133, v135\n"
"s_mov_b64 exec, s[12:13]\n"
"global_store_dwordx2 v129, v[132:133], s[20:21] sc1\n"
"s_mov_b64 exec, -1\n"
"KF_RCRb:\n"
"KF_WF:\n"
"s_branch KF_FIN\n"
"KF_NH1:\n"
"ds_read_b128 v[120:123], v251 offset:0\n"
"ds_read_b64_tr_b16 v[96:97], v245 offset:0\n"
"ds_read_b64_tr_b16 v[98:99], v245 offset:512\n"
"ds_read_b64_tr_b16 v[100:101], v246 offset:0\n"
"ds_read_b64_tr_b16 v[102:103], v246 offset:512\n"
"s_waitcnt lgkmcnt(2)\n"
"v_mfma_f32_32x32x16_bf16 v[0:15], v[96:99], v[120:123], 0\n"
"ds_read_b128 v[124:127], v251 offset:1024\n"
"ds_read_b64_tr_b16 v[108:109], v245 offset:6144\n"
"ds_read_b64_tr_b16 v[110:111], v245 offset:6656\n"
"s_waitcnt lgkmcnt(3)\n"
"v_mfma_f32_32x32x16_bf16 v[16:31], v[100:103], v[120:123], 0\n"
"ds_read_b64_tr_b16 v[112:113], v246 offset:6144\n"
"ds_read_b64_tr_b16 v[114:115], v246 offset:6656\n"
"s_waitcnt lgkmcnt(2)\n"
"v_mfma_f32_32x32x16_bf16 v[0:15], v[108:111], v[124:127], v[0:15]\n"
"ds_read_b128 v[120:123], v251 offset:2048\n"
"ds_read_b64_tr_b16 v[96:97], v245 offset:12288\n"
"ds_read_b64_tr_b16 v[98:99], v245 offset:12800\n"
"s_waitcnt lgkmcnt(3)\n"
"v_mfma_f32_32x32x16_bf16 v[16:31], v[112:115], v[124:127], v[16:31]\n"
"ds_read_b64_tr_b16 v[100:101], v246 offset:12288\n"
"ds_read_b64_tr_b16 v[102:103], v246 offset:12800\n"
"s_waitcnt lgkmcnt(2)\n"
"v_mfma_f32_32x32x16_bf16 v[0:15], v[96:99], v[120:123], v[0:15]\n"
"ds_read_b128 v[124:127], v251 offset:3072\n"
"ds_read_b64_tr_b16 v[108:109], v245 offset:18432\n"
"ds_read_b64_tr_b16 v[110:111], v245 offset:18944\n"
"s_waitcnt lgkmcnt(3)\n"
"v_mfma_f32_32x32x16_bf16 v[16:31], v[100:103], v[120:123], v[16:31]\n"
"ds_read_b64_tr_b16 v[112:113], v246 offset:18432\n"
"ds_read_b64_tr_b16 v[114:115], v246 offset:18944\n"
"s_waitcnt vmcnt(11)\n"
"s_waitcnt lgkmcnt(0)\n"
"s_barrier\n"
"v_mfma_f32_32x32x16_bf16 v[0:15], v[108:111], v[124:127], v[0:15]\n"
"ds_read_b128 v[120:123], v251 offset:40960\n"
"ds_read_b64_tr_b16 v[96:97], v245 offset:40960\n"
"ds_read_b64_tr_b16 v[98:99], v245 offset:41472\n"
"v_mfma_f32_32x32x16_bf16 v[16:31], v[112:115], v[124:127], v[16:31]\n"
"ds_read_b64_tr_b16 v[100:101], v246 offset:40960\n"
"ds_read_b64_tr_b16 v[102:103], v246 offset:41472\n"
"s_add_u32 m0, s94, 0\n"
"s_lshl_b32 s54, s74, 1\n"
"s_add_u32 s54, s54, 2\n"
"s_and_b32 s54, s54, 7\n"
"s_mul_i32 s54, s54, 147456\n"
"s_add_u32 s54, s54, s92\n"
"buffer_load_dwordx4 v242, s[76:79], s54 offen lds\n"
"buffer_load_dwordx4 v243, s[76:79], s54 offen offset:1024 lds\n"
"s_waitcnt lgkmcnt(2)\n"
"v_mfma_f32_32x32x16_bf16 v[0:15], v[96:99], v[120:123], v[0:15]\n"
"ds_read_b128 v[124:127], v251 offset:41984\n"
"ds_read_b64_tr_b16 v[108:109], v245 offset:47104\n"
"ds_read_b64_tr_b16 v[110:111], v245 offset:47616\n"
"s_waitcnt lgkmcnt(3)\n"
"v_mfma_f32_32x32x16_bf16 v[16:31], v[100:103], v[120:123], v[16:31]\n"
"ds_read_b64_tr_b16 v[112:113], v246 offset:47104\n"
"ds_read_b64_tr_b16 v[114:115], v246 offset:47616\n"
"buffer_load_dwordx4 v244, s[76:79], s54 offen offset:2048 lds\n"
"s_waitcnt lgkmcnt(2)\n"
"v_mfma_f32_32x32x16_bf16 v[0:15], v[108:111], v[124:127], v[0:15]\n"
"ds_read_b128 v[120:123], v251 offset:43008\n"
"ds_read_b64_tr_b16 v[96:97], v245 offset:53248\n"
"ds_read_b64_tr_b16 v[98:99], v245 offset:53760\n"
"s_waitcnt lgkmcnt(3)\n"
"v_mfma_f32_32x32x16_bf16 v[16:31], v[112:115], v[124:127], v[16:31]\n"
"ds_read_b64_tr_b16 v[100:101], v246 offset:53248\n"
"ds_read_b64_tr_b16 v[102:103], v246 offset:53760\n"
"s_waitcnt lgkmcnt(2)\n"
"v_mfma_f32_32x32x16_bf16 v[0:15], v[96:99], v[120:123], v[0:15]\n"
"ds_read_b128 v[124:127], v251 offset:44032\n"
"ds_read_b64_tr_b16 v[108:109], v245 offset:59392\n"
"ds_read_b64_tr_b16 v[110:111], v245 offset:59904\n"
"s_waitcnt lgkmcnt(3)\n"
"v_mfma_f32_32x32x16_bf16 v[16:31], v[100:103], v[120:123], v[16:31]\n"
"ds_read_b64_tr_b16 v[112:113], v246 offset:59392\n"
"ds_read_b64_tr_b16 v[114:115], v246 offset:59904\n"
"s_waitcnt vmcnt(3)\n"
"s_waitcnt lgkmcnt(0)\n"
"s_barrier\n"
"v_mfma_f32_32x32x16_bf16 v[0:15], v[108:111], v[124:127], v[0:15]\n"
"ds_read_b128 v[120:123], v252 offset:0\n"
"ds_read_b64_tr_b16 v[96:97], v248 offset:0\n"
"ds_read_b64_tr_b16 v[98:99], v248 offset:512\n"
"v_mfma_f32_32x32x16_bf16 v[16:31], v[112:115], v[124:127], v[16:31]\n"
"ds_read_b64_tr_b16 v[100:101], v249 offset:0\n"
"ds_read_b64_tr_b16 v[102:103], v249 offset:512\n"
"s_add_u32 m0, s94, 40960\n"
"s_lshl_b32 s54, s74, 1\n"
"s_add_u32 s54, s54, 2\n"
"s_and_b32 s54, s54, 7\n"
"s_mul_i32 s54, s54, 147456\n"
"s_add_u32 s54, s54, s92\n"
"s_add_u32 s54, s54, 73728\n"
"buffer_load_dwordx4 v242, s[76:79], s54 offen lds\n"
"buffer_load_dwordx4 v243, s[76:79], s54 offen offset:1024 lds\n"
"s_waitcnt lgkmcnt(2)\n"
"v_mfma_f32_32x32x16_bf16 v[0:15], v[96:99], v[120:123], v[0:15]\n"
"ds_read_b128 v[124:127], v252 offset:1024\n"
"ds_read_b64_tr_b16 v[108:109], v248 offset:6144\n"
"ds_read_b64_tr_b16 v[110:111], v248 offset:6656\n"
"s_waitcnt lgkmcnt(3)\n"
"v_mfma_f32_32x32x16_bf16 v[16:31], v[100:103], v[120:123], v[16:31]\n"
"ds_read_b64_tr_b16 v[112:113], v249 offset:6144\n"
"ds_read_b64_tr_b16 v[114:115], v249 offset:6656\n"
"buffer_load_dwordx4 v244, s[76:79], s54 offen offset:2048 lds\n"
"s_waitcnt lgkmcnt(2)\n"
"v_mfma_f32_32x32x16_bf16 v[0:15], v[108:111], v[124:127], v[0:15]\n"
"ds_read_b128 v[120:123], v252 offset:2048\n"
"ds_read_b64_tr_b16 v[96:97], v248 offset:12288\n"
"ds_read_b64_tr_b16 v[98:99], v248 offset:12800\n"
"s_waitcnt lgkmcnt(3)\n"
"v_mfma_f32_32x32x16_bf16 v[16:31], v[112:115], v[124:127], v[16:31]\n"
"ds_read_b64_tr_b16 v[100:101], v249 offset:12288\n"
"ds_read_b64_tr_b16 v[102:103], v249 offset:12800\n"
"s_waitcnt lgkmcnt(2)\n"
"v_mfma_f32_32x32x16_bf16 v[0:15], v[96:99], v[120:123], v[0:15]\n"
"ds_read_b128 v[124:127], v252 offset:3072\n"
"ds_read_b64_tr_b16 v[108:109], v248 offset:18432\n"
"ds_read_b64_tr_b16 v[110:111], v248 offset:18944\n"
"s_waitcnt lgkmcnt(3)\n"
"v_mfma_f32_32x32x16_bf16 v[16:31], v[100:103], v[120:123], v[16:31]\n"
"ds_read_b64_tr_b16 v[112:113], v249 offset:18432\n"
"ds_read_b64_tr_b16 v[114:115], v249 offset:18944\n"
"s_waitcnt lgkmcnt(0)\n"
"s_barrier\n"
"v_mfma_f32_32x32x16_bf16 v[0:15], v[108:111], v[124:127], v[0:15]\n"
"ds_read_b128 v[120:123], v252 offset:40960\n"
"ds_read_b64_tr_b16 v[96:97], v248 offset:40960\n"
"ds_read_b64_tr_b16 v[98:99], v248 offset:41472\n"
"v_mfma_f32_32x32x16_bf16 v[16:31], v[112:115], v[124:127], v[16:31]\n"
"ds_read_b64_tr_b16 v[100:101], v249 offset:40960\n"
"ds_read_b64_tr_b16 v[102:103], v249 offset:41472\n"
"s_add_u32 m0, s94, 81920\n"
"s_lshl_b32 s54, s74, 1\n"
"s_add_u32 s54, s54, 3\n"
"s_and_b32 s54, s54, 7\n"
"s_mul_i32 s54, s54, 147456\n"
"s_add_u32 s54, s54, s92\n"
"buffer_load_dwordx4 v242, s[76:79], s54 offen lds\n"
"buffer_load_dwordx4 v243, s[76:79], s54 offen offset:1024 lds\n"
"s_waitcnt lgkmcnt(2)\n"
"v_mfma_f32_32x32x16_bf16 v[0:15], v[96:99], v[120:123], v[0:15]\n"
"ds_read_b128 v[124:127], v252 offset:41984\n"
"ds_read_b64_tr_b16 v[108:109], v248 offset:47104\n"
"ds_read_b64_tr_b16 v[110:111], v248 offset:47616\n"
"s_waitcnt lgkmcnt(3)\n"
"v_mfma_f32_32x32x16_bf16 v[16:31], v[100:103], v[120:123], v[16:31]\n"
"ds_read_b64_tr_b16 v[112:113], v249 offset:47104\n"
"ds_read_b64_tr_b16 v[114:115], v249 offset:47616\n"
"buffer_load_dwordx4 v244, s[76:79], s54 offen offset:2048 lds\n"
"s_waitcnt lgkmcnt(2)\n"
"v_mfma_f32_32x32x16_bf16 v[0:15], v[108:111], v[124:127], v[0:15]\n"
"ds_read_b128 v[120:123], v252 offset:43008\n"
"ds_read_b64_tr_b16 v[96:97], v248 offset:53248\n"
"ds_read_b64_tr_b16 v[98:99], v248 offset:53760\n"
"s_waitcnt lgkmcnt(3)\n"
"v_mfma_f32_32x32x16_bf16 v[16:31], v[112:115], v[124:127], v[16:31]\n"
"ds_read_b64_tr_b16 v[100:101], v249 offset:53248\n"
"ds_read_b64_tr_b16 v[102:103], v249 offset:53760\n"
"s_waitcnt lgkmcnt(2)\n"
"v_mfma_f32_32x32x16_bf16 v[0:15], v[96:99], v[120:123], v[0:15]\n"
"ds_read_b128 v[124:127], v252 offset:44032\n"
"ds_read_b64_tr_b16 v[108:109], v248 offset:59392\n"
"ds_read_b64_tr_b16 v[110:111], v248 offset:59904\n"
"s_waitcnt lgkmcnt(3)\n"
"v_mfma_f32_32x32x16_bf16 v[16:31], v[100:103], v[120:123], v[16:31]\n"
"ds_read_b64_tr_b16 v[112:113], v249 offset:59392\n"
"ds_read_b64_tr_b16 v[114:115], v249 offset:59904\n"
"s_waitcnt vmcnt(6)\n"
"s_waitcnt lgkmcnt(0)\n"
"s_barrier\n"
"s_add_u32 m0, s95, 0\n"
"s_lshl_b32 s55, s74, 1\n"
"s_add_u32 s55, s55, 2\n"
"s_and_b32 s55, s55, 7\n"
"s_lshl_b32 s55, s55, 15\n"
"s_add_u32 s55, s55, s93\n"
"buffer_load_dwordx4 v240, s[80:83], s55 offen sc1 lds\n"
"buffer_load_dwordx4 v240, s[80:83], s55 offen offset:1024 sc1 lds\n"
"v_mfma_f32_32x32x16_bf16 v[0:15], v[108:111], v[124:127], v[0:15]\n"
"s_add_u32 m0, s95, 40960\n"
"s_lshl_b32 s55, s74, 1\n"
"s_add_u32 s55, s55, 2\n"
"s_and_b32 s55, s55, 7\n"
"s_lshl_b32 s55, s55, 15\n"
"s_add_u32 s55, s55, s93\n"
"s_add_u32 s55, s55, 4096\n"
"buffer_load_dwordx4 v240, s[80:83], s55 offen sc1 lds\n"
"buffer_load_dwordx4 v240, s[80:83], s55 offen offset:1024 sc1 lds\n"
"v_mfma_f32_32x32x16_bf16 v[16:31], v[112:115], v[124:127], v[16:31]\n"
"s_add_u32 m0, s94, 122880\n"
"s_lshl_b32 s54, s74, 1\n"
"s_add_u32 s54, s54, 3\n"
"s_and_b32 s54, s54, 7\n"
"s_mul_i32 s54, s54, 147456\n"
"s_add_u32 s54, s54, s92\n"
"s_add_u32 s54, s54, 73728\n"
"buffer_load_dwordx4 v242, s[76:79], s54 offen lds\n"
"buffer_load_dwordx4 v243, s[76:79], s54 offen offset:1024 lds\n"
"s_waitcnt vmcnt(4)\n"
"s_waitcnt lgkmcnt(0)\n"
"s_barrier\n"
"ds_read_b128 v[120:123], v251 offset:0\n"
"ds_read_b64_tr_b16 v[96:97], v245 offset:0\n"
"ds_read_b64_tr_b16 v[98:99], v245 offset:512\n"
"ds_read_b64_tr_b16 v[100:101], v246 offset:0\n"
"ds_read_b64_tr_b16 v[102:103], v246 offset:512\n"
"s_waitcnt lgkmcnt(2)\n"
"v_mfma_f32_32x32x16_bf16 v[0:15], v[96:99], v[120:123], v[0:15]\n"
"ds_read_b128 v[124:127], v251 offset:1024\n"
"ds_read_b64_tr_b16 v[108:109], v245 offset:6144\n"
"ds_read_b64_tr_b16 v[110:111], v245 offset:6656\n"
"s_waitcnt lgkmcnt(3)\n"
"v_mfma_f32_32x32x16_bf16 v[16:31], v[100:103], v[120:123], v[16:31]\n"
"ds_read_b64_tr_b16 v[112:113], v246 offset:6144\n"
"ds_read_b64_tr_b16 v[114:115], v246 offset:6656\n"
"buffer_load_dwordx4 v244, s[76:79], s54 offen offset:2048 lds\n"
"s_waitcnt lgkmcnt(2)\n"
"v_mfma_f32_32x32x16_bf16 v[0:15], v[108:111], v[124:127], v[0:15]\n"
"ds_read_b128 v[120:123], v251 offset:2048\n"
"ds_read_b64_tr_b16 v[96:97], v245 offset:12288\n"
"ds_read_b64_tr_b16 v[98:99], v245 offset:12800\n"
"s_waitcnt lgkmcnt(3)\n"
"v_mfma_f32_32x32x16_bf16 v[16:31], v[112:115], v[124:127], v[16:31]\n"
"ds_read_b64_tr_b16 v[100:101], v246 offset:12288\n"
"ds_read_b64_tr_b16 v[102:103], v246 offset:12800\n"
"s_add_u32 m0, s95, 81920\n"
"s_lshl_b32 s55, s74, 1\n"
"s_add_u32 s55, s55, 3\n"
"s_and_b32 s55, s55, 7\n"
"s_lshl_b32 s55, s55, 15\n"
"s_add_u32 s55, s55, s93\n"
"buffer_load_dwordx4 v240, s[80:83], s55 offen sc1 lds\n"
"buffer_load_dwordx4 v240, s[80:83], s55 offen offset:1024 sc1 lds\n"
"s_waitcnt lgkmcnt(2)\n"
"v_mfma_f32_32x32x16_bf16 v[0:15], v[96:99], v[120:123], v[0:15]\n"
"ds_read_b128 v[124:127], v251 offset:3072\n"
"ds_read_b64_tr_b16 v[108:109], v245 offset:18432\n"
"ds_read_b64_tr_b16 v[110:111], v245 offset:18944\n"
"s_waitcnt lgkmcnt(3)\n"
"v_mfma_f32_32x32x16_bf16 v[16:31], v[100:103], v[120:123], v[16:31]\n"
"ds_read_b64_tr_b16 v[112:113], v246 offset:18432\n"
"ds_read_b64_tr_b16 v[114:115], v246 offset:18944\n"
"s_add_u32 m0, s95, 122880\n"
"s_lshl_b32 s55, s74, 1\n"
"s_add_u32 s55, s55, 3\n"
"s_and_b32 s55, s55, 7\n"
"s_lshl_b32 s55, s55, 15\n"
"s_add_u32 s55, s55, s93\n"
"s_add_u32 s55, s55, 4096\n"
"buffer_load_dwordx4 v240, s[80:83], s55 offen sc1 lds\n"
"buffer_load_dwordx4 v240, s[80:83], s55 offen offset:1024 sc1 lds\n"
"s_waitcnt vmcnt(7)\n"
"s_waitcnt lgkmcnt(0)\n"
"s_barrier\n"
"v_mfma_f32_32x32x16_bf16 v[0:15], v[108:111], v[124:127], v[0:15]\n"
"ds_read_b128 v[120:123], v251 offset:40960\n"
"ds_read_b64_tr_b16 v[96:97], v245 offset:40960\n"
"ds_read_b64_tr_b16 v[98:99], v245 offset:41472\n"
"v_mfma_f32_32x32x16_bf16 v[16:31], v[112:115], v[124:127], v[16:31]\n"
"ds_read_b64_tr_b16 v[100:101], v246 offset:40960\n"
"ds_read_b64_tr_b16 v[102:103], v246 offset:41472\n"
"s_add_u32 m0, s94, 0\n"
"s_lshl_b32 s54, s74, 1\n"
"s_add_u32 s54, s54, 4\n"
"s_and_b32 s54, s54, 7\n"
"s_mul_i32 s54, s54, 147456\n"
"s_add_u32 s54, s54, s92\n"
"buffer_load_dwordx4 v242, s[76:79], s54 offen lds\n"
"buffer_load_dwordx4 v243, s[76:79], s54 offen offset:1024 lds\n"
"s_waitcnt lgkmcnt(2)\n"
"v_mfma_f32_32x32x16_bf16 v[0:15], v[96:99], v[120:123], v[0:15]\n"
"ds_read_b128 v[124:127], v251 offset:41984\n"
"ds_read_b64_tr_b16 v[108:109], v245 offset:47104\n"
"ds_read_b64_tr_b16 v[110:111], v245 offset:47616\n"
"s_waitcnt lgkmcnt(3)\n"
"v_mfma_f32_32x32x16_bf16 v[16:31], v[100:103], v[120:123], v[16:31]\n"
"ds_read_b64_tr_b16 v[112:113], v246 offset:47104\n"
"ds_read_b64_tr_b16 v[114:115], v246 offset:47616\n"
"buffer_load_dwordx4 v244, s[76:79], s54 offen offset:2048 lds\n"
"s_waitcnt lgkmcnt(2)\n"
"v_mfma_f32_32x32x16_bf16 v[0:15], v[108:111], v[124:127], v[0:15]\n"
"ds_read_b128 v[120:123], v251 offset:43008\n"
"ds_read_b64_tr_b16 v[96:97], v245 offset:53248\n"
"ds_read_b64_tr_b16 v[98:99], v245 offset:53760\n"
"s_waitcnt lgkmcnt(3)\n"
"v_mfma_f32_32x32x16_bf16 v[16:31], v[112:115], v[124:127], v[16:31]\n"
"ds_read_b64_tr_b16 v[100:101], v246 offset:53248\n"
"ds_read_b64_tr_b16 v[102:103], v246 offset:53760\n"
"s_add_u32 m0, s95, 0\n"
"s_lshl_b32 s55, s74, 1\n"
"s_add_u32 s55, s55, 4\n"
"s_and_b32 s55, s55, 7\n"
"s_lshl_b32 s55, s55, 15\n"
"s_add_u32 s55, s55, s93\n"
"buffer_load_dwordx4 v240, s[80:83], s55 offen sc1 lds\n"
"s_waitcnt lgkmcnt(2)\n"
"v_mfma_f32_32x32x16_bf16 v[0:15], v[96:99], v[120:123], v[0:15]\n"
"ds_read_b128 v[124:127], v251 offset:44032\n"
"ds_read_b64_tr_b16 v[108:109], v245 offset:59392\n"
"ds_read_b64_tr_b16 v[110:111], v245 offset:59904\n"
"s_waitcnt lgkmcnt(3)\n"
"v_mfma_f32_32x32x16_bf16 v[16:31], v[100:103], v[120:123], v[16:31]\n"
"ds_read_b64_tr_b16 v[112:113], v246 offset:59392\n"
"ds_read_b64_tr_b16 v[114:115], v246 offset:59904\n"
"buffer_load_dwordx4 v240, s[80:83], s55 offen offset:1024 sc1 lds\n"
"s_waitcnt vmcnt(7)\n"
"s_waitcnt lgkmcnt(0)\n"
"s_barrier\n"
"v_mfma_f32_32x32x16_bf16 v[0:15], v[108:111], v[124:127], v[0:15]\n"
"ds_read_b128 v[120:123], v252 offset:0\n"
"ds_read_b64_tr_b16 v[96:97], v248 offset:0\n"
"ds_read_b64_tr_b16 v[98:99], v248 offset:512\n"
"v_mfma_f32_32x32x16_bf16 v[16:31], v[112:115], v[124:127], v[16:31]\n"
"ds_read_b64_tr_b16 v[100:101], v249 offset:0\n"
"ds_read_b64_tr_b16 v[102:103], v249 offset:512\n"
"s_add_u32 m0, s94, 40960\n"
"s_lshl_b32 s54, s74, 1\n"
"s_add_u32 s54, s54, 4\n"
"s_and_b32 s54, s54, 7\n"
"s_mul_i32 s54, s54, 147456\n"
"s_add_u32 s54, s54, s92\n"
"s_add_u32 s54, s54, 73728\n"
"buffer_load_dwordx4 v242, s[76:79], s54 offen lds\n"
"buffer_load_dwordx4 v243, s[76:79], s54 offen offset:1024 lds\n"
"s_waitcnt lgkmcnt(2)\n"
"v_mfma_f32_32x32x16_bf16 v[0:15], v[96:99], v[120:123], v[0:15]\n"
"ds_read_b128 v[124:127], v252 offset:1024\n"
"ds_read_b64_tr_b16 v[108:109], v248 offset:6144\n"
"ds_read_b64_tr_b16 v[110:111], v248 offset:6656\n"
"s_waitcnt lgkmcnt(3)\n"
"v_mfma_f32_32x32x16_bf16 v[16:31], v[100:103], v[120:123], v[16:31]\n"
"ds_read_b64_tr_b16 v[112:113], v249 offset:6144\n"
"ds_read_b64_tr_b16 v[114:115], v249 offset:6656\n"
"buffer_load_dwordx4 v244, s[76:79], s54 offen offset:2048 lds\n"
"s_waitcnt lgkmcnt(2)\n"
"v_mfma_f32_32x32x16_bf16 v[0:15], v[108:111], v[124:127], v[0:15]\n"
"ds_read_b128 v[120:123], v252 offset:2048\n"
"ds_read_b64_tr_b16 v[96:97], v248 offset:12288\n"
"ds_read_b64_tr_b16 v[98:99], v248 offset:12800\n"
"s_waitcnt lgkmcnt(3)\n"
"v_mfma_f32_32x32x16_bf16 v[16:31], v[112:115], v[124:127], v[16:31]\n"
"ds_read_b64_tr_b16 v[100:101], v249 offset:12288\n"
"ds_read_b64_tr_b16 v[102:103], v249 offset:12800\n"
"s_add_u32 m0, s95, 40960\n"
"s_lshl_b32 s55, s74, 1\n"
"s_add_u32 s55, s55, 4\n"
"s_and_b32 s55, s55, 7\n"
"s_lshl_b32 s55, s55, 15\n"
"s_add_u32 s55, s55, s93\n"
"s_add_u32 s55, s55, 4096\n"
"buffer_load_dwordx4 v240, s[80:83], s55 offen sc1 lds\n"
"s_waitcnt lgkmcnt(2)\n"
"v_mfma_f32_32x32x16_bf16 v[0:15], v[96:99], v[120:123], v[0:15]\n"
"ds_read_b128 v[124:127], v252 offset:3072\n"
"ds_read_b64_tr_b16 v[108:109], v248 offset:18432\n"
"ds_read_b64_tr_b16 v[110:111], v248 offset:18944\n"
"s_waitcnt lgkmcnt(3)\n"
"v_mfma_f32_32x32x16_bf16 v[16:31], v[100:103], v[120:123], v[16:31]\n"
"ds_read_b64_tr_b16 v[112:113], v249 offset:18432\n"
"ds_read_b64_tr_b16 v[114:115], v249 offset:18944\n"
"buffer_load_dwordx4 v240, s[80:83], s55 offen offset:1024 sc1 lds\n"
"s_waitcnt vmcnt(10)\n"
"s_waitcnt lgkmcnt(0)\n"
"s_barrier\n"
"v_mfma_f32_32x32x16_bf16 v[0:15], v[108:111], v[124:127], v[0:15]\n"
"ds_read_b128 v[120:123], v252 offset:40960\n"
"ds_read_b64_tr_b16 v[96:97], v248 offset:40960\n"
"ds_read_b64_tr_b16 v[98:99], v248 offset:41472\n"
"v_mfma_f32_32x32x16_bf16 v[16:31], v[112:115], v[124:127], v[16:31]\n"
"ds_read_b64_tr_b16 v[100:101], v249 offset:40960\n"
"ds_read_b64_tr_b16 v[102:103], v249 offset:41472\n"
"s_add_u32 m0, s94, 81920\n"
"s_lshl_b32 s54, s74, 1\n"
"s_add_u32 s54, s54, 5\n"
"s_and_b32 s54, s54, 7\n"
"s_mul_i32 s54, s54, 147456\n"
"s_add_u32 s54, s54, s92\n"
"buffer_load_dwordx4 v242, s[76:79], s54 offen lds\n"
"buffer_load_dwordx4 v243, s[76:79], s54 offen offset:1024 lds\n"
"s_waitcnt lgkmcnt(2)\n"
"v_mfma_f32_32x32x16_bf16 v[0:15], v[96:99], v[120:123], v[0:15]\n"
"ds_read_b128 v[124:127], v252 offset:41984\n"
"ds_read_b64_tr_b16 v[108:109], v248 offset:47104\n"
"ds_read_b64_tr_b16 v[110:111], v248 offset:47616\n"
"s_waitcnt lgkmcnt(3)\n"
"v_mfma_f32_32x32x16_bf16 v[16:31], v[100:103], v[120:123], v[16:31]\n"
"ds_read_b64_tr_b16 v[112:113], v249 offset:47104\n"
"ds_read_b64_tr_b16 v[114:115], v249 offset:47616\n"
"buffer_load_dwordx4 v244, s[76:79], s54 offen offset:2048 lds\n"
"s_waitcnt lgkmcnt(2)\n"
"v_mfma_f32_32x32x16_bf16 v[0:15], v[108:111], v[124:127], v[0:15]\n"
"ds_read_b128 v[120:123], v252 offset:43008\n"
"ds_read_b64_tr_b16 v[96:97], v248 offset:53248\n"
"ds_read_b64_tr_b16 v[98:99], v248 offset:53760\n"
"s_waitcnt lgkmcnt(3)\n"
"v_mfma_f32_32x32x16_bf16 v[16:31], v[112:115], v[124:127], v[16:31]\n"
"ds_read_b64_tr_b16 v[100:101], v249 offset:53248\n"
"ds_read_b64_tr_b16 v[102:103], v249 offset:53760\n"
"s_add_u32 m0, s95, 81920\n"
"s_lshl_b32 s55, s74, 1\n"
"s_add_u32 s55, s55, 5\n"
"s_and_b32 s55, s55, 7\n"
"s_lshl_b32 s55, s55, 15\n"
"s_add_u32 s55, s55, s93\n"
"buffer_load_dwordx4 v240, s[80:83], s55 offen sc1 lds\n"
"s_waitcnt lgkmcnt(2)\n"
"v_mfma_f32_32x32x16_bf16 v[0:15], v[96:99], v[120:123], v[0:15]\n"
"ds_read_b128 v[124:127], v252 offset:44032\n"
"ds_read_b64_tr_b16 v[108:109], v248 offset:59392\n"
"ds_read_b64_tr_b16 v[110:111], v248 offset:59904\n"
"s_waitcnt lgkmcnt(3)\n"
"v_mfma_f32_32x32x16_bf16 v[16:31], v[100:103], v[120:123], v[16:31]\n"
"ds_read_b64_tr_b16 v[112:113], v249 offset:59392\n"
"ds_read_b64_tr_b16 v[114:115], v249 offset:59904\n"
"buffer_load_dwordx4 v240, s[80:83], s55 offen offset:1024 sc1 lds\n"
"s_waitcnt vmcnt(10)\n"
"s_waitcnt lgkmcnt(0)\n"
"s_barrier\n"
"v_mfma_f32_32x32x16_bf16 v[0:15], v[108:111], v[124:127], v[0:15]\n"
"ds_read_b128 v[120:123], v251 offset:0\n"
"ds_read_b64_tr_b16 v[96:97], v245 offset:0\n"
"ds_read_b64_tr_b16 v[98:99], v245 offset:512\n"
"v_mfma_f32_32x32x16_bf16 v[16:31], v[112:115], v[124:127], v[16:31]\n"
"ds_read_b64_tr_b16 v[100:101], v246 offset:0\n"
"ds_read_b64_tr_b16 v[102:103], v246 offset:512\n"
"s_add_u32 m0, s94, 122880\n"
"s_lshl_b32 s54, s74, 1\n"
"s_add_u32 s54, s54, 5\n"
"s_and_b32 s54, s54, 7\n"
"s_mul_i32 s54, s54, 147456\n"
"s_add_u32 s54, s54, s92\n"
"s_add_u32 s54, s54, 73728\n"
"buffer_load_dwordx4 v242, s[76:79], s54 offen lds\n"
"buffer_load_dwordx4 v243, s[76:79], s54 offen offset:1024 lds\n"
"s_waitcnt lgkmcnt(2)\n"
"v_mfma_f32_32x32x16_bf16 v[0:15], v[96:99], v[120:123], v[0:15]\n"
"ds_read_b128 v[124:127], v251 offset:1024\n"
"ds_read_b64_tr_b16 v[108:109], v245 offset:6144\n"
"ds_read_b64_tr_b16 v[110:111], v245 offset:6656\n"
"s_waitcnt lgkmcnt(3)\n"
"v_mfma_f32_32x32x16_bf16 v[16:31], v[100:103], v[120:123], v[16:31]\n"
"ds_read_b64_tr_b16 v[112:113], v246 offset:6144\n"
"ds_read_b64_tr_b16 v[114:115], v246 offset:6656\n"
"buffer_load_dwordx4 v244, s[76:79], s54 offen offset:2048 lds\n"
"s_waitcnt lgkmcnt(2)\n"
"v_mfma_f32_32x32x16_bf16 v[0:15], v[108:111], v[124:127], v[0:15]\n"
"ds_read_b128 v[120:123], v251 offset:2048\n"
"ds_read_b64_tr_b16 v[96:97], v245 offset:12288\n"
"ds_read_b64_tr_b16 v[98:99], v245 offset:12800\n"
"s_waitcnt lgkmcnt(3)\n"
"v_mfma_f32_32x32x16_bf16 v[16:31], v[112:115], v[124:127], v[16:31]\n"
"ds_read_b64_tr_b16 v[100:101], v246 offset:12288\n"
"ds_read_b64_tr_b16 v[102:103], v246 offset:12800\n"
"s_add_u32 m0, s95, 122880\n"
"s_lshl_b32 s55, s74, 1\n"
"s_add_u32 s55, s55, 5\n"
"s_and_b32 s55, s55, 7\n"
"s_lshl_b32 s55, s55, 15\n"
"s_add_u32 s55, s55, s93\n"
"s_add_u32 s55, s55, 4096\n"
"buffer_load_dwordx4 v240, s[80:83], s55 offen sc1 lds\n"
"s_waitcnt lgkmcnt(2)\n"
"v_mfma_f32_32x32x16_bf16 v[0:15], v[96:99], v[120:123], v[0:15]\n"
"ds_read_b128 v[124:127], v251 offset:3072\n"
"ds_read_b64_tr_b16 v[108:109], v245 offset:18432\n"
"ds_read_b64_tr_b16 v[110:111], v245 offset:18944\n"
"s_waitcnt lgkmcnt(3)\n"
"v_mfma_f32_32x32x16_bf16 v[16:31], v[100:103], v[120:123], v[16:31]\n"
"ds_read_b64_tr_b16 v[112:113], v246 offset:18432\n"
"ds_read_b64_tr_b16 v[114:115], v246 offset:18944\n"
"buffer_load_dwordx4 v240, s[80:83], s55 offen offset:1024 sc1 lds\n"
"s_waitcnt vmcnt(10)\n"
"s_waitcnt lgkmcnt(0)\n"
"s_barrier\n"
"v_mfma_f32_32x32x16_bf16 v[0:15], v[108:111], v[124:127], v[0:15]\n"
"ds_read_b128 v[120:123], v251 offset:40960\n"
"ds_read_b64_tr_b16 v[96:97], v245 offset:40960\n"
"ds_read_b64_tr_b16 v[98:99], v245 offset:41472\n"
"v_mfma_f32_32x32x16_bf16 v[16:31], v[112:115], v[124:127], v[16:31]\n"
"ds_read_b64_tr_b16 v[100:101], v246 offset:40960\n"
"ds_read_b64_tr_b16 v[102:103], v246 offset:41472\n"
"s_add_u32 m0, s94, 0\n"
"s_lshl_b32 s54, s74, 1\n"
"s_add_u32 s54, s54, 6\n"
"s_and_b32 s54, s54, 7\n"
"s_mul_i32 s54, s54, 147456\n"
"s_add_u32 s54, s54, s92\n"
"buffer_load_dwordx4 v242, s[76:79], s54 offen lds\n"
"buffer_load_dwordx4 v243, s[76:79], s54 offen offset:1024 lds\n"
"s_waitcnt lgkmcnt(2)\n"
"v_mfma_f32_32x32x16_bf16 v[0:15], v[96:99], v[120:123], v[0:15]\n"
"ds_read_b128 v[124:127], v251 offset:41984\n"
"ds_read_b64_tr_b16 v[108:109], v245 offset:47104\n"
"ds_read_b64_tr_b16 v[110:111], v245 offset:47616\n"
"s_waitcnt lgkmcnt(3)\n"
"v_mfma_f32_32x32x16_bf16 v[16:31], v[100:103], v[120:123], v[16:31]\n"
"ds_read_b64_tr_b16 v[112:113], v246 offset:47104\n"
"ds_read_b64_tr_b16 v[114:115], v246 offset:47616\n"
"buffer_load_dwordx4 v244, s[76:79], s54 offen offset:2048 lds\n"
"s_waitcnt lgkmcnt(2)\n"
"v_mfma_f32_32x32x16_bf16 v[0:15], v[108:111], v[124:127], v[0:15]\n"
"ds_read_b128 v[120:123], v251 offset:43008\n"
"ds_read_b64_tr_b16 v[96:97], v245 offset:53248\n"
"ds_read_b64_tr_b16 v[98:99], v245 offset:53760\n"
"s_waitcnt lgkmcnt(3)\n"
"v_mfma_f32_32x32x16_bf16 v[16:31], v[112:115], v[124:127], v[16:31]\n"
"ds_read_b64_tr_b16 v[100:101], v246 offset:53248\n"
"ds_read_b64_tr_b16 v[102:103], v246 offset:53760\n"
"s_add_u32 m0, s95, 0\n"
"s_lshl_b32 s55, s74, 1\n"
"s_add_u32 s55, s55, 6\n"
"s_and_b32 s55, s55, 7\n"
"s_lshl_b32 s55, s55, 15\n"
"s_add_u32 s55, s55, s93\n"
"buffer_load_dwordx4 v240, s[80:83], s55 offen sc1 lds\n"
"s_waitcnt lgkmcnt(2)\n"
"v_mfma_f32_32x32x16_bf16 v[0:15], v[96:99], v[120:123], v[0:15]\n"
"ds_read_b128 v[124:127], v251 offset:44032\n"
"ds_read_b64_tr_b16 v[108:109], v245 offset:59392\n"
"ds_read_b64_tr_b16 v[110:111], v245 offset:59904\n"
"s_waitcnt lgkmcnt(3)\n"
"v_mfma_f32_32x32x16_bf16 v[16:31], v[100:103], v[120:123], v[16:31]\n"
"ds_read_b64_tr_b16 v[112:113], v246 offset:59392\n"
"ds_read_b64_tr_b16 v[114:115], v246 offset:59904\n"
"buffer_load_dwordx4 v240, s[80:83], s55 offen offset:1024 sc1 lds\n"
"s_waitcnt vmcnt(10)\n"
"s_waitcnt lgkmcnt(0)\n"
"s_barrier\n"
"v_mfma_f32_32x32x16_bf16 v[0:15], v[108:111], v[124:127], v[0:15]\n"
"ds_read_b128 v[120:123], v252 offset:0\n"
"ds_read_b64_tr_b16 v[96:97], v248 offset:0\n"
"ds_read_b64_tr_b16 v[98:99], v248 offset:512\n"
"v_mfma_f32_32x32x16_bf16 v[16:31], v[112:115], v[124:127], v[16:31]\n"
"ds_read_b64_tr_b16 v[100:101], v249 offset:0\n"
"ds_read_b64_tr_b16 v[102:103], v249 offset:512\n"
"s_add_u32 m0, s94, 40960\n"
"s_lshl_b32 s54, s74, 1\n"
"s_add_u32 s54, s54, 6\n"
"s_and_b32 s54, s54, 7\n"
"s_mul_i32 s54, s54, 147456\n"
"s_add_u32 s54, s54, s92\n"
"s_add_u32 s54, s54, 73728\n"
"buffer_load_dwordx4 v242, s[76:79], s54 offen lds\n"
"buffer_load_dwordx4 v243, s[76:79], s54 offen offset:1024 lds\n"
"s_waitcnt lgkmcnt(2)\n"
"v_mfma_f32_32x32x16_bf16 v[0:15], v[96:99], v[120:123], v[0:15]\n"
"ds_read_b128 v[124:127], v252 offset:1024\n"
"ds_read_b64_tr_b16 v[108:109], v248 offset:6144\n"
"ds_read_b64_tr_b16 v[110:111], v248 offset:6656\n"
"s_waitcnt lgkmcnt(3)\n"
"v_mfma_f32_32x32x16_bf16 v[16:31], v[100:103], v[120:123], v[16:31]\n"
"ds_read_b64_tr_b16 v[112:113], v249 offset:6144\n"
"ds_read_b64_tr_b16 v[114:115], v249 offset:6656\n"
"buffer_load_dwordx4 v244, s[76:79], s54 offen offset:2048 lds\n"
"s_waitcnt lgkmcnt(2)\n"
"v_mfma_f32_32x32x16_bf16 v[0:15], v[108:111], v[124:127], v[0:15]\n"
"ds_read_b128 v[120:123], v252 offset:2048\n"
"ds_read_b64_tr_b16 v[96:97], v248 offset:12288\n"
"ds_read_b64_tr_b16 v[98:99], v248 offset:12800\n"
"s_waitcnt lgkmcnt(3)\n"
"v_mfma_f32_32x32x16_bf16 v[16:31], v[112:115], v[124:127], v[16:31]\n"
"ds_read_b64_tr_b16 v[100:101], v249 offset:12288\n"
"ds_read_b64_tr_b16 v[102:103], v249 offset:12800\n"
"s_add_u32 m0, s95, 40960\n"
"s_lshl_b32 s55, s74, 1\n"
"s_add_u32 s55, s55, 6\n"
"s_and_b32 s55, s55, 7\n"
"s_lshl_b32 s55, s55, 15\n"
"s_add_u32 s55, s55, s93\n"
"s_add_u32 s55, s55, 4096\n"
"buffer_load_dwordx4 v240, s[80:83], s55 offen sc1 lds\n"
"s_waitcnt lgkmcnt(2)\n"
"v_mfma_f32_32x32x16_bf16 v[0:15], v[96:99], v[120:123], v[0:15]\n"
"ds_read_b128 v[124:127], v252 offset:3072\n"
"ds_read_b64_tr_b16 v[108:109], v248 offset:18432\n"
"ds_read_b64_tr_b16 v[110:111], v248 offset:18944\n"
"s_waitcnt lgkmcnt(3)\n"
"v_mfma_f32_32x32x16_bf16 v[16:31], v[100:103], v[120:123], v[16:31]\n"
"ds_read_b64_tr_b16 v[112:113], v249 offset:18432\n"
"ds_read_b64_tr_b16 v[114:115], v249 offset:18944\n"
"buffer_load_dwordx4 v240, s[80:83], s55 offen offset:1024 sc1 lds\n"
"s_waitcnt vmcnt(10)\n"
"s_waitcnt lgkmcnt(0)\n"
"s_barrier\n"
"v_mfma_f32_32x32x16_bf16 v[0:15], v[108:111], v[124:127], v[0:15]\n"
"ds_read_b128 v[120:123], v252 offset:40960\n"
"ds_read_b64_tr_b16 v[96:97], v248 offset:40960\n"
"ds_read_b64_tr_b16 v[98:99], v248 offset:41472\n"
"v_mfma_f32_32x32x16_bf16 v[16:31], v[112:115], v[124:127], v[16:31]\n"
"ds_read_b64_tr_b16 v[100:101], v249 offset:40960\n"
"ds_read_b64_tr_b16 v[102:103], v249 offset:41472\n"
"s_add_u32 m0, s94, 81920\n"
"s_lshl_b32 s54, s74, 1\n"
"s_add_u32 s54, s54, 7\n"
"s_and_b32 s54, s54, 7\n"
"s_mul_i32 s54, s54, 147456\n"
"s_add_u32 s54, s54, s92\n"
"buffer_load_dwordx4 v242, s[76:79], s54 offen lds\n"
"buffer_load_dwordx4 v243, s[76:79], s54 offen offset:1024 lds\n"
"s_waitcnt lgkmcnt(2)\n"
"v_mfma_f32_32x32x16_bf16 v[0:15], v[96:99], v[120:123], v[0:15]\n"
"ds_read_b128 v[124:127], v252 offset:41984\n"
"ds_read_b64_tr_b16 v[108:109], v248 offset:47104\n"
"ds_read_b64_tr_b16 v[110:111], v248 offset:47616\n"
"s_waitcnt lgkmcnt(3)\n"
"v_mfma_f32_32x32x16_bf16 v[16:31], v[100:103], v[120:123], v[16:31]\n"
"ds_read_b64_tr_b16 v[112:113], v249 offset:47104\n"
"ds_read_b64_tr_b16 v[114:115], v249 offset:47616\n"
"buffer_load_dwordx4 v244, s[76:79], s54 offen offset:2048 lds\n"
"s_waitcnt lgkmcnt(2)\n"
"v_mfma_f32_32x32x16_bf16 v[0:15], v[108:111], v[124:127], v[0:15]\n"
"ds_read_b128 v[120:123], v252 offset:43008\n"
"ds_read_b64_tr_b16 v[96:97], v248 offset:53248\n"
"ds_read_b64_tr_b16 v[98:99], v248 offset:53760\n"
"s_waitcnt lgkmcnt(3)\n"
"v_mfma_f32_32x32x16_bf16 v[16:31], v[112:115], v[124:127], v[16:31]\n"
"ds_read_b64_tr_b16 v[100:101], v249 offset:53248\n"
"ds_read_b64_tr_b16 v[102:103], v249 offset:53760\n"
"s_add_u32 m0, s95, 81920\n"
"s_lshl_b32 s55, s74, 1\n"
"s_add_u32 s55, s55, 7\n"
"s_and_b32 s55, s55, 7\n"
"s_lshl_b32 s55, s55, 15\n"
"s_add_u32 s55, s55, s93\n"
"buffer_load_dwordx4 v240, s[80:83], s55 offen sc1 lds\n"
"s_waitcnt lgkmcnt(2)\n"
"v_mfma_f32_32x32x16_bf16 v[0:15], v[96:99], v[120:123], v[0:15]\n"
"ds_read_b128 v[124:127], v252 offset:44032\n"
"ds_read_b64_tr_b16 v[108:109], v248 offset:59392\n"
"ds_read_b64_tr_b16 v[110:111], v248 offset:59904\n"
"s_waitcnt lgkmcnt(3)\n"
"v_mfma_f32_32x32x16_bf16 v[16:31], v[100:103], v[120:123], v[16:31]\n"
"ds_read_b64_tr_b16 v[112:113], v249 offset:59392\n"
"ds_read_b64_tr_b16 v[114:115], v249 offset:59904\n"
"buffer_load_dwordx4 v240, s[80:83], s55 offen offset:1024 sc1 lds\n"
"s_waitcnt vmcnt(10)\n"
"s_waitcnt lgkmcnt(0)\n"
"s_barrier\n"
"v_mfma_f32_32x32x16_bf16 v[0:15], v[108:111], v[124:127], v[0:15]\n"
"ds_read_b128 v[120:123], v251 offset:0\n"
"ds_read_b64_tr_b16 v[96:97], v245 offset:0\n"
"ds_read_b64_tr_b16 v[98:99], v245 offset:512\n"
"v_mfma_f32_32x32x16_bf16 v[16:31], v[112:115], v[124:127], v[16:31]\n"
"ds_read_b64_tr_b16 v[100:101], v246 offset:0\n"
"ds_read_b64_tr_b16 v[102:103], v246 offset:512\n"
"s_add_u32 m0, s94, 122880\n"
"s_lshl_b32 s54, s74, 1\n"
"s_add_u32 s54, s54, 7\n"
"s_and_b32 s54, s54, 7\n"
"s_mul_i32 s54, s54, 147456\n"
"s_add_u32 s54, s54, s92\n"
"s_add_u32 s54, s54, 73728\n"
"buffer_load_dwordx4 v242, s[76:79], s54 offen lds\n"
"buffer_load_dwordx4 v243, s[76:79], s54 offen offset:1024 lds\n"
"s_waitcnt lgkmcnt(2)\n"
"v_mfma_f32_32x32x16_bf16 v[0:15], v[96:99], v[120:123], v[0:15]\n"
"ds_read_b128 v[124:127], v251 offset:1024\n"
"ds_read_b64_tr_b16 v[108:109], v245 offset:6144\n"
"ds_read_b64_tr_b16 v[110:111], v245 offset:6656\n"
"s_waitcnt lgkmcnt(3)\n"
"v_mfma_f32_32x32x16_bf16 v[16:31], v[100:103], v[120:123], v[16:31]\n"
"ds_read_b64_tr_b16 v[112:113], v246 offset:6144\n"
"ds_read_b64_tr_b16 v[114:115], v246 offset:6656\n"
"buffer_load_dwordx4 v244, s[76:79], s54 offen offset:2048 lds\n"
"s_waitcnt lgkmcnt(2)\n"
"v_mfma_f32_32x32x16_bf16 v[0:15], v[108:111], v[124:127], v[0:15]\n"
"ds_read_b128 v[120:123], v251 offset:2048\n"
"ds_read_b64_tr_b16 v[96:97], v245 offset:12288\n"
"ds_read_b64_tr_b16 v[98:99], v245 offset:12800\n"
"s_waitcnt lgkmcnt(3)\n"
"v_mfma_f32_32x32x16_bf16 v[16:31], v[112:115], v[124:127], v[16:31]\n"
"ds_read_b64_tr_b16 v[100:101], v246 offset:12288\n"
"ds_read_b64_tr_b16 v[102:103], v246 offset:12800\n"
"s_add_u32 m0, s95, 122880\n"
"s_lshl_b32 s55, s74, 1\n"
"s_add_u32 s55, s55, 7\n"
"s_and_b32 s55, s55, 7\n"
"s_lshl_b32 s55, s55, 15\n"
"s_add_u32 s55, s55, s93\n"
"s_add_u32 s55, s55, 4096\n"
"buffer_load_dwordx4 v240, s[80:83], s55 offen sc1 lds\n"
"s_waitcnt lgkmcnt(2)\n"
"v_mfma_f32_32x32x16_bf16 v[0:15], v[96:99], v[120:123], v[0:15]\n"
"ds_read_b128 v[124:127], v251 offset:3072\n"
"ds_read_b64_tr_b16 v[108:109], v245 offset:18432\n"
"ds_read_b64_tr_b16 v[110:111], v245 offset:18944\n"
"s_waitcnt lgkmcnt(3)\n"
"v_mfma_f32_32x32x16_bf16 v[16:31], v[100:103], v[120:123], v[16:31]\n"
"ds_read_b64_tr_b16 v[112:113], v246 offset:18432\n"
"ds_read_b64_tr_b16 v[114:115], v246 offset:18944\n"
"buffer_load_dwordx4 v240, s[80:83], s55 offen offset:1024 sc1 lds\n"
"s_waitcnt vmcnt(10)\n"
"s_waitcnt lgkmcnt(0)\n"
"s_barrier\n"
"v_mfma_f32_32x32x16_bf16 v[0:15], v[108:111], v[124:127], v[0:15]\n"
"ds_read_b128 v[120:123], v251 offset:40960\n"
"ds_read_b64_tr_b16 v[96:97], v245 offset:40960\n"
"ds_read_b64_tr_b16 v[98:99], v245 offset:41472\n"
"v_mfma_f32_32x32x16_bf16 v[16:31], v[112:115], v[124:127], v[16:31]\n"
"ds_read_b64_tr_b16 v[100:101], v246 offset:40960\n"
"ds_read_b64_tr_b16 v[102:103], v246 offset:41472\n"
"s_waitcnt lgkmcnt(2)\n"
"v_mfma_f32_32x32x16_bf16 v[0:15], v[96:99], v[120:123], v[0:15]\n"
"ds_read_b128 v[124:127], v251 offset:41984\n"
"ds_read_b64_tr_b16 v[108:109], v245 offset:47104\n"
"ds_read_b64_tr_b16 v[110:111], v245 offset:47616\n"
"s_waitcnt lgkmcnt(3)\n"
"v_mfma_f32_32x32x16_bf16 v[16:31], v[100:103], v[120:123], v[16:31]\n"
"ds_read_b64_tr_b16 v[112:113], v246 offset:47104\n"
"ds_read_b64_tr_b16 v[114:115], v246 offset:47616\n"
"s_waitcnt lgkmcnt(2)\n"
"v_mfma_f32_32x32x16_bf16 v[0:15], v[108:111], v[124:127], v[0:15]\n"
"ds_read_b128 v[120:123], v251 offset:43008\n"
"ds_read_b64_tr_b16 v[96:97], v245 offset:53248\n"
"ds_read_b64_tr_b16 v[98:99], v245 offset:53760\n"
"s_waitcnt lgkmcnt(3)\n"
"v_mfma_f32_32x32x16_bf16 v[16:31], v[112:115], v[124:127], v[16:31]\n"
"ds_read_b64_tr_b16 v[100:101], v246 offset:53248\n"
"ds_read_b64_tr_b16 v[102:103], v246 offset:53760\n"
"s_waitcnt lgkmcnt(2)\n"
"v_mfma_f32_32x32x16_bf16 v[0:15], v[96:99], v[120:123], v[0:15]\n"
"ds_read_b128 v[124:127], v251 offset:44032\n"
"ds_read_b64_tr_b16 v[108:109], v245 offset:59392\n"
"ds_read_b64_tr_b16 v[110:111], v245 offset:59904\n"
"s_waitcnt lgkmcnt(3)\n"
"v_mfma_f32_32x32x16_bf16 v[16:31], v[100:103], v[120:123], v[16:31]\n"
"ds_read_b64_tr_b16 v[112:113], v246 offset:59392\n"
"ds_read_b64_tr_b16 v[114:115], v246 offset:59904\n"
"s_waitcnt vmcnt(5)\n"
"s_waitcnt lgkmcnt(0)\n"
"s_barrier\n"
"v_mfma_f32_32x32x16_bf16 v[0:15], v[108:111], v[124:127], v[0:15]\n"
"ds_read_b128 v[120:123], v252 offset:0\n"
"ds_read_b64_tr_b16 v[96:97], v248 offset:0\n"
"ds_read_b64_tr_b16 v[98:99], v248 offset:512\n"
"v_mfma_f32_32x32x16_bf16 v[16:31], v[112:115], v[124:127], v[16:31]\n"
"ds_read_b64_tr_b16 v[100:101], v249 offset:0\n"
"ds_read_b64_tr_b16 v[102:103], v249 offset:512\n"
"s_waitcnt lgkmcnt(2)\n"
"v_mfma_f32_32x32x16_bf16 v[0:15], v[96:99], v[120:123], v[0:15]\n"
"ds_read_b128 v[124:127], v252 offset:1024\n"
"ds_read_b64_tr_b16 v[108:109], v248 offset:6144\n"
"ds_read_b64_tr_b16 v[110:111], v248 offset:6656\n"
"s_waitcnt lgkmcnt(3)\n"
"v_mfma_f32_32x32x16_bf16 v[16:31], v[100:103], v[120:123], v[16:31]\n"
"ds_read_b64_tr_b16 v[112:113], v249 offset:6144\n"
"ds_read_b64_tr_b16 v[114:115], v249 offset:6656\n"
"s_waitcnt lgkmcnt(2)\n"
"v_mfma_f32_32x32x16_bf16 v[0:15], v[108:111], v[124:127], v[0:15]\n"
"ds_read_b128 v[120:123], v252 offset:2048\n"
"ds_read_b64_tr_b16 v[96:97], v248 offset:12288\n"
"ds_read_b64_tr_b16 v[98:99], v248 offset:12800\n"
"s_waitcnt lgkmcnt(3)\n"
"v_mfma_f32_32x32x16_bf16 v[16:31], v[112:115], v[124:127], v[16:31]\n"
"ds_read_b64_tr_b16 v[100:101], v249 offset:12288\n"
"ds_read_b64_tr_b16 v[102:103], v249 offset:12800\n"
"s_waitcnt lgkmcnt(2)\n"
"v_mfma_f32_32x32x16_bf16 v[0:15], v[96:99], v[120:123], v[0:15]\n"
"ds_read_b128 v[124:127], v252 offset:3072\n"
"ds_read_b64_tr_b16 v[108:109], v248 offset:18432\n"
"ds_read_b64_tr_b16 v[110:111], v248 offset:18944\n"
"s_waitcnt lgkmcnt(3)\n"
"v_mfma_f32_32x32x16_bf16 v[16:31], v[100:103], v[120:123], v[16:31]\n"
"ds_read_b64_tr_b16 v[112:113], v249 offset:18432\n"
"ds_read_b64_tr_b16 v[114:115], v249 offset:18944\n"
"s_waitcnt vmcnt(0)\n"
"s_waitcnt lgkmcnt(0)\n"
"s_barrier\n"
"v_mfma_f32_32x32x16_bf16 v[0:15], v[108:111], v[124:127], v[0:15]\n"
"ds_read_b128 v[120:123], v252 offset:40960\n"
"ds_read_b64_tr_b16 v[96:97], v248 offset:40960\n"
"ds_read_b64_tr_b16 v[98:99], v248 offset:41472\n"
"v_mfma_f32_32x32x16_bf16 v[16:31], v[112:115], v[124:127], v[16:31]\n"
"ds_read_b64_tr_b16 v[100:101], v249 offset:40960\n"
"ds_read_b64_tr_b16 v[102:103], v249 offset:41472\n"
"s_waitcnt lgkmcnt(2)\n"
"v_mfma_f32_32x32x16_bf16 v[0:15], v[96:99], v[120:123], v[0:15]\n"
"ds_read_b128 v[124:127], v252 offset:41984\n"
"ds_read_b64_tr_b16 v[108:109], v248 offset:47104\n"
"ds_read_b64_tr_b16 v[110:111], v248 offset:47616\n"
"s_waitcnt lgkmcnt(3)\n"
"v_mfma_f32_32x32x16_bf16 v[16:31], v[100:103], v[120:123], v[16:31]\n"
"ds_read_b64_tr_b16 v[112:113], v249 offset:47104\n"
"ds_read_b64_tr_b16 v[114:115], v249 offset:47616\n"
"s_waitcnt lgkmcnt(2)\n"
"v_mfma_f32_32x32x16_bf16 v[0:15], v[108:111], v[124:127], v[0:15]\n"
"ds_read_b128 v[120:123], v252 offset:43008\n"
"ds_read_b64_tr_b16 v[96:97], v248 offset:53248\n"
"ds_read_b64_tr_b16 v[98:99], v248 offset:53760\n"
"s_waitcnt lgkmcnt(3)\n"
"v_mfma_f32_32x32x16_bf16 v[16:31], v[112:115], v[124:127], v[16:31]\n"
"ds_read_b64_tr_b16 v[100:101], v249 offset:53248\n"
"ds_read_b64_tr_b16 v[102:103], v249 offset:53760\n"
"s_waitcnt lgkmcnt(2)\n"
"v_mfma_f32_32x32x16_bf16 v[0:15], v[96:99], v[120:123], v[0:15]\n"
"ds_read_b128 v[124:127], v252 offset:44032\n"
"ds_read_b64_tr_b16 v[108:109], v248 offset:59392\n"
"ds_read_b64_tr_b16 v[110:111], v248 offset:59904\n"
"s_waitcnt lgkmcnt(3)\n"
"v_mfma_f32_32x32x16_bf16 v[16:31], v[100:103], v[120:123], v[16:31]\n"
"ds_read_b64_tr_b16 v[112:113], v249 offset:59392\n"
"ds_read_b64_tr_b16 v[114:115], v249 offset:59904\n"
"s_waitcnt lgkmcnt(2)\n"
"v_mfma_f32_32x32x16_bf16 v[0:15], v[108:111], v[124:127], v[0:15]\n"
"s_waitcnt lgkmcnt(0)\n"
"v_mfma_f32_32x32x16_bf16 v[16:31], v[112:115], v[124:127], v[16:31]\n"
"s_nop 11\n"
"s_cmp_eq_u32 s46, s73\n"
"s_cbranch_scc1 KF_NS1\n"
"s_lshl_b32 s12, s46, 2\n"
"s_add_u32 s12, s12, s73\n"
"s_mul_i32 s12, s12, 9216\n"
"s_add_u32 s12, s12, 6144\n"
"s_add_u32 s13, s12, 4096\n"
"v_cvt_pk_bf16_f32 v48, v0, v1\n"
"v_cvt_pk_bf16_f32 v49, v2, v3\n"
"v_cvt_pk_bf16_f32 v50, v4, v5\n"
"v_cvt_pk_bf16_f32 v51, v6, v7\n"
"buffer_store_dwordx4 v[48:51], v240, s[84:87], s12 offen sc1\n"
"v_cvt_pk_bf16_f32 v52, v8, v9\n"
"v_cvt_pk_bf16_f32 v53, v10, v11\n"
"v_cvt_pk_bf16_f32 v54, v12, v13\n"
"v_cvt_pk_bf16_f32 v55, v14, v15\n"
"buffer_store_dwordx4 v[52:55], v240, s[84:87], s12 offen offset:1024 sc1\n"
"v_cvt_pk_bf16_f32 v56, v16, v17\n"
"v_cvt_pk_bf16_f32 v57, v18, v19\n"
"v_cvt_pk_bf16_f32 v58, v20, v21\n"
"v_cvt_pk_bf16_f32 v59, v22, v23\n"
"buffer_store_dwordx4 v[56:59], v240, s[84:87], s12 offen offset:2048 sc1\n"
"KF_NS1:\n"
"s_waitcnt vmcnt(0)\n"
"s_waitcnt lgkmcnt(0)\n"
"s_barrier\n"
"s_waitcnt lgkmcnt(0)\n"
"s_barrier\n"
"s_cmp_eq_u32 s46, s73\n"
"s_cbranch_scc0 KF_LV1\n"
"s_lshl_b32 s17, s73, 2\n"
"s_add_u32 s12, s73, 1\n"
"s_and_b32 s12, s12, 3\n"
"s_add_u32 s12, s12, s17\n"
"s_mul_i32 s12, s12, 9216\n"
"s_add_u32 s12, s12, 6144\n"
"s_add_u32 s13, s12, 4096\n"
"buffer_load_dwordx4 v[48:51], v240, s[84:87], s12 offen sc1\n"
"buffer_load_dwordx4 v[52:55], v240, s[84:87], s12 offen offset:1024 sc1\n"
"buffer_load_dwordx4 v[56:59], v240, s[84:87], s12 offen offset:2048 sc1\n"
"s_add_u32 s12, s73, 2\n"
"s_and_b32 s12, s12, 3\n"
"s_add_u32 s12, s12, s17\n"
"s_mul_i32 s12, s12, 9216\n"
"s_add_u32 s12, s12, 6144\n"
"s_add_u32 s13, s12, 4096\n"
"buffer_load_dwordx4 v[60:63], v240, s[84:87], s12 offen sc1\n"
"buffer_load_dwordx4 v[64:67], v240, s[84:87], s12 offen offset:1024 sc1\n"
"buffer_load_dwordx4 v[68:71], v240, s[84:87], s12 offen offset:2048 sc1\n"
"s_add_u32 s12, s73, 3\n"
"s_and_b32 s12, s12, 3\n"
"s_add_u32 s12, s12, s17\n"
"s_mul_i32 s12, s12, 9216\n"
"s_add_u32 s12, s12, 6144\n"
"s_add_u32 s13, s12, 4096\n"
"buffer_load_dwordx4 v[72:75], v240, s[84:87], s12 offen sc1\n"
"buffer_load_dwordx4 v[76:79], v240, s[84:87], s12 offen offset:1024 sc1\n"
"buffer_load_dwordx4 v[80:83], v240, s[84:87], s12 offen offset:2048 sc1\n"
"v_lshrrev_b32 v230, 4, v240\n"
"v_and_b32 v231, 31, v230\n"
"v_mul_u32_u24 v231, 0x900, v231\n"
"v_lshrrev_b32 v230, 5, v230\n"
"v_lshl_add_u32 v231, v230, 4, v231\n"
"s_mul_i32 s59, s46, 73728\n"
"s_waitcnt vmcnt(8)\n"
"v_lshlrev_b32 v234, 16, v48\n"
"v_and_b32 v235, 0xffff0000, v48\n"
"v_add_f32 v0, v0, v234\n"
"v_add_f32 v1, v1, v235\n"
"v_lshlrev_b32 v236, 16, v49\n"
"v_and_b32 v237, 0xffff0000, v49\n"
"v_add_f32 v2, v2, v236\n"
"v_add_f32 v3, v3, v237\n"
"v_lshlrev_b32 v234, 16, v50\n"
"v_and_b32 v235, 0xffff0000, v50\n"
"v_add_f32 v4, v4, v234\n"
"v_add_f32 v5, v5, v235\n"
"v_lshlrev_b32 v236, 16, v51\n"
"v_and_b32 v237, 0xffff0000, v51\n"
"v_add_f32 v6, v6, v236\n"
"v_add_f32 v7, v7, v237\n"
"s_waitcnt vmcnt(7)\n"
"v_lshlrev_b32 v234, 16, v52\n"
"v_and_b32 v235, 0xffff0000, v52\n"
"v_add_f32 v8, v8, v234\n"
"v_add_f32 v9, v9, v235\n"
"v_lshlrev_b32 v236, 16, v53\n"
"v_and_b32 v237, 0xffff0000, v53\n"
"v_add_f32 v10, v10, v236\n"
"v_add_f32 v11, v11, v237\n"
"v_lshlrev_b32 v234, 16, v54\n"
"v_and_b32 v235, 0xffff0000, v54\n"
"v_add_f32 v12, v12, v234\n"
"v_add_f32 v13, v13, v235\n"
"v_lshlrev_b32 v236, 16, v55\n"
"v_and_b32 v237, 0xffff0000, v55\n"
"v_add_f32 v14, v14, v236\n"
"v_add_f32 v15, v15, v237\n"
"s_waitcnt vmcnt(6)\n"
"v_lshlrev_b32 v234, 16, v56\n"
"v_and_b32 v235, 0xffff0000, v56\n"
"v_add_f32 v16, v16, v234\n"
"v_add_f32 v17, v17, v235\n"
"v_lshlrev_b32 v236, 16, v57\n"
"v_and_b32 v237, 0xffff0000, v57\n"
"v_add_f32 v18, v18, v236\n"
"v_add_f32 v19, v19, v237\n"
"v_lshlrev_b32 v234, 16, v58\n"
"v_and_b32 v235, 0xffff0000, v58\n"
"v_add_f32 v20, v20, v234\n"
"v_add_f32 v21, v21, v235\n"
"v_lshlrev_b32 v236, 16, v59\n"
"v_and_b32 v237, 0xffff0000, v59\n"
"v_add_f32 v22, v22, v236\n"
"v_add_f32 v23, v23, v237\n"
"s_waitcnt vmcnt(5)\n"
"v_lshlrev_b32 v234, 16, v60\n"
"v_and_b32 v235, 0xffff0000, v60\n"
"v_add_f32 v0, v0, v234\n"
"v_add_f32 v1, v1, v235\n"
"v_lshlrev_b32 v236, 16, v61\n"
"v_and_b32 v237, 0xffff0000, v61\n"
"v_add_f32 v2, v2, v236\n"
"v_add_f32 v3, v3, v237\n"
"v_lshlrev_b32 v234, 16, v62\n"
"v_and_b32 v235, 0xffff0000, v62\n"
"v_add_f32 v4, v4, v234\n"
"v_add_f32 v5, v5, v235\n"
"v_lshlrev_b32 v236, 16, v63\n"
"v_and_b32 v237, 0xffff0000, v63\n"
"v_add_f32 v6, v6, v236\n"
"v_add_f32 v7, v7, v237\n"
"s_waitcnt vmcnt(4)\n"
"v_lshlrev_b32 v234, 16, v64\n"
"v_and_b32 v235, 0xffff0000, v64\n"
"v_add_f32 v8, v8, v234\n"
"v_add_f32 v9, v9, v235\n"
"v_lshlrev_b32 v236, 16, v65\n"
"v_and_b32 v237, 0xffff0000, v65\n"
"v_add_f32 v10, v10, v236\n"
"v_add_f32 v11, v11, v237\n"
"v_lshlrev_b32 v234, 16, v66\n"
"v_and_b32 v235, 0xffff0000, v66\n"
"v_add_f32 v12, v12, v234\n"
"v_add_f32 v13, v13, v235\n"
"v_lshlrev_b32 v236, 16, v67\n"
"v_and_b32 v237, 0xffff0000, v67\n"
"v_add_f32 v14, v14, v236\n"
"v_add_f32 v15, v15, v237\n"
"s_waitcnt vmcnt(3)\n"
"v_lshlrev_b32 v234, 16, v68\n"
"v_and_b32 v235, 0xffff0000, v68\n"
"v_add_f32 v16, v16, v234\n"
"v_add_f32 v17, v17, v235\n"
"v_lshlrev_b32 v236, 16, v69\n"
"v_and_b32 v237, 0xffff0000, v69\n"
"v_add_f32 v18, v18, v236\n"
"v_add_f32 v19, v19, v237\n"
"v_lshlrev_b32 v234, 16, v70\n"
"v_and_b32 v235, 0xffff0000, v70\n"
"v_add_f32 v20, v20, v234\n"
"v_add_f32 v21, v21, v235\n"
"v_lshlrev_b32 v236, 16, v71\n"
"v_and_b32 v237, 0xffff0000, v71\n"
"v_add_f32 v22, v22, v236\n"
"v_add_f32 v23, v23, v237\n"
"s_waitcnt vmcnt(2)\n"
"v_lshlrev_b32 v234, 16, v72\n"
"v_and_b32 v235, 0xffff0000, v72\n"
"v_add_f32 v0, v0, v234\n"
"v_add_f32 v1, v1, v235\n"
"v_lshlrev_b32 v236, 16, v73\n"
"v_and_b32 v237, 0xffff0000, v73\n"
"v_add_f32 v2, v2, v236\n"
"v_add_f32 v3, v3, v237\n"
"v_lshlrev_b32 v234, 16, v74\n"
"v_and_b32 v235, 0xffff0000, v74\n"
"v_add_f32 v4, v4, v234\n"
"v_add_f32 v5, v5, v235\n"
"v_lshlrev_b32 v236, 16, v75\n"
"v_and_b32 v237, 0xffff0000, v75\n"
"v_add_f32 v6, v6, v236\n"
"v_add_f32 v7, v7, v237\n"
"s_waitcnt vmcnt(1)\n"
"v_lshlrev_b32 v234, 16, v76\n"
"v_and_b32 v235, 0xffff0000, v76\n"
"v_add_f32 v8, v8, v234\n"
"v_add_f32 v9, v9, v235\n"
"v_lshlrev_b32 v236, 16, v77\n"
"v_and_b32 v237, 0xffff0000, v77\n"
"v_add_f32 v10, v10, v236\n"
"v_add_f32 v11, v11, v237\n"
"v_lshlrev_b32 v234, 16, v78\n"
"v_and_b32 v235, 0xffff0000, v78\n"
"v_add_f32 v12, v12, v234\n"
"v_add_f32 v13, v13, v235\n"
"v_lshlrev_b32 v236, 16, v79\n"
"v_and_b32 v237, 0xffff0000, v79\n"
"v_add_f32 v14, v14, v236\n"
"v_add_f32 v15, v15, v237\n"
"s_waitcnt vmcnt(0)\n"
"v_lshlrev_b32 v234, 16, v80\n"
"v_and_b32 v235, 0xffff0000, v80\n"
"v_add_f32 v16, v16, v234\n"
"v_add_f32 v17, v17, v235\n"
"v_lshlrev_b32 v236, 16, v81\n"
"v_and_b32 v237, 0xffff0000, v81\n"
"v_add_f32 v18, v18, v236\n"
"v_add_f32 v19, v19, v237\n"
"v_lshlrev_b32 v234, 16, v82\n"
"v_and_b32 v235, 0xffff0000, v82\n"
"v_add_f32 v20, v20, v234\n"
"v_add_f32 v21, v21, v235\n"
"v_lshlrev_b32 v236, 16, v83\n"
"v_and_b32 v237, 0xffff0000, v83\n"
"v_add_f32 v22, v22, v236\n"
"v_add_f32 v23, v23, v237\n"
"v_cvt_pk_bf16_f32 v140, v0, v1\n"
"v_cvt_pk_bf16_f32 v141, v2, v3\n"
"v_cvt_pk_bf16_f32 v142, v4, v5\n"
"v_cvt_pk_bf16_f32 v143, v6, v7\n"
"s_nop 0\n"
"v_permlane32_swap_b32 v140, v142\n"
"v_permlane32_swap_b32 v141, v143\n"
"buffer_store_dwordx4 v[140:143], v231, s[88:91], s59 offen offset:192\n"
"v_cvt_pk_bf16_f32 v144, v8, v9\n"
"v_cvt_pk_bf16_f32 v145, v10, v11\n"
"v_cvt_pk_bf16_f32 v146, v12, v13\n"
"v_cvt_pk_bf16_f32 v147, v14, v15\n"
"s_nop 0\n"
"v_permlane32_swap_b32 v144, v146\n"
"v_permlane32_swap_b32 v145, v147\n"
"buffer_store_dwordx4 v[144:147], v231, s[88:91], s59 offen offset:224\n"
"v_cvt_pk_bf16_f32 v148, v16, v17\n"
"v_cvt_pk_bf16_f32 v149, v18, v19\n"
"v_cvt_pk_bf16_f32 v150, v20, v21\n"
"v_cvt_pk_bf16_f32 v151, v22, v23\n"
"s_nop 0\n"
"v_permlane32_swap_b32 v148, v150\n"
"v_permlane32_swap_b32 v149, v151\n"
"buffer_store_dwordx4 v[148:151], v231, s[88:91], s59 offen offset:256\n"
"KF_LV1:\n"
"s_branch KF_FIN\n"
"KF_FIN:\n"
"s_waitcnt vmcnt(0)\n"
"s_branch KF_END\n"
"KF_RA1:\n"
"s_nop 3\n"
"v_max_f32 v230, 0, v226\n"
"v_exp_f32 v228, -v230\n"
"v_add_f32 v225, v225, v230\n"
"v_xor_b32 v231, 0x80000000, v230\n"
"v_xor_b32 v232, 0x80000000, v225\n"
"s_nop 15\n"
"v_mfma_f32_32x32x2_f32 v[96:111], v229, v231, v[96:111]\n"
"v_mfma_f32_32x32x2_f32 v[128:143], v229, v231, v[128:143]\n"
"v_mfma_f32_32x32x2_f32 v[176:191], v229, v232, 0\n"
"s_nop 15\n"
"s_nop 15\n"
"s_mov_b32 s60, 1\n"
"s_branch KF_RB1\n"
"KF_RC1:\n"
"s_nop 15\n"
"s_nop 7\n"
"v_mul_f32 v0, v228, v0\n"
"v_mul_f32 v1, v228, v1\n"
"v_mul_f32 v2, v228, v2\n"
"v_mul_f32 v3, v228, v3\n"
"v_mul_f32 v4, v228, v4\n"
"v_mul_f32 v5, v228, v5\n"
"v_mul_f32 v6, v228, v6\n"
"v_mul_f32 v7, v228, v7\n"
"v_mul_f32 v8, v228, v8\n"
"v_mul_f32 v9, v228, v9\n"
"v_mul_f32 v10, v228, v10\n"
"v_mul_f32 v11, v228, v11\n"
"v_mul_f32 v12, v228, v12\n"
"v_mul_f32 v13, v228, v13\n"
"v_mul_f32 v14, v228, v14\n"
"v_mul_f32 v15, v228, v15\n"
"v_mul_f32 v16, v228, v16\n"
"v_mul_f32 v17, v228, v17\n"
"v_mul_f32 v18, v228, v18\n"
"v_mul_f32 v19, v228, v19\n"
"v_mul_f32 v20, v228, v20\n"
"v_mul_f32 v21, v228, v21\n"
"v_mul_f32 v22, v228, v22\n"
"v_mul_f32 v23, v228, v23\n"
"v_mul_f32 v24, v228, v24\n"
"v_mul_f32 v25, v228, v25\n"
"v_mul_f32 v26, v228, v26\n"
"v_mul_f32 v27, v228, v27\n"
"v_mul_f32 v28, v228, v28\n"
"v_mul_f32 v29, v228, v29\n"
"v_mul_f32 v30, v228, v30\n"
"v_mul_f32 v31, v228, v31\n"
"v_mul_f32 v32, v228, v32\n"
"v_mul_f32 v33, v228, v33\n"
"v_mul_f32 v34, v228, v34\n"
"v_mul_f32 v35, v228, v35\n"
"v_mul_f32 v36, v228, v36\n"
"v_mul_f32 v37, v228, v37\n"
"v_mul_f32 v38, v228, v38\n"
"v_mul_f32 v39, v228, v39\n"
"v_mul_f32 v40, v228, v40\n"
"v_mul_f32 v41, v228, v41\n"
"v_mul_f32 v42, v228, v42\n"
"v_mul_f32 v43, v228, v43\n"
"v_mul_f32 v44, v228, v44\n"
"v_mul_f32 v45, v228, v45\n"
"v_mul_f32 v46, v228, v46\n"
"v_mul_f32 v47, v228, v47\n"
"v_mul_f32 v48, v228, v48\n"
"v_mul_f32 v49, v228, v49\n"
"v_mul_f32 v50, v228, v50\n"
"v_mul_f32 v51, v228, v51\n"
"v_mul_f32 v52, v228, v52\n"
"v_mul_f32 v53, v228, v53\n"
"v_mul_f32 v54, v228, v54\n"
"v_mul_f32 v55, v228, v55\n"
"v_mul_f32 v56, v228, v56\n"
"v_mul_f32 v57, v228, v57\n"
"v_mul_f32 v58, v228, v58\n"
"v_mul_f32 v59, v228, v59\n"
"v_mul_f32 v60, v228, v60\n"
"v_mul_f32 v61, v228, v61\n"
"v_mul_f32 v62, v228, v62\n"
"v_mul_f32 v63, v228, v63\n"
"v_mul_f32 v192, v228, v192\n"
"s_nop 3\n"
"s_branch KF_RD1\n"
"KF_RA2:\n"
"s_nop 3\n"
"v_max_f32 v230, 0, v226\n"
"v_exp_f32 v228, -v230\n"
"v_add_f32 v225, v225, v230\n"
"v_xor_b32 v231, 0x80000000, v230\n"
"v_xor_b32 v232, 0x80000000, v225\n"
"s_nop 15\n"
"v_mfma_f32_32x32x2_f32 v[96:111], v229, v231, v[96:111]\n"
"v_mfma_f32_32x32x2_f32 v[112:127], v229, v231, v[112:127]\n"
"v_mfma_f32_32x32x2_f32 v[176:191], v229, v232, 0\n"
"s_nop 15\n"
"s_nop 15\n"
"s_mov_b32 s60, 1\n"
"s_branch KF_RB2\n"
"KF_RC2:\n"
"s_nop 15\n"
"s_nop 7\n"
"v_mul_f32 v0, v228, v0\n"
"v_mul_f32 v1, v228, v1\n"
"v_mul_f32 v2, v228, v2\n"
"v_mul_f32 v3, v228, v3\n"
"v_mul_f32 v4, v228, v4\n"
"v_mul_f32 v5, v228, v5\n"
"v_mul_f32 v6, v228, v6\n"
"v_mul_f32 v7, v228, v7\n"
"v_mul_f32 v8, v228, v8\n"
"v_mul_f32 v9, v228, v9\n"
"v_mul_f32 v10, v228, v10\n"
"v_mul_f32 v11, v228, v11\n"
"v_mul_f32 v12, v228, v12\n"
"v_mul_f32 v13, v228, v13\n"
"v_mul_f32 v14, v228, v14\n"
"v_mul_f32 v15, v228, v15\n"
"v_mul_f32 v16, v228, v16\n"
"v_mul_f32 v17, v228, v17\n"
"v_mul_f32 v18, v228, v18\n"
"v_mul_f32 v19, v228, v19\n"
"v_mul_f32 v20, v228, v20\n"
"v_mul_f32 v21, v228, v21\n"
"v_mul_f32 v22, v228, v22\n"
"v_mul_f32 v23, v228, v23\n"
"v_mul_f32 v24, v228, v24\n"
"v_mul_f32 v25, v228, v25\n"
"v_mul_f32 v26, v228, v26\n"
"v_mul_f32 v27, v228, v27\n"
"v_mul_f32 v28, v228, v28\n"
"v_mul_f32 v29, v228, v29\n"
"v_mul_f32 v30, v228, v30\n"
"v_mul_f32 v31, v228, v31\n"
"v_mul_f32 v32, v228, v32\n"
"v_mul_f32 v33, v228, v33\n"
"v_mul_f32 v34, v228, v34\n"
"v_mul_f32 v35, v228, v35\n"
"v_mul_f32 v36, v228, v36\n"
"v_mul_f32 v37, v228, v37\n"
"v_mul_f32 v38, v228, v38\n"
"v_mul_f32 v39, v228, v39\n"
"v_mul_f32 v40, v228, v40\n"
"v_mul_f32 v41, v228, v41\n"
"v_mul_f32 v42, v228, v42\n"
"v_mul_f32 v43, v228, v43\n"
"v_mul_f32 v44, v228, v44\n"
"v_mul_f32 v45, v228, v45\n"
"v_mul_f32 v46, v228, v46\n"
"v_mul_f32 v47, v228, v47\n"
"v_mul_f32 v48, v228, v48\n"
"v_mul_f32 v49, v228, v49\n"
"v_mul_f32 v50, v228, v50\n"
"v_mul_f32 v51, v228, v51\n"
"v_mul_f32 v52, v228, v52\n"
"v_mul_f32 v53, v228, v53\n"
"v_mul_f32 v54, v228, v54\n"
"v_mul_f32 v55, v228, v55\n"
"v_mul_f32 v56, v228, v56\n"
"v_mul_f32 v57, v228, v57\n"
"v_mul_f32 v58, v228, v58\n"
"v_mul_f32 v59, v228, v59\n"
"v_mul_f32 v60, v228, v60\n"
"v_mul_f32 v61, v228, v61\n"
"v_mul_f32 v62, v228, v62\n"
"v_mul_f32 v63, v228, v63\n"
"v_mul_f32 v192, v228, v192\n"
"s_nop 3\n"
"s_branch KF_RD2\n"
"KF_RA3:\n"
"s_nop 3\n"
"v_max_f32 v230, 0, v226\n"
"v_exp_f32 v228, -v230\n"
"v_add_f32 v225, v225, v230\n"
"v_xor_b32 v231, 0x80000000, v230\n"
"v_xor_b32 v232, 0x80000000, v225\n"
"s_nop 15\n"
"v_mfma_f32_32x32x2_f32 v[96:111], v229, v231, v[96:111]\n"
"v_mfma_f32_32x32x2_f32 v[128:143], v229, v231, v[128:143]\n"
"v_mfma_f32_32x32x2_f32 v[176:191], v229, v232, 0\n"
"s_nop 15\n"
"s_nop 15\n"
"s_mov_b32 s60, 1\n"
"s_branch KF_RB3\n"
"KF_RC3:\n"
"s_nop 15\n"
"s_nop 7\n"
"v_mul_f32 v0, v228, v0\n"
"v_mul_f32 v1, v228, v1\n"
"v_mul_f32 v2, v228, v2\n"
"v_mul_f32 v3, v228, v3\n"
"v_mul_f32 v4, v228, v4\n"
"v_mul_f32 v5, v228, v5\n"
"v_mul_f32 v6, v228, v6\n"
"v_mul_f32 v7, v228, v7\n"
"v_mul_f32 v8, v228, v8\n"
"v_mul_f32 v9, v228, v9\n"
"v_mul_f32 v10, v228, v10\n"
"v_mul_f32 v11, v228, v11\n"
"v_mul_f32 v12, v228, v12\n"
"v_mul_f32 v13, v228, v13\n"
"v_mul_f32 v14, v228, v14\n"
"v_mul_f32 v15, v228, v15\n"
"v_mul_f32 v16, v228, v16\n"
"v_mul_f32 v17, v228, v17\n"
"v_mul_f32 v18, v228, v18\n"
"v_mul_f32 v19, v228, v19\n"
"v_mul_f32 v20, v228, v20\n"
"v_mul_f32 v21, v228, v21\n"
"v_mul_f32 v22, v228, v22\n"
"v_mul_f32 v23, v228, v23\n"
"v_mul_f32 v24, v228, v24\n"
"v_mul_f32 v25, v228, v25\n"
"v_mul_f32 v26, v228, v26\n"
"v_mul_f32 v27, v228, v27\n"
"v_mul_f32 v28, v228, v28\n"
"v_mul_f32 v29, v228, v29\n"
"v_mul_f32 v30, v228, v30\n"
"v_mul_f32 v31, v228, v31\n"
"v_mul_f32 v32, v228, v32\n"
"v_mul_f32 v33, v228, v33\n"
"v_mul_f32 v34, v228, v34\n"
"v_mul_f32 v35, v228, v35\n"
"v_mul_f32 v36, v228, v36\n"
"v_mul_f32 v37, v228, v37\n"
"v_mul_f32 v38, v228, v38\n"
"v_mul_f32 v39, v228, v39\n"
"v_mul_f32 v40, v228, v40\n"
"v_mul_f32 v41, v228, v41\n"
"v_mul_f32 v42, v228, v42\n"
"v_mul_f32 v43, v228, v43\n"
"v_mul_f32 v44, v228, v44\n"
"v_mul_f32 v45, v228, v45\n"
"v_mul_f32 v46, v228, v46\n"
"v_mul_f32 v47, v228, v47\n"
"v_mul_f32 v48, v228, v48\n"
"v_mul_f32 v49, v228, v49\n"
"v_mul_f32 v50, v228, v50\n"
"v_mul_f32 v51, v228, v51\n"
"v_mul_f32 v52, v228, v52\n"
"v_mul_f32 v53, v228, v53\n"
"v_mul_f32 v54, v228, v54\n"
"v_mul_f32 v55, v228, v55\n"
"v_mul_f32 v56, v228, v56\n"
"v_mul_f32 v57, v228, v57\n"
"v_mul_f32 v58, v228, v58\n"
"v_mul_f32 v59, v228, v59\n"
"v_mul_f32 v60, v228, v60\n"
"v_mul_f32 v61, v228, v61\n"
"v_mul_f32 v62, v228, v62\n"
"v_mul_f32 v63, v228, v63\n"
"v_mul_f32 v192, v228, v192\n"
"s_nop 3\n"
"s_branch KF_RD3\n"
"KF_RA4:\n"
"s_nop 3\n"
"v_max_f32 v230, 0, v226\n"
"v_exp_f32 v228, -v230\n"
"v_add_f32 v225, v225, v230\n"
"v_xor_b32 v231, 0x80000000, v230\n"
"v_xor_b32 v232, 0x80000000, v225\n"
"s_nop 15\n"
"v_mfma_f32_32x32x2_f32 v[96:111], v229, v231, v[96:111]\n"
"v_mfma_f32_32x32x2_f32 v[112:127], v229, v231, v[112:127]\n"
"v_mfma_f32_32x32x2_f32 v[176:191], v229, v232, 0\n"
"s_nop 15\n"
"s_nop 15\n"
"s_mov_b32 s60, 1\n"
"s_branch KF_RB4\n"
"KF_RC4:\n"
"s_nop 15\n"
"s_nop 7\n"
"v_mul_f32 v0, v228, v0\n"
"v_mul_f32 v1, v228, v1\n"
"v_mul_f32 v2, v228, v2\n"
"v_mul_f32 v3, v228, v3\n"
"v_mul_f32 v4, v228, v4\n"
"v_mul_f32 v5, v228, v5\n"
"v_mul_f32 v6, v228, v6\n"
"v_mul_f32 v7, v228, v7\n"
"v_mul_f32 v8, v228, v8\n"
"v_mul_f32 v9, v228, v9\n"
"v_mul_f32 v10, v228, v10\n"
"v_mul_f32 v11, v228, v11\n"
"v_mul_f32 v12, v228, v12\n"
"v_mul_f32 v13, v228, v13\n"
"v_mul_f32 v14, v228, v14\n"
"v_mul_f32 v15, v228, v15\n"
"v_mul_f32 v16, v228, v16\n"
"v_mul_f32 v17, v228, v17\n"
"v_mul_f32 v18, v228, v18\n"
"v_mul_f32 v19, v228, v19\n"
"v_mul_f32 v20, v228, v20\n"
"v_mul_f32 v21, v228, v21\n"
"v_mul_f32 v22, v228, v22\n"
"v_mul_f32 v23, v228, v23\n"
"v_mul_f32 v24, v228, v24\n"
"v_mul_f32 v25, v228, v25\n"
"v_mul_f32 v26, v228, v26\n"
"v_mul_f32 v27, v228, v27\n"
"v_mul_f32 v28, v228, v28\n"
"v_mul_f32 v29, v228, v29\n"
"v_mul_f32 v30, v228, v30\n"
"v_mul_f32 v31, v228, v31\n"
"v_mul_f32 v32, v228, v32\n"
"v_mul_f32 v33, v228, v33\n"
"v_mul_f32 v34, v228, v34\n"
"v_mul_f32 v35, v228, v35\n"
"v_mul_f32 v36, v228, v36\n"
"v_mul_f32 v37, v228, v37\n"
"v_mul_f32 v38, v228, v38\n"
"v_mul_f32 v39, v228, v39\n"
"v_mul_f32 v40, v228, v40\n"
"v_mul_f32 v41, v228, v41\n"
"v_mul_f32 v42, v228, v42\n"
"v_mul_f32 v43, v228, v43\n"
"v_mul_f32 v44, v228, v44\n"
"v_mul_f32 v45, v228, v45\n"
"v_mul_f32 v46, v228, v46\n"
"v_mul_f32 v47, v228, v47\n"
"v_mul_f32 v48, v228, v48\n"
"v_mul_f32 v49, v228, v49\n"
"v_mul_f32 v50, v228, v50\n"
"v_mul_f32 v51, v228, v51\n"
"v_mul_f32 v52, v228, v52\n"
"v_mul_f32 v53, v228, v53\n"
"v_mul_f32 v54, v228, v54\n"
"v_mul_f32 v55, v228, v55\n"
"v_mul_f32 v56, v228, v56\n"
"v_mul_f32 v57, v228, v57\n"
"v_mul_f32 v58, v228, v58\n"
"v_mul_f32 v59, v228, v59\n"
"v_mul_f32 v60, v228, v60\n"
"v_mul_f32 v61, v228, v61\n"
"v_mul_f32 v62, v228, v62\n"
"v_mul_f32 v63, v228, v63\n"
"v_mul_f32 v192, v228, v192\n"
"s_nop 3\n"
"s_branch KF_RD4\n"
"KF_RA5:\n"
"s_nop 3\n"
"v_max_f32 v230, 0, v226\n"
"v_exp_f32 v228, -v230\n"
"v_add_f32 v225, v225, v230\n"
"v_xor_b32 v231, 0x80000000, v230\n"
"v_xor_b32 v232, 0x80000000, v225\n"
"s_nop 15\n"
"v_mfma_f32_32x32x2_f32 v[96:111], v229, v231, v[96:111]\n"
"v_mfma_f32_32x32x2_f32 v[128:143], v229, v231, v[128:143]\n"
"v_mfma_f32_32x32x2_f32 v[176:191], v229, v232, 0\n"
"s_nop 15\n"
"s_nop 15\n"
"s_mov_b32 s60, 1\n"
"s_branch KF_RB5\n"
"KF_RC5:\n"
"s_nop 15\n"
"s_nop 7\n"
"v_mul_f32 v0, v228, v0\n"
"v_mul_f32 v1, v228, v1\n"
"v_mul_f32 v2, v228, v2\n"
"v_mul_f32 v3, v228, v3\n"
"v_mul_f32 v4, v228, v4\n"
"v_mul_f32 v5, v228, v5\n"
"v_mul_f32 v6, v228, v6\n"
"v_mul_f32 v7, v228, v7\n"
"v_mul_f32 v8, v228, v8\n"
"v_mul_f32 v9, v228, v9\n"
"v_mul_f32 v10, v228, v10\n"
"v_mul_f32 v11, v228, v11\n"
"v_mul_f32 v12, v228, v12\n"
"v_mul_f32 v13, v228, v13\n"
"v_mul_f32 v14, v228, v14\n"
"v_mul_f32 v15, v228, v15\n"
"v_mul_f32 v16, v228, v16\n"
"v_mul_f32 v17, v228, v17\n"
"v_mul_f32 v18, v228, v18\n"
"v_mul_f32 v19, v228, v19\n"
"v_mul_f32 v20, v228, v20\n"
"v_mul_f32 v21, v228, v21\n"
"v_mul_f32 v22, v228, v22\n"
"v_mul_f32 v23, v228, v23\n"
"v_mul_f32 v24, v228, v24\n"
"v_mul_f32 v25, v228, v25\n"
"v_mul_f32 v26, v228, v26\n"
"v_mul_f32 v27, v228, v27\n"
"v_mul_f32 v28, v228, v28\n"
"v_mul_f32 v29, v228, v29\n"
"v_mul_f32 v30, v228, v30\n"
"v_mul_f32 v31, v228, v31\n"
"v_mul_f32 v32, v228, v32\n"
"v_mul_f32 v33, v228, v33\n"
"v_mul_f32 v34, v228, v34\n"
"v_mul_f32 v35, v228, v35\n"
"v_mul_f32 v36, v228, v36\n"
"v_mul_f32 v37, v228, v37\n"
"v_mul_f32 v38, v228, v38\n"
"v_mul_f32 v39, v228, v39\n"
"v_mul_f32 v40, v228, v40\n"
"v_mul_f32 v41, v228, v41\n"
"v_mul_f32 v42, v228, v42\n"
"v_mul_f32 v43, v228, v43\n"
"v_mul_f32 v44, v228, v44\n"
"v_mul_f32 v45, v228, v45\n"
"v_mul_f32 v46, v228, v46\n"
"v_mul_f32 v47, v228, v47\n"
"v_mul_f32 v48, v228, v48\n"
"v_mul_f32 v49, v228, v49\n"
"v_mul_f32 v50, v228, v50\n"
"v_mul_f32 v51, v228, v51\n"
"v_mul_f32 v52, v228, v52\n"
"v_mul_f32 v53, v228, v53\n"
"v_mul_f32 v54, v228, v54\n"
"v_mul_f32 v55, v228, v55\n"
"v_mul_f32 v56, v228, v56\n"
"v_mul_f32 v57, v228, v57\n"
"v_mul_f32 v58, v228, v58\n"
"v_mul_f32 v59, v228, v59\n"
"v_mul_f32 v60, v228, v60\n"
"v_mul_f32 v61, v228, v61\n"
"v_mul_f32 v62, v228, v62\n"
"v_mul_f32 v63, v228, v63\n"
"v_mul_f32 v192, v228, v192\n"
"s_nop 3\n"
"s_branch KF_RD5\n"
"KF_RA6:\n"
"s_nop 3\n"
"v_max_f32 v230, 0, v226\n"
"v_exp_f32 v228, -v230\n"
"v_add_f32 v225, v225, v230\n"
"v_xor_b32 v231, 0x80000000, v230\n"
"v_xor_b32 v232, 0x80000000, v225\n"
"s_nop 15\n"
"v_mfma_f32_32x32x2_f32 v[96:111], v229, v231, v[96:111]\n"
"v_mfma_f32_32x32x2_f32 v[112:127], v229, v231, v[112:127]\n"
"v_mfma_f32_32x32x2_f32 v[176:191], v229, v232, 0\n"
"s_nop 15\n"
"s_nop 15\n"
"s_mov_b32 s60, 1\n"
"s_branch KF_RB6\n"
"KF_RC6:\n"
"s_nop 15\n"
"s_nop 7\n"
"v_mul_f32 v0, v228, v0\n"
"v_mul_f32 v1, v228, v1\n"
"v_mul_f32 v2, v228, v2\n"
"v_mul_f32 v3, v228, v3\n"
"v_mul_f32 v4, v228, v4\n"
"v_mul_f32 v5, v228, v5\n"
"v_mul_f32 v6, v228, v6\n"
"v_mul_f32 v7, v228, v7\n"
"v_mul_f32 v8, v228, v8\n"
"v_mul_f32 v9, v228, v9\n"
"v_mul_f32 v10, v228, v10\n"
"v_mul_f32 v11, v228, v11\n"
"v_mul_f32 v12, v228, v12\n"
"v_mul_f32 v13, v228, v13\n"
"v_mul_f32 v14, v228, v14\n"
"v_mul_f32 v15, v228, v15\n"
"v_mul_f32 v16, v228, v16\n"
"v_mul_f32 v17, v228, v17\n"
"v_mul_f32 v18, v228, v18\n"
"v_mul_f32 v19, v228, v19\n"
"v_mul_f32 v20, v228, v20\n"
"v_mul_f32 v21, v228, v21\n"
"v_mul_f32 v22, v228, v22\n"
"v_mul_f32 v23, v228, v23\n"
"v_mul_f32 v24, v228, v24\n"
"v_mul_f32 v25, v228, v25\n"
"v_mul_f32 v26, v228, v26\n"
"v_mul_f32 v27, v228, v27\n"
"v_mul_f32 v28, v228, v28\n"
"v_mul_f32 v29, v228, v29\n"
"v_mul_f32 v30, v228, v30\n"
"v_mul_f32 v31, v228, v31\n"
"v_mul_f32 v32, v228, v32\n"
"v_mul_f32 v33, v228, v33\n"
"v_mul_f32 v34, v228, v34\n"
"v_mul_f32 v35, v228, v35\n"
"v_mul_f32 v36, v228, v36\n"
"v_mul_f32 v37, v228, v37\n"
"v_mul_f32 v38, v228, v38\n"
"v_mul_f32 v39, v228, v39\n"
"v_mul_f32 v40, v228, v40\n"
"v_mul_f32 v41, v228, v41\n"
"v_mul_f32 v42, v228, v42\n"
"v_mul_f32 v43, v228, v43\n"
"v_mul_f32 v44, v228, v44\n"
"v_mul_f32 v45, v228, v45\n"
"v_mul_f32 v46, v228, v46\n"
"v_mul_f32 v47, v228, v47\n"
"v_mul_f32 v48, v228, v48\n"
"v_mul_f32 v49, v228, v49\n"
"v_mul_f32 v50, v228, v50\n"
"v_mul_f32 v51, v228, v51\n"
"v_mul_f32 v52, v228, v52\n"
"v_mul_f32 v53, v228, v53\n"
"v_mul_f32 v54, v228, v54\n"
"v_mul_f32 v55, v228, v55\n"
"v_mul_f32 v56, v228, v56\n"
"v_mul_f32 v57, v228, v57\n"
"v_mul_f32 v58, v228, v58\n"
"v_mul_f32 v59, v228, v59\n"
"v_mul_f32 v60, v228, v60\n"
"v_mul_f32 v61, v228, v61\n"
"v_mul_f32 v62, v228, v62\n"
"v_mul_f32 v63, v228, v63\n"
"v_mul_f32 v192, v228, v192\n"
"s_nop 3\n"
"s_branch KF_RD6\n"
"KF_RA7:\n"
"s_nop 3\n"
"v_max_f32 v230, 0, v226\n"
"v_exp_f32 v228, -v230\n"
"v_add_f32 v225, v225, v230\n"
"v_xor_b32 v231, 0x80000000, v230\n"
"v_xor_b32 v232, 0x80000000, v225\n"
"s_nop 15\n"
"v_mfma_f32_32x32x2_f32 v[96:111], v229, v231, v[96:111]\n"
"v_mfma_f32_32x32x2_f32 v[128:143], v229, v231, v[128:143]\n"
"v_mfma_f32_32x32x2_f32 v[176:191], v229, v232, 0\n"
"s_nop 15\n"
"s_nop 15\n"
"s_mov_b32 s60, 1\n"
"s_branch KF_RB7\n"
"KF_RC7:\n"
"s_nop 15\n"
"s_nop 7\n"
"v_mul_f32 v0, v228, v0\n"
"v_mul_f32 v1, v228, v1\n"
"v_mul_f32 v2, v228, v2\n"
"v_mul_f32 v3, v228, v3\n"
"v_mul_f32 v4, v228, v4\n"
"v_mul_f32 v5, v228, v5\n"
"v_mul_f32 v6, v228, v6\n"
"v_mul_f32 v7, v228, v7\n"
"v_mul_f32 v8, v228, v8\n"
"v_mul_f32 v9, v228, v9\n"
"v_mul_f32 v10, v228, v10\n"
"v_mul_f32 v11, v228, v11\n"
"v_mul_f32 v12, v228, v12\n"
"v_mul_f32 v13, v228, v13\n"
"v_mul_f32 v14, v228, v14\n"
"v_mul_f32 v15, v228, v15\n"
"v_mul_f32 v16, v228, v16\n"
"v_mul_f32 v17, v228, v17\n"
"v_mul_f32 v18, v228, v18\n"
"v_mul_f32 v19, v228, v19\n"
"v_mul_f32 v20, v228, v20\n"
"v_mul_f32 v21, v228, v21\n"
"v_mul_f32 v22, v228, v22\n"
"v_mul_f32 v23, v228, v23\n"
"v_mul_f32 v24, v228, v24\n"
"v_mul_f32 v25, v228, v25\n"
"v_mul_f32 v26, v228, v26\n"
"v_mul_f32 v27, v228, v27\n"
"v_mul_f32 v28, v228, v28\n"
"v_mul_f32 v29, v228, v29\n"
"v_mul_f32 v30, v228, v30\n"
"v_mul_f32 v31, v228, v31\n"
"v_mul_f32 v32, v228, v32\n"
"v_mul_f32 v33, v228, v33\n"
"v_mul_f32 v34, v228, v34\n"
"v_mul_f32 v35, v228, v35\n"
"v_mul_f32 v36, v228, v36\n"
"v_mul_f32 v37, v228, v37\n"
"v_mul_f32 v38, v228, v38\n"
"v_mul_f32 v39, v228, v39\n"
"v_mul_f32 v40, v228, v40\n"
"v_mul_f32 v41, v228, v41\n"
"v_mul_f32 v42, v228, v42\n"
"v_mul_f32 v43, v228, v43\n"
"v_mul_f32 v44, v228, v44\n"
"v_mul_f32 v45, v228, v45\n"
"v_mul_f32 v46, v228, v46\n"
"v_mul_f32 v47, v228, v47\n"
"v_mul_f32 v48, v228, v48\n"
"v_mul_f32 v49, v228, v49\n"
"v_mul_f32 v50, v228, v50\n"
"v_mul_f32 v51, v228, v51\n"
"v_mul_f32 v52, v228, v52\n"
"v_mul_f32 v53, v228, v53\n"
"v_mul_f32 v54, v228, v54\n"
"v_mul_f32 v55, v228, v55\n"
"v_mul_f32 v56, v228, v56\n"
"v_mul_f32 v57, v228, v57\n"
"v_mul_f32 v58, v228, v58\n"
"v_mul_f32 v59, v228, v59\n"
"v_mul_f32 v60, v228, v60\n"
"v_mul_f32 v61, v228, v61\n"
"v_mul_f32 v62, v228, v62\n"
"v_mul_f32 v63, v228, v63\n"
"v_mul_f32 v192, v228, v192\n"
"s_nop 3\n"
"s_branch KF_RD7\n"
"KF_END:\n"
  : "+{v0}"(t) : "{s[0:1]}"(__builtin_amdgcn_kernarg_segment_ptr()), "{s2}"(blockIdx.x)
  : "memory", "vcc", "scc", "v1", "v2", "v3", "v4", "v5", "v6", "v7", "v8", "v9", "v10", "v11", "v12", "v13", "v14", "v15", "v16", "v17", "v18", "v19", "v20", "v21", "v22", "v23", "v24", "v25", "v26", "v27", "v28", "v29", "v30", "v31", "v32", "v33", "v34", "v35", "v36", "v37", "v38", "v39", "v40", "v41", "v42", "v43", "v44", "v45", "v46", "v47", "v48", "v49", "v50", "v51", "v52", "v53", "v54", "v55", "v56", "v57", "v58", "v59", "v60", "v61", "v62", "v63", "v64", "v65", "v66", "v67", "v68", "v69", "v70", "v71", "v72", "v73", "v74", "v75", "v76", "v77", "v78", "v79", "v80", "v81", "v82", "v83", "v84", "v85", "v86", "v87", "v88", "v89", "v90", "v91", "v92", "v93", "v94", "v95", "v96", "v97", "v98", "v99", "v100", "v101", "v102", "v103", "v104", "v105", "v106", "v107", "v108", "v109", "v110", "v111", "v112", "v113", "v114", "v115", "v116", "v117", "v118", "v119", "v120", "v121", "v122", "v123", "v124", "v125", "v126", "v127", "v128", "v129", "v130", "v131", "v132", "v133", "v134", "v135", "v136", "v137", "v138", "v139", "v140", "v141", "v142", "v143", "v144", "v145", "v146", "v147", "v148", "v149", "v150", "v151", "v152", "v153", "v154", "v155", "v156", "v157", "v158", "v159", "v160", "v161", "v162", "v163", "v164", "v165", "v166", "v167", "v168", "v169", "v170", "v171", "v172", "v173", "v174", "v175", "v176", "v177", "v178", "v179", "v180", "v181", "v182", "v183", "v184", "v185", "v186", "v187", "v188", "v189", "v190", "v191", "v192", "v193", "v194", "v195", "v196", "v197", "v198", "v199", "v200", "v201", "v202", "v203", "v204", "v205", "v206", "v207", "v208", "v209", "v210", "v211", "v212", "v213", "v214", "v215", "v216", "v217", "v218", "v219", "v220", "v221", "v222", "v223", "v224", "v225", "v226", "v227", "v228", "v229", "v230", "v231", "v232", "v233", "v234", "v235", "v236", "v237", "v238", "v239", "v240", "v241", "v242", "v243", "v244", "v245", "v246", "v247", "v248", "v249", "v250", "v251", "v252", "v253", "v254", "v255", "s3", "s4", "s5", "s6", "s7", "s8", "s9", "s10", "s11", "s12", "s13", "s14", "s15", "s16", "s17", "s18", "s19", "s20", "s21", "s22", "s23", "s24", "s25", "s26", "s27", "s28", "s29", "s30", "s31", "s32", "s33", "s34", "s35", "s36", "s37", "s38", "s39", "s40", "s41", "s42", "s43", "s44", "s45", "s46", "s47", "s48", "s49", "s50", "s51", "s52", "s53", "s54", "s55", "s56", "s57", "s58", "s59", "s60", "s61", "s62", "s63", "s64", "s65", "s66", "s67", "s68", "s69", "s70", "s71", "s72", "s73", "s74", "s75", "s76", "s77", "s78", "s79", "s80", "s81", "s82", "s83", "s84", "s85", "s86", "s87", "s88", "s89", "s90", "s91", "s92", "s93", "s94", "s95", "s96", "s97", "s98", "s99");
}

extern "C" void kernel_launch(void* const* d_in, const int* in_sizes, int n_in, void* d_out, int out_size, void* d_ws, size_t ws_size, hipStream_t stream) {
  (void)in_sizes; (void)n_in; (void)out_size; (void)ws_size;
  (void)hipFuncSetAttribute((const void*)fused_mha_k, hipFuncAttributeMaxDynamicSharedMemorySize, 163840);
  hipLaunchKernelGGL(fused_mha_k, dim3(256), dim3(512), 163840, stream, (const char*)d_in[0], (const char*)d_in[1], (const char*)d_in[2], (const char*)d_in[3],
                     (const char*)d_in[5], (const char*)d_in[6], (const char*)d_in[7], (const char*)d_in[8], (char*)d_ws,
                     (const char*)d_in[4], (const char*)d_in[9], (char*)d_out);
}
```
